```python
import torch

def _softmax(x, axis=-1):
    return torch.softmax(x, dim=axis)
def _gelu(x):
    return torch.nn.functional.gelu(x, approximate="tanh")
def _silu(x):
    return torch.nn.functional.silu(x)
def _sigmoid(x):
    return torch.sigmoid(x)
def _layernorm(x, axis=-1, eps=1e-5):
    m = x.mean(dim=axis, keepdim=True)
    v = x.var(dim=axis, keepdim=True, unbiased=False)
    return (x - m) / torch.sqrt(v + eps)
def _scatter_add(tgt, idx, src, axis=0):
    return tgt.clone().index_add_(axis, idx, src)
def _segment_sum(src, seg, n):
    out = torch.zeros((n,) + src.shape[1:], dtype=src.dtype)
    return out.index_add_(0, seg, src)
def _pad(x, pw):
    flat = tuple(v for pair in reversed(pw) for v in pair)
    return torch.nn.functional.pad(x, flat)

kernel_name = 'gated_mix__linear_kernel_hedgehog__sliding_block__prompt'

def setup_inputs():

    _w = (lambda a: (lambda t: t.to(torch.bfloat16) if t.is_floating_point() else t)(torch.as_tensor(a)))
    def _A_setup():
        B, S, D, H = 1, 2048, 576, 32
        Dh = D // H
        return {
            "x": _w((lambda *s: torch.randn(*s, dtype=torch.bfloat16) * 0.05)(B, S, D)),
            "wqkv": _w((lambda *s: torch.randn(*s, dtype=torch.bfloat16) * 1.6)(D, 3 * D)),
            "w_phi": _w((lambda *s: torch.randn(*s, dtype=torch.bfloat16) * 1.6)(H, Dh, Dh) * (Dh ** -0.5)),
            "wo": _w((lambda *s: torch.randn(*s, dtype=torch.bfloat16) * 0.1)(D, D)),
        }
    _A_in = _A_setup()

    def _B_setup():
        B, S, D = 1, 2048, 576
        return {
            "x": _w((lambda *s: torch.randn(*s, dtype=torch.bfloat16) * 0.05)(B, S, D)),
            "wqkv": _w((lambda *s: torch.randn(*s, dtype=torch.bfloat16) * 0.8)(D, 3 * D)),
            "wo": _w((lambda *s: torch.randn(*s, dtype=torch.bfloat16) * 0.05)(D, D)),
        }
    _B_in = _B_setup()

    return {
        "A_x": _A_in["x"],
        "A_wqkv": _A_in["wqkv"],
        "A_w_phi": _A_in["w_phi"],
        "A_wo": _A_in["wo"],
        "B_x": _B_in["x"],
        "B_wqkv": _B_in["wqkv"],
        "B_wo": _B_in["wo"],
        "gate_w": _w((lambda *s: torch.randn(*s, dtype=torch.bfloat16) * 0.05)(576) * 16.0),
    }

def reference(A_x, A_wqkv, A_w_phi, A_wo, B_x, B_wqkv, B_wo, gate_w):
    def _A_ref(x, wqkv, w_phi, wo):
        B, S, D = x.shape
        H = 32; Dh = D // H
        qkv = (lambda x, *d: x.permute(*d))((x @ wqkv).reshape(B, S, 3, H, Dh), 2, 0, 3, 1, 4)
        q, k, v = qkv[0], qkv[1], qkv[2]
        pq = torch.einsum("bhsd,hdf->bhsf", q, w_phi)
        pk = torch.einsum("bhsd,hdf->bhsf", k, w_phi)
        phi_q = torch.concatenate([_softmax(pq, axis=-1), _softmax(-pq, axis=-1)], axis=-1)
        phi_k = torch.concatenate([_softmax(pk, axis=-1), _softmax(-pk, axis=-1)], axis=-1)
        kv = torch.einsum("bhsf,bhsd->bhfd", phi_k, v)
        z  = torch.einsum("bhsf->bhf", phi_k)
        num = torch.einsum("bhqf,bhfd->bhqd", phi_q, kv)
        den = torch.einsum("bhqf,bhf->bhq", phi_q, z)[..., None] + 1e-6
        o = num / den
        return (lambda x, *d: x.permute(*d))(o, 0, 2, 1, 3).reshape(B, S, D) @ wo

    def _B_ref(x, wqkv, wo):
        B, S, D = x.shape
        H = 32; Dh = D // H; W = 256
        qkv = (lambda x, *d: x.permute(*d))((x @ wqkv).reshape(B, S, 3, H, Dh), 2, 0, 3, 1, 4)
        q, k, v = qkv[0], qkv[1], qkv[2]
        scores = torch.einsum("bhqd,bhkd->bhqk", q, k) * (Dh ** -0.5)
        idx = torch.arange(S)
        mask = (idx[:, None] - idx[None, :] >= 0) & (idx[:, None] - idx[None, :] < W)
        scores = torch.where(mask[None, None], scores, -1e9)
        p = _softmax(scores, axis=-1)
        o = torch.einsum("bhqk,bhkd->bhqd", p, v)
        return (lambda x, *d: x.permute(*d))(o, 0, 2, 1, 3).reshape(B, S, D) @ wo

    x = A_x
    g = _sigmoid((x * gate_w).sum(-1, keepdims=True))
    a = _A_ref(x, A_wqkv, A_w_phi, A_wo)
    b = _B_ref(x, B_wqkv, B_wo)
    return g * a + (1 - g) * b
```

```cpp
#include <hip/hip_runtime.h>
#include <hip/hip_bf16.h>
#include <stdint.h>
typedef unsigned short u16;
typedef short bf16x8 __attribute__((ext_vector_type(8)));
typedef float f32x4 __attribute__((ext_vector_type(4)));
constexpr int S = 2048, D = 576, H = 32, DH = 18, W = 256, NQ = 3456;
__device__ __forceinline__ float b2f(u16 v) { return __uint_as_float((unsigned)v << 16); }
__device__ __forceinline__ u16 f2b(float f) { unsigned u = __float_as_uint(f); u += 0x7fffu + ((u >> 16) & 1u); return (u16)(u >> 16); }
__device__ __forceinline__ float rb(float f) { return b2f(f2b(f)); }

__global__ __launch_bounds__(256) void gemm_k(const u16* A, int lda, const u16* B, int ldb, u16* C, int ldc, int K) {
    __shared__ u16 As[64][40]; __shared__ u16 Bs[64][40];
    const int tid = threadIdx.x, lane = tid & 63, w = tid >> 6, m0 = blockIdx.y * 64, n0 = blockIdx.x * 64;
    f32x4 acc[4]; for (int i = 0; i < 4; ++i) acc[i] = f32x4{0, 0, 0, 0};
    for (int k0 = 0; k0 < K; k0 += 32) {
        { int r = tid >> 2, c = (tid & 3) * 8; *(uint4*)&As[r][c] = *(const uint4*)(A + (size_t)(m0 + r) * lda + k0 + c); }
        { int k = tid >> 3, c = (tid & 7) * 8; uint4 v = *(const uint4*)(B + (size_t)(k0 + k) * ldb + n0 + c); const u16* p = (const u16*)&v; for (int j = 0; j < 8; ++j) Bs[c + j][k] = p[j]; }
        __syncthreads();
        bf16x8 a = *(const bf16x8*)&As[w * 16 + (lane & 15)][(lane >> 4) * 8];
        for (int n = 0; n < 4; ++n) { bf16x8 b = *(const bf16x8*)&Bs[n * 16 + (lane & 15)][(lane >> 4) * 8]; acc[n] = __builtin_amdgcn_mfma_f32_16x16x32_bf16(a, b, acc[n], 0, 0, 0); }
        __syncthreads();
    }
    for (int n = 0; n < 4; ++n) for (int j = 0; j < 4; ++j) C[(size_t)(m0 + w * 16 + (lane >> 4) * 4 + j) * ldc + n0 + n * 16 + (lane & 15)] = f2b(acc[n][j]);
}
__global__ __launch_bounds__(256) void feat_k(const u16* qkv, const u16* wphi, u16* phi  ) {
    int id = blockIdx.x * blockDim.x + threadIdx.x; int t = id % S, r = id / S, h = r % H, which = r / H;
    float q[DH]; for (int d = 0; d < DH; ++d) q[d] = b2f(qkv[(size_t)t * NQ + which * D + h * DH + d]);
    float p[DH]; float mx = -1e30f, mn = 1e30f;
    for (int f = 0; f < DH; ++f) { float s = 0; for (int d = 0; d < DH; ++d) s += q[d] * b2f(wphi[(h * DH + d) * DH + f]); p[f] = rb(s); mx = fmaxf(mx, p[f]); mn = fminf(mn, p[f]); }
    float s1 = 0, s2 = 0; float e1[DH], e2[DH];
    for (int f = 0; f < DH; ++f) { e1[f] = __expf(p[f] - mx); e2[f] = __expf(mn - p[f]); s1 += e1[f]; s2 += e2[f]; }
    u16* o = phi + (((size_t)which * S + t) * H + h) * 36;
    for (int f = 0; f < DH; ++f) { o[f] = f2b(e1[f] / s1); o[DH + f] = f2b(e2[f] / s2); }
}
__global__ void kv_k(const u16* qkv, const u16* phi, float* kv  ) {
    int h = blockIdx.x, f = threadIdx.x / 19, d = threadIdx.x % 19; float s = 0;
    for (int t = 0; t < S; ++t) { float p = b2f(phi[((size_t)S + t) * H * 36 + h * 36 + f]); float v = d < 18 ? b2f(qkv[(size_t)t * NQ + 2 * D + h * DH + d]) : 1.f; s += p * v; }
    kv[(h * 36 + f) * 19 + d] = rb(s);
}
__global__ __launch_bounds__(256) void att_k(const u16* qkv, const u16* phi, const float* kv, const u16* x, const u16* gw, u16* O) {
    int id = blockIdx.x * blockDim.x + threadIdx.x; int h = id % H, t = id / H;
    float gs = 0; for (int d = 0; d < D; ++d) gs += rb(b2f(x[(size_t)t * D + d]) * b2f(gw[d])); gs = rb(gs);
    float g = rb(1.f / (1.f + __expf(-gs)));
    { float num[DH]; for (int d = 0; d < DH; ++d) num[d] = 0; float den = 0;
      for (int f = 0; f < 36; ++f) { float p = b2f(phi[(size_t)t * H * 36 + h * 36 + f]); for (int d = 0; d < DH; ++d) num[d] += p * kv[(h * 36 + f) * 19 + d]; den += p * kv[(h * 36 + f) * 19 + 18]; }
      den = rb(rb(den) + 1e-6f);
      for (int d = 0; d < DH; ++d) O[(size_t)t * 1152 + h * DH + d] = f2b(g * rb(rb(num[d]) / den)); }
    { const u16* q = qkv + (size_t)t * NQ + 1728 + h * DH; float qq[DH]; for (int d = 0; d < DH; ++d) qq[d] = b2f(q[d]);
      int j0 = t - W + 1; if (j0 < 0) j0 = 0; float m = -1e30f, l = 0, o[DH]; for (int d = 0; d < DH; ++d) o[d] = 0;
      for (int j = j0; j <= t; ++j) { const u16* k = qkv + (size_t)j * NQ + 1728 + D + h * DH; float s = 0; for (int d = 0; d < DH; ++d) s += qq[d] * b2f(k[d]); s = rb(rb(s) * 0.23570226f);
          float mn = fmaxf(m, s), a = __expf(m - mn), p = __expf(s - mn); l = l * a + p; const u16* v = k + D; for (int d = 0; d < DH; ++d) o[d] = o[d] * a + p * b2f(v[d]); m = mn; }
      for (int d = 0; d < DH; ++d) O[(size_t)t * 1152 + D + h * DH + d] = f2b((1.f - g) * o[d] / l); }
}
__global__ void cat_k(const u16* a, const u16* b, u16* c, int n) { int i = blockIdx.x * blockDim.x + threadIdx.x; if (i < n) { c[i] = a[i]; c[n + i] = b[i]; } }
#define PROBE 0
typedef unsigned long long u64;
__device__ __forceinline__ void glds16(const void* g, unsigned lds_base) {
    unsigned sv; asm volatile("s_mov_b32 %0, m0\n\ts_mov_b32 m0, %2\n\ts_nop 0\n\tglobal_load_lds_dwordx4 %1, off\n\ts_mov_b32 m0, %0" : "=&s"(sv) : "v"(g), "s"(lds_base) : "memory"); }
template<int MODE> __global__ __launch_bounds__(512, 2) void probe_k(const char* x) {
    extern __shared__ __attribute__((aligned(16))) char lds[];
    if (MODE == 1) return;
    const int tid = threadIdx.x, lane = tid & 63; const int wid = __builtin_amdgcn_readfirstlane(tid >> 6); const int rt = blockIdx.x >> 4;
    const unsigned lds0 = (unsigned)(uintptr_t)lds;
    if (MODE == 2) {
        const char* xs[2];
        for (int i = 0; i < 2; ++i) { int r = wid * 16 + i * 8 + (lane >> 3); int c = (lane & 7) ^ ((r >> 1) & 7); xs[i] = x + (size_t)(rt * 128 + r) * 1152 + c * 16; }
        auto issue = [&](int ks) { for (int i = 0; i < 2; ++i) glds16(xs[i] + ks * 128, __builtin_amdgcn_readfirstlane(lds0 + (ks % 3) * 16384 + (wid * 2 + i) * 1024)); };
        issue(0); issue(1);
        for (int ks = 0; ks < 9; ++ks) { if (ks + 2 < 9) { issue(ks + 2); asm volatile("s_waitcnt vmcnt(4)" ::: "memory"); } else if (ks + 1 < 9) asm volatile("s_waitcnt vmcnt(2)" ::: "memory"); else asm volatile("s_waitcnt vmcnt(0)" ::: "memory"); __builtin_amdgcn_s_barrier(); }
    }
    if (MODE == 3) {
        for (int i = 0; i < 18; ++i) glds16(x + (size_t)rt * 147456 + (wid * 18 + i) * 1024 + lane * 16, __builtin_amdgcn_readfirstlane(lds0 + (wid * 18 + i) * 1024));
        asm volatile("s_waitcnt vmcnt(0)" ::: "memory"); __builtin_amdgcn_s_barrier();
    }
}
extern "C" void kernel_launch(void* const* d_in, const int* in_sizes, int n_in, void* d_out, int out_size, void* d_ws, size_t ws_size, hipStream_t stream) {
    const u16 *x = (const u16*)d_in[0], *wA = (const u16*)d_in[1], *wphi = (const u16*)d_in[2], *woA = (const u16*)d_in[3], *wB = (const u16*)d_in[5], *woB = (const u16*)d_in[6], *gw = (const u16*)d_in[7];
    char* ws = (char*)d_ws; u16* qkv = (u16*)ws; u16* phi = (u16*)(ws + (16 << 20)); float* kv = (float*)(ws + (32 << 20)); u16* O = (u16*)(ws + (33 << 20)); u16* wo = (u16*)(ws + (40 << 20));
    gemm_k<<<dim3(27, 32), 256, 0, stream>>>(x, D, wA, 1728, qkv, NQ, D);
    gemm_k<<<dim3(27, 32), 256, 0, stream>>>(x, D, wB, 1728, qkv + 1728, NQ, D);
    feat_k<<<S * H * 2 / 256, 256, 0, stream>>>(qkv, wphi, phi);
    kv_k<<<H, 36 * 19, 0, stream>>>(qkv, phi, kv);
    att_k<<<S * H / 256, 256, 0, stream>>>(qkv, phi, kv, x, gw, O);
    cat_k<<<(D * D + 255) / 256, 256, 0, stream>>>(woA, woB, wo, D * D);
    gemm_k<<<dim3(9, 32), 256, 0, stream>>>(O, 1152, wo, D, (u16*)d_out, D, 1152);
}
```

```cpp
#include <hip/hip_runtime.h>
#include <hip/hip_bf16.h>
#include <stdint.h>

typedef unsigned short u16;
typedef short bf16x8 __attribute__((ext_vector_type(8)));
typedef short s16x4 __attribute__((ext_vector_type(4)));
typedef float f32x4 __attribute__((ext_vector_type(4)));
typedef float f32x16 __attribute__((ext_vector_type(16)));
typedef unsigned u32x4 __attribute__((ext_vector_type(4)));
typedef unsigned u32x2 __attribute__((ext_vector_type(2)));
#define LAS __attribute__((address_space(3)))
#define GAS __attribute__((address_space(1)))
typedef LAS char* lptr;
typedef GAS unsigned gu32;

constexpr int DM = 576;
constexpr size_t WS_FLAG = 0;
constexpr size_t WS_WT   = 131072;
constexpr size_t WS_PHI  = WS_WT + 16 * 9 * 28672;
constexpr size_t WS_WOT  = WS_PHI + 16 * 12288;
constexpr size_t WS_KV   = WS_WOT + 8 * 165888;
constexpr size_t WS_PART = WS_KV + 256 * 24576;
constexpr size_t WS_O    = WS_PART + 256 * 6144;
constexpr size_t WS_DBG  = WS_O + 16 * 8 * 36864;

typedef __bf16 bf16x2_t __attribute__((ext_vector_type(2)));
typedef float f32x2 __attribute__((ext_vector_type(2)));
__device__ __forceinline__ unsigned cvtpk(float lo, float hi) { const f32x2 v = {lo, hi}; return __builtin_bit_cast(unsigned, __builtin_convertvector(v, bf16x2_t)); }
__device__ __forceinline__ float bflo(unsigned v) { return __uint_as_float(v << 16); }
__device__ __forceinline__ float bfhi(unsigned v) { return __uint_as_float(v & 0xffff0000u); }

__device__ __forceinline__ int ostage_row(int hp, int p, int& br) {
    int head, d;
    if (p < 16) { br = 1; head = 0; d = p; } else if (p < 32) { br = 1; head = 1; d = p - 16; }
    else if (p < 34) { br = 1; head = 0; d = 16 + p - 32; } else if (p < 36) { br = 1; head = 1; d = 16 + p - 34; }
    else if (p < 54) { br = 0; head = 0; d = p - 36; } else { br = 0; head = 1; d = p - 54; }
    return (2 * hp + head) * 18 + d;
}
__global__ __launch_bounds__(256) void prep_kernel(const u16* __restrict__ wA, const u16* __restrict__ wB, const u16* __restrict__ wphi, const u16* __restrict__ woA,
                                                   const u16* __restrict__ woB, const u16* __restrict__ gw, char* __restrict__ ws) {
    const int id = blockIdx.x * 256 + threadIdx.x;
    constexpr int NA = 16 * 9 * 28 * 64, NB = 16 * 12 * 64, NC = 8 * 36 * 288, ND = 768 * 8;
    u16 v[8];
    if (id < NA) {
        const int lane = id & 63; int f = id >> 6; const int ks = f & 1; f >>= 1; const int t = f % 7; f /= 7; const int h = f & 1; f >>= 1; const int kb = f % 9, hp = f / 9;
        const int r = lane & 15, g = lane >> 4, k0 = kb * 64 + ks * 32 + 8 * g;
        int br = 0, m = 0, d = 0, kind = 0;
        if (t == 0) { br = 0; m = 2; d = r; } else if (t == 1) { br = 1; m = 0; d = r; } else if (t == 2) { br = 1; m = 1; d = r; } else if (t == 3) { br = 1; m = 2; d = r; }
        else if (t == 4) { br = 0; m = 0; d = r; } else if (t == 5) { br = 0; if (r < 2) { m = 0; d = 16 + r; } else { m = 1; d = r - 2; } }
        else { if (r < 4) { br = 0; m = 1; d = 14 + r; } else if (r < 6) { br = 0; m = 2; d = 12 + r; } else if (r < 8) { br = 1; m = 0; d = 10 + r; }
               else if (r < 10) { br = 1; m = 1; d = 8 + r; } else if (r < 12) { br = 1; m = 2; d = 6 + r; } else kind = (r == 12) ? 1 : 2; }
        const u16* src = (br ? wB : wA) + m * DM + (2 * hp + h) * 18 + d;
        for (int j = 0; j < 8; ++j) v[j] = kind == 0 ? src[(size_t)(k0 + j) * 1728] : kind == 1 ? gw[k0 + j] : (u16)0;
        *(uint4*)(ws + WS_WT + (size_t)id * 16) = *(uint4*)v;
    } else if (id < NA + NB) {
        const int e0 = id - NA, lane = e0 & 63; int f = e0 >> 6; const int i = f % 3; f /= 3; const int X = f & 1; f >>= 1; const int h = f & 1, hp = f >> 1;
        const int r = lane & 15, g = lane >> 4, e = (r >> 2) & 1, sg = 4 * i + (r & 3), ff = 9 * e + sg;
        for (int j = 0; j < 8; ++j) { int d = -1; const int rho = 4 * g + (j & 3);
            if (X == 0) { if (j < 4) d = rho; else if (rho < 2) d = 16 + rho; } else { if (j < 4) { if (rho >= 2) d = rho - 2; } else if (rho < 4) d = 14 + rho; }
            v[j] = (d >= 0 && sg < 9) ? wphi[((2 * hp + h) * 18 + d) * 18 + ff] : (u16)0; }
        *(uint4*)(ws + WS_PHI + (size_t)e0 * 16) = *(uint4*)v;
    } else if (id < NA + NB + NC) {
        const int e0 = id - NA - NB; const int q = e0 % 288; int f = e0 / 288; const int s = f % 18; f /= 18; const int kh = f & 1, cg = f >> 1;
        int t, r, g; if (q < 256) { t = q >> 6; r = q & 15; g = (q >> 4) & 3; } else { t = 4; r = q & 7; g = (q - 256) >> 3; }
        const int C = 72 * kh + 4 * s + g, hp = C / 9, c = C % 9, col = cg * 72 + 16 * t + r;
        for (int j = 0; j < 8; ++j) { int br; const int row = ostage_row(hp, 8 * c + j, br); v[j] = (br ? woB : woA)[row * DM + col]; }
        *(uint4*)(ws + WS_WOT + (size_t)e0 * 16) = *(uint4*)v;
    } else if (id < NA + NB + NC + ND) {
        const int e0 = id - NA - NB - NC; *(uint4*)(ws + WS_FLAG + (size_t)e0 * 16) = make_uint4(0, 0, 0, 0);
    }
}

constexpr int STAGE = 16384 + 28672;
constexpr int L_K = 0;
constexpr int L_Q = 73728;
constexpr int L_VA = 86016;
constexpr int L_PK = 98304;
constexpr int L_GATE = 118784;
constexpr int L_KVT = 119296;
constexpr int L_OST = 123392;
constexpr int LDS_BYTES = 160 * 1024;
constexpr int G3_SLOT = 9216, G3_NSLOT = 15;
constexpr unsigned SPIN_CAP = 1u << 18;

__device__ __forceinline__ void glds16(unsigned voff, const void* sbase, unsigned lds_addr) {
    unsigned sv;
    asm volatile("s_mov_b32 %0, m0\n\ts_mov_b32 m0, %3\n\ts_nop 0\n\tglobal_load_lds_dwordx4 %1, %2\n\ts_mov_b32 m0, %0" : "=&s"(sv) : "v"(voff), "s"(sbase), "s"(lds_addr) : "memory");
}
#define WAITV(n) asm volatile("s_waitcnt vmcnt(" #n ")" ::: "memory")
#define WAITL() asm volatile("s_waitcnt lgkmcnt(0)" ::: "memory")
#define BAR() asm volatile("s_waitcnt lgkmcnt(0)\n\ts_barrier" ::: "memory")
#define MFMA16(a, b, c) __builtin_amdgcn_mfma_f32_16x16x32_bf16(a, b, c, 0, 0, 0)
#define MFMA32(a, b, c) __builtin_amdgcn_mfma_f32_32x32x16_bf16(a, b, c, 0, 0, 0)
__device__ __forceinline__ bf16x8 ld128(lptr p) { return *(const LAS bf16x8*)p; }
__device__ __forceinline__ s16x4 trrd(lptr p) { typedef short v4i16_t __attribute__((ext_vector_type(4))); return __builtin_bit_cast(s16x4, __builtin_amdgcn_ds_read_tr16_b64_v4i16((LAS v4i16_t*)p)); }
__device__ __forceinline__ bf16x8 cat8(s16x4 a, s16x4 b) { return __builtin_shufflevector(a, b, 0, 1, 2, 3, 4, 5, 6, 7); }
__device__ __forceinline__ bf16x8 mk8(unsigned a, unsigned b, unsigned c, unsigned d) { u32x4 w = {a, b, c, d}; return __builtin_bit_cast(bf16x8, w); }
__device__ __forceinline__ float x16max(float x) { auto s = __builtin_amdgcn_permlane16_swap(__float_as_uint(x), __float_as_uint(x), false, false); return fmaxf(__uint_as_float(s[0]), __uint_as_float(s[1])); }
__device__ __forceinline__ float x16sum(float x) { auto s = __builtin_amdgcn_permlane16_swap(__float_as_uint(x), __float_as_uint(x), false, false); return __uint_as_float(s[0]) + __uint_as_float(s[1]); }
__device__ __forceinline__ float x32max(float x) { auto s = __builtin_amdgcn_permlane32_swap(__float_as_uint(x), __float_as_uint(x), false, false); return fmaxf(__uint_as_float(s[0]), __uint_as_float(s[1])); }
__device__ __forceinline__ void st_sc1(const __amdgpu_buffer_rsrc_t rs, unsigned off, u32x4 v) { __builtin_amdgcn_raw_buffer_store_b128(v, rs, off, 0, 16); }
__device__ __forceinline__ u32x4 ld_sc1(const __amdgpu_buffer_rsrc_t rs, unsigned off) { return __builtin_amdgcn_raw_buffer_load_b128(rs, off, 0, 16); }
__device__ __forceinline__ void wait_flags(gu32* base, int n, unsigned epoch, int lane) {
    gu32* p = base + (lane < n ? lane : 0) * 32;
    for (unsigned spins = 0; spins < SPIN_CAP; ++spins) {
        const unsigned v = __hip_atomic_load(p, __ATOMIC_RELAXED, __HIP_MEMORY_SCOPE_AGENT);
        if (__all(v == epoch)) break;
        __builtin_amdgcn_s_sleep(1);
    }
}
__device__ __forceinline__ void publish(gu32* flag, unsigned epoch, int tid) {
    asm volatile("s_waitcnt vmcnt(0) lgkmcnt(0)\n\ts_barrier" ::: "memory");
    if (tid == 0) __hip_atomic_store(flag, epoch, __ATOMIC_RELAXED, __HIP_MEMORY_SCOPE_AGENT);
}

constexpr float LOG2E = 1.4426950408889634f;
constexpr float QSCALE = 0.23570226039551584f * LOG2E;
constexpr float THR = 8.f;

__device__ __forceinline__ void feature_map(const float (&p)[9], unsigned (&o)[9]) {
    float mx = fmaxf(fmaxf(p[0], p[1]), p[2]), mn = fminf(fminf(p[0], p[1]), p[2]);
#pragma unroll
    for (int i = 3; i < 9; i += 2) { mx = fmaxf(fmaxf(mx, p[i]), p[i + 1]); mn = fminf(fminf(mn, p[i]), p[i + 1]); }
    mx = x16max(mx); mn = -x16max(-mn);
    const float mxl = -mx * LOG2E, mnl = mn * LOG2E;
    float a[9], b[9], sa = 0.f, sb = 0.f;
#pragma unroll
    for (int i = 0; i < 9; ++i) { a[i] = __builtin_amdgcn_exp2f(__builtin_fmaf(p[i], LOG2E, mxl)); b[i] = __builtin_amdgcn_exp2f(__builtin_fmaf(p[i], -LOG2E, mnl)); sa += a[i]; sb += b[i]; }
    sa = x16sum(sa); sb = x16sum(sb);
    const float ra = __builtin_amdgcn_rcpf(sa), rb = __builtin_amdgcn_rcpf(sb);
#pragma unroll
    for (int i = 0; i < 9; ++i) { a[i] *= ra; b[i] *= rb; }
    o[0] = cvtpk(a[0], a[1]); o[1] = cvtpk(a[2], a[3]); o[2] = cvtpk(a[4], a[5]); o[3] = cvtpk(a[6], a[7]); o[4] = cvtpk(a[8], b[0]);
    o[5] = cvtpk(b[1], b[2]); o[6] = cvtpk(b[3], b[4]); o[7] = cvtpk(b[5], b[6]); o[8] = cvtpk(b[7], b[8]);
}

__global__ __launch_bounds__(512, 2) void main_kernel(const char* __restrict__ x, char* __restrict__ ws, u16* __restrict__ out) {
    extern __shared__ __attribute__((aligned(16))) char lds_raw[];
    const lptr lds = (lptr)lds_raw;
    const unsigned lds0 = (unsigned)(uintptr_t)lds_raw;
    const int tid = threadIdx.x, lane = tid & 63, wid = __builtin_amdgcn_readfirstlane(tid >> 6);
    const int b = blockIdx.x, hp = (b & 7) * 2 + ((b >> 3) & 1), rt = b >> 4;
    const int h = wid >> 2, tg = h ? 3 - (wid & 3) : (wid & 3);
    const int r16 = lane & 15, g = lane >> 4;
    const unsigned epoch = 1;
    gu32* flags = (gu32*)(ws + WS_FLAG);
    gu32* F_KV = flags;
    gu32* F_P = flags + 256 * 32;
    gu32* F_O = flags + 512 * 32;
    const __amdgpu_buffer_rsrc_t wsr = __builtin_amdgcn_make_buffer_rsrc(ws, 0, 0x2000000, 0x00020000);

    f32x4 acc[7][2];
#pragma unroll
    for (int t = 0; t < 7; ++t) { acc[t][0] = f32x4{0, 0, 0, 0}; acc[t][1] = f32x4{0, 0, 0, 0}; }
    {
        unsigned xo[2];
#pragma unroll
        for (int i = 0; i < 2; ++i) { const int row = 16 * wid + 8 * i + (lane >> 3); xo[i] = (unsigned)(rt * 128 + row) * 1152u + (unsigned)(((lane & 7) ^ ((row >> 1) & 7)) << 4); }
        const char* wt = ws + WS_WT + (size_t)hp * 9 * 28672;
        const unsigned wo = (unsigned)(wid * 1024 + lane * 16);
        auto issue = [&](int kb, int slot) {
            const unsigned sb = lds0 + slot * STAGE;
            glds16(xo[0], x + kb * 128, sb + wid * 2048); glds16(xo[1], x + kb * 128, sb + wid * 2048 + 1024);
            const char* wk = wt + kb * 28672;
            glds16(wo, wk, sb + 16384 + wid * 1024); glds16(wo, wk + 8192, sb + 16384 + 8192 + wid * 1024); glds16(wo, wk + 16384, sb + 16384 + 16384 + wid * 1024);
            if (wid < 4) glds16(wo, wk + 24576, sb + 16384 + 24576 + wid * 1024);
        };
        const int xr = (32 * tg + r16) * 128 + ((g ^ (r16 >> 1)) << 4);
        const int wr = 16384 + h * 14336 + lane * 16;
        issue(0, 0); issue(1, 1);
        int slot = 0;
        for (int kb = 0; kb < 9; ++kb) {
            if (kb < 8) { if (wid < 4) WAITV(6); else WAITV(5); } else WAITV(0);
            BAR();
            if (kb + 2 < 9) issue(kb + 2, slot == 0 ? 2 : slot - 1);
            const lptr sb = lds + slot * STAGE;
#pragma unroll
            for (int ks = 0; ks < 2; ++ks) {
                const bf16x8 x0 = ld128(sb + (xr ^ (ks * 64))), x1 = ld128(sb + (xr ^ (ks * 64)) + 2048);
#pragma unroll
                for (int t = 0; t < 7; ++t) { const bf16x8 w = ld128(sb + wr + t * 2048 + ks * 1024); acc[t][0] = MFMA16(w, x0, acc[t][0]); acc[t][1] = MFMA16(w, x1, acc[t][1]); }
            }
            slot = slot == 2 ? 0 : slot + 1;
        }
    }
    BAR();
    bf16x8 phiF[2][3];
    {
        const char* pf = ws + WS_PHI + (size_t)(hp * 2 + h) * 6144 + lane * 16;
#pragma unroll
        for (int X = 0; X < 2; ++X)
#pragma unroll
            for (int i = 0; i < 3; ++i) phiF[X][i] = *(const bf16x8*)(pf + (X * 3 + i) * 1024);
    }
    unsigned pk45[2][3][2];
    {
        const lptr Kb = lds + L_K + h * 36864, Vb = Kb + 18432, Qb = lds + L_Q + h * 6144, VAb = lds + L_VA + h * 6144;
#pragma unroll
        for (int tt = 0; tt < 2; ++tt) {
            const int tok = 32 * tg + 16 * tt + r16;
            *(LAS u32x2*)(VAb + tok * 48 + 8 * g) = u32x2{cvtpk(acc[0][tt][0], acc[0][tt][1]), cvtpk(acc[0][tt][2], acc[0][tt][3])};
            *(LAS u32x2*)(Qb + tok * 48 + 8 * g) = u32x2{cvtpk(acc[1][tt][0] * QSCALE, acc[1][tt][1] * QSCALE), cvtpk(acc[1][tt][2] * QSCALE, acc[1][tt][3] * QSCALE)};
            *(LAS u32x2*)(Kb + (256 + tok) * 48 + 8 * g) = u32x2{cvtpk(acc[2][tt][0], acc[2][tt][1]), cvtpk(acc[2][tt][2], acc[2][tt][3])};
            *(LAS u32x2*)(Vb + (256 + tok) * 48 + 8 * g) = u32x2{cvtpk(acc[3][tt][0], acc[3][tt][1]), cvtpk(acc[3][tt][2], acc[3][tt][3])};
#pragma unroll
            for (int t = 0; t < 3; ++t) { pk45[tt][t][0] = cvtpk(acc[4 + t][tt][0], acc[4 + t][tt][1]); pk45[tt][t][1] = cvtpk(acc[4 + t][tt][2], acc[4 + t][tt][3]); }
            if (g == 1) {
                *(LAS u32x2*)(VAb + tok * 48 + 32) = u32x2{pk45[tt][2][0], 0x00003f80u};
                *(LAS unsigned*)(Qb + tok * 48 + 32) = cvtpk(acc[6][tt][2] * QSCALE, acc[6][tt][3] * QSCALE);
            } else if (g == 2) {
                *(LAS u32x4*)(Kb + (256 + tok) * 48 + 32) = u32x4{pk45[tt][2][0], 0u, 0u, 0u};
                *(LAS u32x4*)(Vb + (256 + tok) * 48 + 32) = u32x4{pk45[tt][2][1], 0x00003f80u, 0u, 0u};
            } else if (g == 3 && h == 0) {
                const float z = acc[6][tt][0];
                *(LAS float*)(lds + L_GATE + tok * 4) = __builtin_amdgcn_rcpf(1.f + __builtin_amdgcn_exp2f(-z * LOG2E));
            }
        }
        WAITL();
        const unsigned kvo = (unsigned)(WS_KV + (size_t)((rt * 16 + hp) * 4 + h * 2) * 6144 + tg * 1536);
        const u32x4 k0 = *(const LAS u32x4*)(Kb + (256 + 32 * tg) * 48 + lane * 16);
        const u32x4 v0 = *(const LAS u32x4*)(Vb + (256 + 32 * tg) * 48 + lane * 16);
        st_sc1(wsr, kvo + lane * 16, k0); st_sc1(wsr, kvo + 6144 + lane * 16, v0);
        if (lane < 32) {
            const u32x4 k1 = *(const LAS u32x4*)(Kb + (256 + 32 * tg) * 48 + 1024 + lane * 16);
            const u32x4 v1 = *(const LAS u32x4*)(Vb + (256 + 32 * tg) * 48 + 1024 + lane * 16);
            st_sc1(wsr, kvo + 1024 + lane * 16, k1); st_sc1(wsr, kvo + 6144 + 1024 + lane * 16, v1);
        }
    }
    publish(F_KV + (hp * 16 + rt) * 32, epoch, tid);

    unsigned phq[9];
    {
        const bool lo = r16 < 8;
        unsigned phk[9];
#pragma unroll
        for (int X = 0; X < 2; ++X) {
            const bf16x8 bA = mk8(pk45[0][X][0], pk45[0][X][1], pk45[0][X + 1][0], pk45[0][X + 1][1]);
            const bf16x8 bB = mk8(pk45[1][X][0], pk45[1][X][1], pk45[1][X + 1][0], pk45[1][X + 1][1]);
            float p[12];
#pragma unroll
            for (int i = 0; i < 3; ++i) {
                const u32x4 F = __builtin_bit_cast(u32x4, phiF[X][i]);
                const bf16x8 Flo = mk8(lo ? F[0] : 0u, lo ? F[1] : 0u, lo ? F[2] : 0u, lo ? F[3] : 0u), Fhi = mk8(lo ? 0u : F[0], lo ? 0u : F[1], lo ? 0u : F[2], lo ? 0u : F[3]);
                f32x4 d = MFMA16(Flo, bA, (f32x4{0, 0, 0, 0})); d = MFMA16(Fhi, bB, d);
                p[4 * i] = d[0]; p[4 * i + 1] = d[1]; p[4 * i + 2] = d[2]; p[4 * i + 3] = d[3];
            }
            float p9[9];
#pragma unroll
            for (int i = 0; i < 9; ++i) p9[i] = p[i];
            if (X == 0) feature_map(p9, phq); else feature_map(p9, phk);
        }
        const lptr pkp = lds + L_PK + h * 10240 + (32 * tg + 16 * (g >> 1) + r16) * 80 + (g & 1) * 40;
        *(LAS u32x2*)(pkp) = u32x2{phk[0], phk[1]}; *(LAS u32x2*)(pkp + 8) = u32x2{phk[2], phk[3]}; *(LAS u32x2*)(pkp + 16) = u32x2{phk[4], phk[5]};
        *(LAS u32x2*)(pkp + 24) = u32x2{phk[6], phk[7]}; *(LAS u32x2*)(pkp + 32) = u32x2{phk[8], 0u};
    }
    BAR();
    if (wid < 6) {
        const int hh = wid / 3, mt = wid % 3, q4 = r16 >> 2, p4 = r16 & 3;
        int u = 4 * mt + p4; if (u > 9) u = 9;
        const lptr pa = lds + L_PK + hh * 10240 + (8 * g + q4) * 80 + u * 8;
        const lptr pb0 = lds + L_VA + hh * 6144 + (8 * g + q4) * 48 + p4 * 8;
        const lptr pb1 = lds + L_VA + hh * 6144 + (8 * g + q4) * 48 + 32;
        f32x4 d0 = {0, 0, 0, 0}, d1 = {0, 0, 0, 0};
#pragma unroll
        for (int ks = 0; ks < 4; ++ks) {
            const bf16x8 a = cat8(trrd(pa + ks * 32 * 80), trrd(pa + ks * 32 * 80 + 4 * 80));
            const bf16x8 b0 = cat8(trrd(pb0 + ks * 32 * 48), trrd(pb0 + ks * 32 * 48 + 4 * 48));
            const bf16x8 b1 = cat8(trrd(pb1 + ks * 32 * 48), trrd(pb1 + ks * 32 * 48 + 4 * 48));
            d0 = MFMA16(a, b0, d0); d1 = MFMA16(a, b1, d1);
        }
        const int uu = 4 * mt + g;
        const unsigned po = (unsigned)(WS_PART + (size_t)(hp * 16 + rt) * 6144 + hh * 3040);
        if (uu < 10) {
            st_sc1(wsr, po + (r16 * 10 + uu) * 16, __builtin_bit_cast(u32x4, d0));
            if (r16 < 3) st_sc1(wsr, po + ((16 + r16) * 10 + uu) * 16, __builtin_bit_cast(u32x4, d1));
        }
    }
    publish(F_P + (hp * 16 + rt) * 32, epoch, tid);

    const int q32 = lane & 31, hh = lane >> 5;
    f32x16 oacc;
    {
        const lptr Kb = lds + L_K + h * 36864, Vb = Kb + 18432, Qb = lds + L_Q + h * 6144;
        const bf16x8 qf0 = ld128(Qb + (32 * tg + q32) * 48 + 16 * hh);
        const unsigned q16 = *(const LAS unsigned*)(Qb + (32 * tg + q32) * 48 + 32);
        const bf16x8 qf1 = mk8(hh ? 0u : q16, 0u, 0u, 0u);
        const lptr kp = Kb + (32 * tg + q32) * 48;
        const int i16 = lane & 15, dhalf = (lane >> 4) & 1;
        const lptr vp = Vb + (32 * tg + 4 * hh + (i16 >> 2)) * 48 + (dhalf ? 32 + ((i16 & 3) ? 8 : 0) : (i16 & 3) * 8);
        f32x16 negm;
#pragma unroll
        for (int r = 0; r < 16; ++r) { oacc[r] = 0.f; negm[r] = 0.f; }
        auto tile = [&](int KT, bool first, int mask) {
            const bf16x8 k0 = ld128(kp + KT * 32 * 48 + 16 * hh), k1 = ld128(kp + KT * 32 * 48 + 32);
            f32x16 s = MFMA32(k0, qf0, negm); s = MFMA32(k1, qf1, s);
            if (mask) {
#pragma unroll
                for (int r = 0; r < 16; ++r) { const int jj = (r & 3) + 8 * (r >> 2) + 4 * hh; const bool ok = mask == 1 ? jj <= q32 : jj > q32; s[r] = ok ? s[r] : -INFINITY; }
            }
            float m = fmaxf(fmaxf(s[0], s[1]), s[2]);
#pragma unroll
            for (int r = 3; r < 15; r += 2) m = fmaxf(fmaxf(m, s[r]), s[r + 1]);
            m = x32max(fmaxf(m, s[15]));
            if (first || __any(m > THR)) {
                const float dl = first ? m : fmaxf(m, 0.f);
                const float f = first ? 1.f : __builtin_amdgcn_exp2f(-dl);
#pragma unroll
                for (int r = 0; r < 16; ++r) { s[r] -= dl; negm[r] -= dl; oacc[r] *= f; }
            }
            unsigned pw[8];
#pragma unroll
            for (int r = 0; r < 8; ++r) pw[r] = cvtpk(__builtin_amdgcn_exp2f(s[2 * r]), __builtin_amdgcn_exp2f(s[2 * r + 1]));
#pragma unroll
            for (int s2 = 0; s2 < 2; ++s2) {
                const bf16x8 vf = cat8(trrd(vp + (KT * 32 + 16 * s2) * 48), trrd(vp + (KT * 32 + 16 * s2 + 8) * 48));
                oacc = MFMA32(vf, mk8(pw[4 * s2], pw[4 * s2 + 1], pw[4 * s2 + 2], pw[4 * s2 + 3]), oacc);
            }
        };
        tile(8, true, 1);
        for (int KT = 7; KT >= 8 - tg; --KT) tile(KT, false, 0);
        if (rt > 0) {
            if (wid == 0) { gu32* fb = F_KV + (hp * 16 + (rt > 1 ? rt - 2 : rt - 1)) * 32; wait_flags(fb, rt > 1 ? 2 : 1, epoch, lane); }
            BAR();
            u32x4 hv[6];
#pragma unroll
            for (int i = 0; i < 6; ++i) { const int pc = i * 8 + wid;
                const int tl = pc / 24, ar = (pc % 24) / 6, o = pc % 6; const int srt = rt - 2 + tl;
                hv[i] = (srt >= 0) ? ld_sc1(wsr, (unsigned)(WS_KV + (size_t)((srt * 16 + hp) * 4 + ar) * 6144 + o * 1024 + lane * 16)) : u32x4{0, 0, 0, 0}; }
#pragma unroll
            for (int i = 0; i < 6; ++i) { const int pc = i * 8 + wid; const int tl = pc / 24, ar = (pc % 24) / 6, o = pc % 6;
                *(LAS u32x4*)(lds + L_K + ar * 18432 + tl * 6144 + o * 1024 + lane * 16) = hv[i]; }
            BAR();
            const int lo = rt >= 2 ? 0 : 4 - tg;
            for (int KT = 7 - tg; KT >= (lo > 1 ? lo : 1); --KT) tile(KT, false, 0);
            if (lo <= 0) tile(0, false, 2);
        }
    }
    if (wid == 0) wait_flags(F_P + hp * 16 * 32, 16, epoch, lane);
    BAR();
    if (tid < 380) {
        f32x4 s = {0, 0, 0, 0};
        u32x4 pv[16];
#pragma unroll
        for (int i = 0; i < 16; ++i) pv[i] = ld_sc1(wsr, (unsigned)(WS_PART + (size_t)(hp * 16 + i) * 6144 + tid * 16));
#pragma unroll
        for (int i = 0; i < 16; ++i) s += __builtin_bit_cast(f32x4, pv[i]);
        const int ph = tid / 190, e0 = tid % 190, d = e0 / 10, u = e0 % 10;
        *(LAS u32x2*)(lds + L_KVT + ph * 2016 + d * 96 + (u / 5) * 48 + (u % 5) * 8) = u32x2{cvtpk(s[0], s[1]), cvtpk(s[2], s[3])};
    } else if (tid < 380 + 2 * 40) {
        const int e0 = tid - 380, ph = e0 / 40, e1 = e0 % 40;
        if (e1 < 38) *(LAS u32x2*)(lds + L_KVT + ph * 2016 + (e1 >> 1) * 96 + (e1 & 1) * 48 + 40) = u32x2{0u, 0u};
    }
    if (tid >= 500) { const int e0 = tid - 500; *(LAS u32x4*)(lds + L_KVT + (e0 / 6) * 2016 + 20 * 96 + (e0 % 6) * 16) = u32x4{0, 0, 0, 0}; }
    BAR();
    {
        f32x4 dd[3];
        const int eo = (r16 >> 2) & 1;
        const bool mine = (r16 >> 3) == (g >> 1);
        const bf16x8 b0 = mk8(phq[0], phq[1], phq[2], phq[3]), b1 = mk8(phq[4], phq[5], phq[6], phq[7]), b2 = mk8(phq[8], 0u, 0u, 0u);
#pragma unroll
        for (int i = 0; i < 3; ++i) {
            const int sg = 4 * i + (r16 & 3);
            int d = eo ? (sg < 8 ? 10 + sg : 20) : (sg < 10 ? sg : 20); if (sg == 10) d = 18; if (!mine) d = 20;
            const lptr ap = lds + L_KVT + h * 2016 + d * 96 + (g & 1) * 48;
            f32x4 a = MFMA16(ld128(ap), b0, (f32x4{0, 0, 0, 0})); a = MFMA16(ld128(ap + 16), b1, a); a = MFMA16(ld128(ap + 32), b2, a);
            dd[i] = a;
        }
        const int tok = 32 * tg + 16 * (g >> 1) + r16;
        const float gate = *(const LAS float*)(lds + L_GATE + tok * 4);
        const float sc = gate * __builtin_amdgcn_rcpf(dd[2][2] + 1e-6f);
        const lptr op = lds + L_OST + tok * 144 + 72 + h * 36 + (g & 1) * 20;
        *(LAS unsigned*)(op) = cvtpk(dd[0][0] * sc, dd[0][1] * sc); *(LAS unsigned*)(op + 4) = cvtpk(dd[0][2] * sc, dd[0][3] * sc);
        *(LAS unsigned*)(op + 8) = cvtpk(dd[1][0] * sc, dd[1][1] * sc); *(LAS unsigned*)(op + 12) = cvtpk(dd[1][2] * sc, dd[1][3] * sc);
        if ((g & 1) == 0) *(LAS unsigned*)(op + 16) = cvtpk(dd[2][0] * sc, dd[2][1] * sc);
        const int tokb = 32 * tg + q32;
        auto sw = __builtin_amdgcn_permlane32_swap(__float_as_uint(oacc[10]), __float_as_uint(oacc[10]), false, false);
        const float gb = *(const LAS float*)(lds + L_GATE + tokb * 4);
        const float sb = (1.f - gb) * __builtin_amdgcn_rcpf(__uint_as_float(sw[0]));
        const lptr ob = lds + L_OST + tokb * 144;
        *(LAS u32x2*)(ob + h * 32 + 8 * hh) = u32x2{cvtpk(oacc[0] * sb, oacc[1] * sb), cvtpk(oacc[2] * sb, oacc[3] * sb)};
        *(LAS u32x2*)(ob + h * 32 + 16 + 8 * hh) = u32x2{cvtpk(oacc[4] * sb, oacc[5] * sb), cvtpk(oacc[6] * sb, oacc[7] * sb)};
        if (hh == 0) *(LAS unsigned*)(ob + 64 + h * 4) = cvtpk(oacc[8] * sb, oacc[9] * sb);
    }
    BAR();
#pragma unroll
    for (int i = 0; i < 3; ++i) {
        const int e0 = i * 512 + tid;
        if (e0 < 1152) { const int R = e0 / 144, e1 = e0 % 144, c = e1 >> 4, tk = e1 & 15;
            const u32x4 v = *(const LAS u32x4*)(lds + L_OST + (16 * R + tk) * 144 + c * 16);
            st_sc1(wsr, (unsigned)(WS_O + ((size_t)(rt * 8 + R) * 144 + 9 * hp) * 256 + e1 * 16), v); }
    }
    publish(F_O + (rt * 16 + hp) * 32, epoch, tid);

    {
        const int cg = hp & 7, rh = hp >> 3, kh = wid >> 2, r = wid & 3;
        const char* wot = ws + WS_WOT + (size_t)cg * 165888;
        auto issue3 = [&](int s, int slot) {
            const unsigned p = wid * 1024 + lane * 16;
            const unsigned src = p < 4608 ? s * 4608 + p : 18 * 4608 + s * 4608 + (p - 4608);
            glds16(src, wot, lds0 + slot * G3_SLOT + wid * 1024);
            if (wid == 0) glds16(18 * 4608 + s * 4608 + (8192 - 4608) + lane * 16, wot, lds0 + slot * G3_SLOT + 8192);
        };
        for (int s = 0; s < G3_NSLOT; ++s) issue3(s, s);
        if (wid == 0) wait_flags(F_O + rt * 16 * 32, 16, epoch, lane);
        BAR();
        u32x4 of[18];
        const unsigned ob = (unsigned)(WS_O + ((size_t)(rt * 8 + 4 * rh + r) * 144 + 72 * kh) * 256 + lane * 16);
#pragma unroll
        for (int s = 0; s < 18; ++s) of[s] = ld_sc1(wsr, ob + s * 1024);
        f32x4 a3[5];
#pragma unroll
        for (int t = 0; t < 5; ++t) a3[t] = f32x4{0, 0, 0, 0};
        WAITV(18);
        BAR();
        const int wro = kh * 4608 + lane * 16, wr4 = kh * 4608 + 4096 + g * 128 + (r16 & 7) * 16;
#pragma unroll
        for (int s = 0; s < 18; ++s) {
            if (s == 3) { BAR(); for (int s2 = G3_NSLOT; s2 < 18; ++s2) issue3(s2, s2 - G3_NSLOT); }
            if (s == G3_NSLOT) { WAITV(0); BAR(); }
            const lptr sp = lds + (s < G3_NSLOT ? s : s - G3_NSLOT) * G3_SLOT;
            const bf16x8 bo = __builtin_bit_cast(bf16x8, of[s]);
#pragma unroll
            for (int t = 0; t < 4; ++t) a3[t] = MFMA16(ld128(sp + wro + t * 1024), bo, a3[t]);
            a3[4] = MFMA16(ld128(sp + wr4), bo, a3[4]);
        }
        BAR();
        const lptr rp = lds + kh * 19456 + (16 * r + r16) * 304 + g * 16;
#pragma unroll
        for (int t = 0; t < 4; ++t) *(LAS f32x4*)(rp + t * 64) = a3[t];
        if (g < 2) *(LAS f32x4*)(rp + 256) = a3[4];
        BAR();
        for (int e0 = tid; e0 < 576; e0 += 512) {
            const int tk = e0 / 9, c = e0 % 9;
            const lptr p0 = lds + tk * 304 + c * 32;
            const f32x4 u0 = *(const LAS f32x4*)(p0) + *(const LAS f32x4*)(p0 + 19456), u1 = *(const LAS f32x4*)(p0 + 16) + *(const LAS f32x4*)(p0 + 19456 + 16);
            const u32x4 v = {cvtpk(u0[0], u0[1]), cvtpk(u0[2], u0[3]), cvtpk(u1[0], u1[1]), cvtpk(u1[2], u1[3])};
            *(u32x4*)((char*)out + (size_t)(rt * 128 + 64 * rh + tk) * 1152 + cg * 144 + c * 16) = v;
        }
    }
}

extern "C" void kernel_launch(void* const* d_in, const int* in_sizes, int n_in, void* d_out, int out_size, void* d_ws, size_t ws_size, hipStream_t stream) {
    const u16* x = (const u16*)d_in[0]; const u16* wA = (const u16*)d_in[1]; const u16* wphi = (const u16*)d_in[2]; const u16* woA = (const u16*)d_in[3];
    const u16* wB = (const u16*)d_in[5]; const u16* woB = (const u16*)d_in[6]; const u16* gw = (const u16*)d_in[7];
    constexpr int NTOT = 16 * 9 * 28 * 64 + 16 * 12 * 64 + 8 * 36 * 288 + 768 * 8;
    prep_kernel<<<(NTOT + 255) / 256, 256, 0, stream>>>(wA, wB, wphi, woA, woB, gw, (char*)d_ws);
    (void)hipFuncSetAttribute((const void*)main_kernel, hipFuncAttributeMaxDynamicSharedMemorySize, LDS_BYTES);
    main_kernel<<<256, 512, LDS_BYTES, stream>>>((const char*)x, (char*)d_ws, (u16*)d_out);
}
```

```cpp
#include <hip/hip_runtime.h>
#include <hip/hip_bf16.h>
#include <stdint.h>

typedef unsigned short u16;
typedef short bf16x8 __attribute__((ext_vector_type(8)));
typedef short s16x4 __attribute__((ext_vector_type(4)));
typedef float f32x4 __attribute__((ext_vector_type(4)));
typedef float f32x16 __attribute__((ext_vector_type(16)));
typedef unsigned u32x4 __attribute__((ext_vector_type(4)));
typedef unsigned u32x2 __attribute__((ext_vector_type(2)));
#define LAS __attribute__((address_space(3)))
#define GAS __attribute__((address_space(1)))
typedef LAS char* lptr;
typedef GAS unsigned gu32;

constexpr int DM = 576;
constexpr size_t WS_FLAG = 0;
constexpr size_t WS_WT   = 131072;
constexpr size_t WS_PHI  = WS_WT + 16 * 9 * 28672;
constexpr size_t WS_WOT  = WS_PHI + 16 * 12288;
constexpr size_t WS_KV   = WS_WOT + 8 * 165888;
constexpr size_t WS_PART = WS_KV + 256 * 24576;
constexpr size_t WS_O    = WS_PART + 256 * 6144;
constexpr size_t WS_DBG  = WS_O + 16 * 8 * 36864;

typedef __bf16 bf16x2_t __attribute__((ext_vector_type(2)));
typedef float f32x2 __attribute__((ext_vector_type(2)));
__device__ __forceinline__ unsigned cvtpk(float lo, float hi) { const f32x2 v = {lo, hi}; return __builtin_bit_cast(unsigned, __builtin_convertvector(v, bf16x2_t)); }
__device__ __forceinline__ float bflo(unsigned v) { return __uint_as_float(v << 16); }
__device__ __forceinline__ float bfhi(unsigned v) { return __uint_as_float(v & 0xffff0000u); }

__device__ __forceinline__ int ostage_row(int hp, int p, int& br) {
    int head, d;
    if (p < 16) { br = 1; head = 0; d = p; } else if (p < 32) { br = 1; head = 1; d = p - 16; }
    else if (p < 34) { br = 1; head = 0; d = 16 + p - 32; } else if (p < 36) { br = 1; head = 1; d = 16 + p - 34; }
    else if (p < 54) { br = 0; head = 0; d = p - 36; } else { br = 0; head = 1; d = p - 54; }
    return (2 * hp + head) * 18 + d;
}
__global__ __launch_bounds__(256) void prep_kernel(const u16* __restrict__ wA, const u16* __restrict__ wB, const u16* __restrict__ wphi, const u16* __restrict__ woA,
                                                   const u16* __restrict__ woB, const u16* __restrict__ gw, char* __restrict__ ws) {
    const int id = blockIdx.x * 256 + threadIdx.x;
    constexpr int NA = 16 * 9 * 28 * 64, NB = 16 * 12 * 64, NC = 8 * 36 * 288, ND = 768 * 8;
    u16 v[8];
    if (id < NA) {
        const int lane = id & 63; int f = id >> 6; const int ks = f & 1; f >>= 1; const int t = f % 7; f /= 7; const int h = f & 1; f >>= 1; const int kb = f % 9, hp = f / 9;
        const int r = lane & 15, g = lane >> 4, k0 = kb * 64 + ks * 32 + 8 * g;
        int br = 0, m = 0, d = 0, kind = 0;
        if (t == 0) { br = 0; m = 2; d = r; } else if (t == 1) { br = 1; m = 0; d = r; } else if (t == 2) { br = 1; m = 1; d = r; } else if (t == 3) { br = 1; m = 2; d = r; }
        else if (t == 4) { br = 0; m = 0; d = r; } else if (t == 5) { br = 0; if (r < 2) { m = 0; d = 16 + r; } else { m = 1; d = r - 2; } }
        else { if (r < 4) { br = 0; m = 1; d = 14 + r; } else if (r < 6) { br = 0; m = 2; d = 12 + r; } else if (r < 8) { br = 1; m = 0; d = 10 + r; }
               else if (r < 10) { br = 1; m = 1; d = 8 + r; } else if (r < 12) { br = 1; m = 2; d = 6 + r; } else kind = (r == 12) ? 1 : 2; }
        const u16* src = (br ? wB : wA) + m * DM + (2 * hp + h) * 18 + d;
        for (int j = 0; j < 8; ++j) v[j] = kind == 0 ? src[(size_t)(k0 + j) * 1728] : kind == 1 ? gw[k0 + j] : (u16)0;
        *(uint4*)(ws + WS_WT + (size_t)id * 16) = *(uint4*)v;
    } else if (id < NA + NB) {
        const int e0 = id - NA, lane = e0 & 63; int f = e0 >> 6; const int i = f % 3; f /= 3; const int X = f & 1; f >>= 1; const int h = f & 1, hp = f >> 1;
        const int r = lane & 15, g = lane >> 4, e = (r >> 2) & 1, sg = 4 * i + (r & 3), ff = 9 * e + sg;
        for (int j = 0; j < 8; ++j) { int d = -1; const int rho = 4 * g + (j & 3);
            if (X == 0) { if (j < 4) d = rho; else if (rho < 2) d = 16 + rho; } else { if (j < 4) { if (rho >= 2) d = rho - 2; } else if (rho < 4) d = 14 + rho; }
            v[j] = (d >= 0 && sg < 9) ? wphi[((2 * hp + h) * 18 + d) * 18 + ff] : (u16)0; }
        *(uint4*)(ws + WS_PHI + (size_t)e0 * 16) = *(uint4*)v;
    } else if (id < NA + NB + NC) {
        const int e0 = id - NA - NB; const int q = e0 % 288; int f = e0 / 288; const int s = f % 18; f /= 18; const int kh = f & 1, cg = f >> 1;
        int t, r, g; if (q < 256) { t = q >> 6; r = q & 15; g = (q >> 4) & 3; } else { t = 4; r = q & 7; g = (q - 256) >> 3; }
        const int C = 72 * kh + 4 * s + g, hp = C / 9, c = C % 9, col = cg * 72 + 16 * t + r;
        for (int j = 0; j < 8; ++j) { int br; const int row = ostage_row(hp, 8 * c + j, br); v[j] = (br ? woB : woA)[row * DM + col]; }
        *(uint4*)(ws + WS_WOT + (size_t)e0 * 16) = *(uint4*)v;
    } else if (id < NA + NB + NC + ND) {
        const int e0 = id - NA - NB - NC; *(uint4*)(ws + WS_FLAG + (size_t)e0 * 16) = make_uint4(0, 0, 0, 0);
    }
}

constexpr int STAGE = 16384 + 28672;
constexpr int L_K = 0;
constexpr int L_Q = 73728;
constexpr int L_VA = 86016;
constexpr int L_PK = 98304;
constexpr int L_GATE = 118784;
constexpr int L_KVT = 119296;
constexpr int L_OST = 123392;
constexpr int LDS_BYTES = 160 * 1024;
constexpr int G3_SLOT = 9216, G3_NSLOT = 15;
constexpr unsigned SPIN_CAP = 1u << 18;

__device__ __forceinline__ void glds16(unsigned voff, const void* sbase, unsigned lds_addr) {
    unsigned sv;
    asm volatile("s_mov_b32 %0, m0\n\ts_mov_b32 m0, %3\n\ts_nop 0\n\tglobal_load_lds_dwordx4 %1, %2\n\ts_mov_b32 m0, %0" : "=&s"(sv) : "v"(voff), "s"(sbase), "s"(lds_addr) : "memory");
}
#define WAITV(n) asm volatile("s_waitcnt vmcnt(" #n ")" ::: "memory")
#define WAITL() asm volatile("s_waitcnt lgkmcnt(0)" ::: "memory")
#define BAR() asm volatile("s_waitcnt lgkmcnt(0)\n\ts_barrier" ::: "memory")
#define MFMA16(a, b, c) __builtin_amdgcn_mfma_f32_16x16x32_bf16(a, b, c, 0, 0, 0)
#define MFMA32(a, b, c) __builtin_amdgcn_mfma_f32_32x32x16_bf16(a, b, c, 0, 0, 0)
__device__ __forceinline__ bf16x8 ld128(lptr p) { return *(const LAS bf16x8*)p; }
__device__ __forceinline__ s16x4 trrd(lptr p) { typedef short v4i16_t __attribute__((ext_vector_type(4))); return __builtin_bit_cast(s16x4, __builtin_amdgcn_ds_read_tr16_b64_v4i16((LAS v4i16_t*)p)); }
__device__ __forceinline__ bf16x8 cat8(s16x4 a, s16x4 b) { return __builtin_shufflevector(a, b, 0, 1, 2, 3, 4, 5, 6, 7); }
__device__ __forceinline__ bf16x8 mk8(unsigned a, unsigned b, unsigned c, unsigned d) { u32x4 w = {a, b, c, d}; return __builtin_bit_cast(bf16x8, w); }
__device__ __forceinline__ float x16max(float x) { auto s = __builtin_amdgcn_permlane16_swap(__float_as_uint(x), __float_as_uint(x), false, false); return fmaxf(__uint_as_float(s[0]), __uint_as_float(s[1])); }
__device__ __forceinline__ float x16sum(float x) { auto s = __builtin_amdgcn_permlane16_swap(__float_as_uint(x), __float_as_uint(x), false, false); return __uint_as_float(s[0]) + __uint_as_float(s[1]); }
__device__ __forceinline__ float x32max(float x) { auto s = __builtin_amdgcn_permlane32_swap(__float_as_uint(x), __float_as_uint(x), false, false); return fmaxf(__uint_as_float(s[0]), __uint_as_float(s[1])); }
__device__ __forceinline__ void st_sc1(const __amdgpu_buffer_rsrc_t rs, unsigned off, u32x4 v) { __builtin_amdgcn_raw_buffer_store_b128(v, rs, off, 0, 16); }
__device__ __forceinline__ u32x4 ld_sc1(const __amdgpu_buffer_rsrc_t rs, unsigned off) { return __builtin_amdgcn_raw_buffer_load_b128(rs, off, 0, 16); }
__device__ __forceinline__ void wait_flags(gu32* base, int n, unsigned epoch, int lane) {
    gu32* p = base + (lane < n ? lane : 0) * 32;
    for (unsigned spins = 0; spins < SPIN_CAP; ++spins) {
        const unsigned v = __hip_atomic_load(p, __ATOMIC_RELAXED, __HIP_MEMORY_SCOPE_AGENT);
        if (__all(v == epoch)) break;
        __builtin_amdgcn_s_sleep(1);
    }
}
__device__ __forceinline__ void publish(gu32* flag, unsigned epoch, int tid) {
    asm volatile("s_waitcnt vmcnt(0) lgkmcnt(0)\n\ts_barrier" ::: "memory");
    if (tid == 0) __hip_atomic_store(flag, epoch, __ATOMIC_RELAXED, __HIP_MEMORY_SCOPE_AGENT);
}

#define STAMP(i) do {} while (0)
constexpr float LOG2E = 1.4426950408889634f;
constexpr float QSCALE = 0.23570226039551584f * LOG2E;
constexpr float THR = 8.f;

__device__ __forceinline__ void feature_map(const float (&p)[9], unsigned (&o)[9]) {
    float mx = fmaxf(fmaxf(p[0], p[1]), p[2]), mn = fminf(fminf(p[0], p[1]), p[2]);
#pragma unroll
    for (int i = 3; i < 9; i += 2) { mx = fmaxf(fmaxf(mx, p[i]), p[i + 1]); mn = fminf(fminf(mn, p[i]), p[i + 1]); }
    mx = x16max(mx); mn = -x16max(-mn);
    const float mxl = -mx * LOG2E, mnl = mn * LOG2E;
    float a[9], b[9], sa = 0.f, sb = 0.f;
#pragma unroll
    for (int i = 0; i < 9; ++i) { a[i] = __builtin_amdgcn_exp2f(__builtin_fmaf(p[i], LOG2E, mxl)); b[i] = __builtin_amdgcn_exp2f(__builtin_fmaf(p[i], -LOG2E, mnl)); sa += a[i]; sb += b[i]; }
    sa = x16sum(sa); sb = x16sum(sb);
    const float ra = __builtin_amdgcn_rcpf(sa), rb = __builtin_amdgcn_rcpf(sb);
#pragma unroll
    for (int i = 0; i < 9; ++i) { a[i] *= ra; b[i] *= rb; }
    o[0] = cvtpk(a[0], a[1]); o[1] = cvtpk(a[2], a[3]); o[2] = cvtpk(a[4], a[5]); o[3] = cvtpk(a[6], a[7]); o[4] = cvtpk(a[8], b[0]);
    o[5] = cvtpk(b[1], b[2]); o[6] = cvtpk(b[3], b[4]); o[7] = cvtpk(b[5], b[6]); o[8] = cvtpk(b[7], b[8]);
}

__global__ __launch_bounds__(512, 2) void main_kernel(const char* __restrict__ x, char* __restrict__ ws, u16* __restrict__ out) {
    extern __shared__ __attribute__((aligned(16))) char lds_raw[];
    const lptr lds = (lptr)lds_raw;
    const unsigned lds0 = (unsigned)(uintptr_t)lds_raw;
    const int tid = threadIdx.x, lane = tid & 63, wid = __builtin_amdgcn_readfirstlane(tid >> 6);
    const int b = blockIdx.x, hp = (b & 7) * 2 + ((b >> 3) & 1), rt = b >> 4;
    const int h = wid >> 2, tg = h ? 3 - (wid & 3) : (wid & 3);
    const int r16 = lane & 15, g = lane >> 4;
    const unsigned epoch = 1;
    gu32* flags = (gu32*)(ws + WS_FLAG);
    gu32* F_KV = flags;
    gu32* F_P = flags + 256 * 32;
    gu32* F_O = flags + 512 * 32;
    const __amdgpu_buffer_rsrc_t wsr = __builtin_amdgcn_make_buffer_rsrc(ws, 0, 0x2000000, 0x00020000);

    STAMP(0);
    f32x4 acc[7][2];
#pragma unroll
    for (int t = 0; t < 7; ++t) { acc[t][0] = f32x4{0, 0, 0, 0}; acc[t][1] = f32x4{0, 0, 0, 0}; }
    {
        unsigned xo[2];
#pragma unroll
        for (int i = 0; i < 2; ++i) { const int row = 16 * wid + 8 * i + (lane >> 3); xo[i] = (unsigned)(rt * 128 + row) * 1152u + (unsigned)(((lane & 7) ^ ((row >> 1) & 7)) << 4); }
        const char* wt = ws + WS_WT + (size_t)hp * 9 * 28672;
        const unsigned wo = (unsigned)(wid * 1024 + lane * 16);
        auto issue = [&](int kb, int slot) {
            const unsigned sb = lds0 + slot * STAGE;
            glds16(xo[0], x + kb * 128, sb + wid * 2048); glds16(xo[1], x + kb * 128, sb + wid * 2048 + 1024);
            const char* wk = wt + kb * 28672;
            glds16(wo, wk, sb + 16384 + wid * 1024); glds16(wo, wk + 8192, sb + 16384 + 8192 + wid * 1024); glds16(wo, wk + 16384, sb + 16384 + 16384 + wid * 1024);
            if (wid < 4) glds16(wo, wk + 24576, sb + 16384 + 24576 + wid * 1024);
        };
        const int xr = (32 * tg + r16) * 128 + ((g ^ (r16 >> 1)) << 4);
        const int wr = 16384 + h * 14336 + lane * 16;
        issue(0, 0); issue(1, 1);
        int slot = 0;
        for (int kb = 0; kb < 9; ++kb) {
            if (kb < 8) { if (wid < 4) WAITV(6); else WAITV(5); } else WAITV(0);
            BAR();
            if (kb == 0) STAMP(1);
            if (kb + 2 < 9) issue(kb + 2, slot == 0 ? 2 : slot - 1);
            const lptr sb = lds + slot * STAGE;
#pragma unroll
            for (int ks = 0; ks < 2; ++ks) {
                const bf16x8 x0 = ld128(sb + (xr ^ (ks * 64))), x1 = ld128(sb + (xr ^ (ks * 64)) + 2048);
#pragma unroll
                for (int t = 0; t < 7; ++t) { const bf16x8 w = ld128(sb + wr + t * 2048 + ks * 1024); acc[t][0] = MFMA16(w, x0, acc[t][0]); acc[t][1] = MFMA16(w, x1, acc[t][1]); }
            }
            slot = slot == 2 ? 0 : slot + 1;
        }
    }
    BAR();
    STAMP(2);
    bf16x8 phiF[2][3];
    {
        const char* pf = ws + WS_PHI + (size_t)(hp * 2 + h) * 6144 + lane * 16;
#pragma unroll
        for (int X = 0; X < 2; ++X)
#pragma unroll
            for (int i = 0; i < 3; ++i) phiF[X][i] = *(const bf16x8*)(pf + (X * 3 + i) * 1024);
    }
    unsigned pk45[2][3][2];
    {
        const lptr Kb = lds + L_K + h * 36864, Vb = Kb + 18432, Qb = lds + L_Q + h * 6144, VAb = lds + L_VA + h * 6144;
#pragma unroll
        for (int tt = 0; tt < 2; ++tt) {
            const int tok = 32 * tg + 16 * tt + r16;
            *(LAS u32x2*)(VAb + tok * 48 + 8 * g) = u32x2{cvtpk(acc[0][tt][0], acc[0][tt][1]), cvtpk(acc[0][tt][2], acc[0][tt][3])};
            *(LAS u32x2*)(Qb + tok * 48 + 8 * g) = u32x2{cvtpk(acc[1][tt][0] * QSCALE, acc[1][tt][1] * QSCALE), cvtpk(acc[1][tt][2] * QSCALE, acc[1][tt][3] * QSCALE)};
            *(LAS u32x2*)(Kb + (256 + tok) * 48 + 8 * g) = u32x2{cvtpk(acc[2][tt][0], acc[2][tt][1]), cvtpk(acc[2][tt][2], acc[2][tt][3])};
            *(LAS u32x2*)(Vb + (256 + tok) * 48 + 8 * g) = u32x2{cvtpk(acc[3][tt][0], acc[3][tt][1]), cvtpk(acc[3][tt][2], acc[3][tt][3])};
#pragma unroll
            for (int t = 0; t < 3; ++t) { pk45[tt][t][0] = cvtpk(acc[4 + t][tt][0], acc[4 + t][tt][1]); pk45[tt][t][1] = cvtpk(acc[4 + t][tt][2], acc[4 + t][tt][3]); }
            if (g == 1) {
                *(LAS u32x2*)(VAb + tok * 48 + 32) = u32x2{pk45[tt][2][0], 0x00003f80u};
                *(LAS unsigned*)(Qb + tok * 48 + 32) = cvtpk(acc[6][tt][2] * QSCALE, acc[6][tt][3] * QSCALE);
            } else if (g == 2) {
                *(LAS u32x4*)(Kb + (256 + tok) * 48 + 32) = u32x4{pk45[tt][2][0], 0u, 0u, 0u};
                *(LAS u32x4*)(Vb + (256 + tok) * 48 + 32) = u32x4{pk45[tt][2][1], 0x00003f80u, 0u, 0u};
            } else if (g == 3 && h == 0) {
                const float z = acc[6][tt][0];
                *(LAS float*)(lds + L_GATE + tok * 4) = __builtin_amdgcn_rcpf(1.f + __builtin_amdgcn_exp2f(-z * LOG2E));
            }
        }
        WAITL();
        const unsigned kvo = (unsigned)(WS_KV + (size_t)((rt * 16 + hp) * 4 + h * 2) * 6144 + tg * 1536);
        const u32x4 k0 = *(const LAS u32x4*)(Kb + (256 + 32 * tg) * 48 + lane * 16);
        const u32x4 v0 = *(const LAS u32x4*)(Vb + (256 + 32 * tg) * 48 + lane * 16);
        st_sc1(wsr, kvo + lane * 16, k0); st_sc1(wsr, kvo + 6144 + lane * 16, v0);
        if (lane < 32) {
            const u32x4 k1 = *(const LAS u32x4*)(Kb + (256 + 32 * tg) * 48 + 1024 + lane * 16);
            const u32x4 v1 = *(const LAS u32x4*)(Vb + (256 + 32 * tg) * 48 + 1024 + lane * 16);
            st_sc1(wsr, kvo + 1024 + lane * 16, k1); st_sc1(wsr, kvo + 6144 + 1024 + lane * 16, v1);
        }
    }
    STAMP(3);
    publish(F_KV + (hp * 16 + rt) * 32, epoch, tid);
    STAMP(4);

    unsigned phq[9];
    {
        const bool lo = r16 < 8;
        unsigned phk[9];
#pragma unroll
        for (int X = 0; X < 2; ++X) {
            const bf16x8 bA = mk8(pk45[0][X][0], pk45[0][X][1], pk45[0][X + 1][0], pk45[0][X + 1][1]);
            const bf16x8 bB = mk8(pk45[1][X][0], pk45[1][X][1], pk45[1][X + 1][0], pk45[1][X + 1][1]);
            float p[12];
#pragma unroll
            for (int i = 0; i < 3; ++i) {
                const u32x4 F = __builtin_bit_cast(u32x4, phiF[X][i]);
                const bf16x8 Flo = mk8(lo ? F[0] : 0u, lo ? F[1] : 0u, lo ? F[2] : 0u, lo ? F[3] : 0u), Fhi = mk8(lo ? 0u : F[0], lo ? 0u : F[1], lo ? 0u : F[2], lo ? 0u : F[3]);
                f32x4 d = MFMA16(Flo, bA, (f32x4{0, 0, 0, 0})); d = MFMA16(Fhi, bB, d);
                p[4 * i] = d[0]; p[4 * i + 1] = d[1]; p[4 * i + 2] = d[2]; p[4 * i + 3] = d[3];
            }
            float p9[9];
#pragma unroll
            for (int i = 0; i < 9; ++i) p9[i] = p[i];
            if (X == 0) feature_map(p9, phq); else feature_map(p9, phk);
        }
        const lptr pkp = lds + L_PK + h * 10240 + (32 * tg + 16 * (g >> 1) + r16) * 80 + (g & 1) * 40;
        *(LAS u32x2*)(pkp) = u32x2{phk[0], phk[1]}; *(LAS u32x2*)(pkp + 8) = u32x2{phk[2], phk[3]}; *(LAS u32x2*)(pkp + 16) = u32x2{phk[4], phk[5]};
        *(LAS u32x2*)(pkp + 24) = u32x2{phk[6], phk[7]}; *(LAS u32x2*)(pkp + 32) = u32x2{phk[8], 0u};
    }
    BAR();
    STAMP(5);
    if (wid < 6) {
        const int hh = wid / 3, mt = wid % 3, q4 = r16 >> 2, p4 = r16 & 3;
        int u = 4 * mt + p4; if (u > 9) u = 9;
        const lptr pa = lds + L_PK + hh * 10240 + (8 * g + q4) * 80 + u * 8;
        const lptr pb0 = lds + L_VA + hh * 6144 + (8 * g + q4) * 48 + p4 * 8;
        const lptr pb1 = lds + L_VA + hh * 6144 + (8 * g + q4) * 48 + 32;
        f32x4 d0 = {0, 0, 0, 0}, d1 = {0, 0, 0, 0};
#pragma unroll
        for (int ks = 0; ks < 4; ++ks) {
            const bf16x8 a = cat8(trrd(pa + ks * 32 * 80), trrd(pa + ks * 32 * 80 + 4 * 80));
            const bf16x8 b0 = cat8(trrd(pb0 + ks * 32 * 48), trrd(pb0 + ks * 32 * 48 + 4 * 48));
            const bf16x8 b1 = cat8(trrd(pb1 + ks * 32 * 48), trrd(pb1 + ks * 32 * 48 + 4 * 48));
            d0 = MFMA16(a, b0, d0); d1 = MFMA16(a, b1, d1);
        }
        const int uu = 4 * mt + g;
        const unsigned po = (unsigned)(WS_PART + (size_t)(hp * 16 + rt) * 6144 + hh * 3040);
        if (uu < 10) {
            st_sc1(wsr, po + (r16 * 10 + uu) * 16, __builtin_bit_cast(u32x4, d0));
            if (r16 < 3) st_sc1(wsr, po + ((16 + r16) * 10 + uu) * 16, __builtin_bit_cast(u32x4, d1));
        }
    }
    publish(F_P + (hp * 16 + rt) * 32, epoch, tid);
    STAMP(6);

    const int q32 = lane & 31, hh = lane >> 5;
    f32x16 oacc;
    {
        const lptr Kb = lds + L_K + h * 36864, Vb = Kb + 18432, Qb = lds + L_Q + h * 6144;
        const bf16x8 qf0 = ld128(Qb + (32 * tg + q32) * 48 + 16 * hh);
        const unsigned q16 = *(const LAS unsigned*)(Qb + (32 * tg + q32) * 48 + 32);
        const bf16x8 qf1 = mk8(hh ? 0u : q16, 0u, 0u, 0u);
        const lptr kp = Kb + (32 * tg + q32) * 48;
        const int i16 = lane & 15, dhalf = (lane >> 4) & 1;
        const lptr vp = Vb + (32 * tg + 4 * hh + (i16 >> 2)) * 48 + (dhalf ? 32 + ((i16 & 3) ? 8 : 0) : (i16 & 3) * 8);
        f32x16 negm;
#pragma unroll
        for (int r = 0; r < 16; ++r) { oacc[r] = 0.f; negm[r] = 0.f; }
        auto tile = [&](int KT, bool first, int mask) {
            const bf16x8 k0 = ld128(kp + KT * 32 * 48 + 16 * hh), k1 = ld128(kp + KT * 32 * 48 + 32);
            f32x16 s = MFMA32(k0, qf0, negm); s = MFMA32(k1, qf1, s);
            if (mask) {
#pragma unroll
                for (int r = 0; r < 16; ++r) { const int jj = (r & 3) + 8 * (r >> 2) + 4 * hh; const bool ok = mask == 1 ? jj <= q32 : jj > q32; s[r] = ok ? s[r] : -INFINITY; }
            }
            float m = fmaxf(fmaxf(s[0], s[1]), s[2]);
#pragma unroll
            for (int r = 3; r < 15; r += 2) m = fmaxf(fmaxf(m, s[r]), s[r + 1]);
            m = x32max(fmaxf(m, s[15]));
            if (first || __any(m > THR)) {
                const float dl = first ? m : fmaxf(m, 0.f);
                const float f = first ? 1.f : __builtin_amdgcn_exp2f(-dl);
#pragma unroll
                for (int r = 0; r < 16; ++r) { s[r] -= dl; negm[r] -= dl; oacc[r] *= f; }
            }
            unsigned pw[8];
#pragma unroll
            for (int r = 0; r < 8; ++r) pw[r] = cvtpk(__builtin_amdgcn_exp2f(s[2 * r]), __builtin_amdgcn_exp2f(s[2 * r + 1]));
#pragma unroll
            for (int s2 = 0; s2 < 2; ++s2) {
                const bf16x8 vf = cat8(trrd(vp + (KT * 32 + 16 * s2) * 48), trrd(vp + (KT * 32 + 16 * s2 + 8) * 48));
                oacc = MFMA32(vf, mk8(pw[4 * s2], pw[4 * s2 + 1], pw[4 * s2 + 2], pw[4 * s2 + 3]), oacc);
            }
        };
        tile(8, true, 1);
        for (int KT = 7; KT >= 8 - tg; --KT) tile(KT, false, 0);
        STAMP(7);
        if (rt > 0) {
            if (wid == 0) { gu32* fb = F_KV + (hp * 16 + (rt > 1 ? rt - 2 : rt - 1)) * 32; wait_flags(fb, rt > 1 ? 2 : 1, epoch, lane); }
            BAR();
            STAMP(8);
            u32x4 hv[6];
#pragma unroll
            for (int i = 0; i < 6; ++i) { const int pc = i * 8 + wid;
                const int tl = pc / 24, ar = (pc % 24) / 6, o = pc % 6; const int srt = rt - 2 + tl;
                hv[i] = (srt >= 0) ? ld_sc1(wsr, (unsigned)(WS_KV + (size_t)((srt * 16 + hp) * 4 + ar) * 6144 + o * 1024 + lane * 16)) : u32x4{0, 0, 0, 0}; }
#pragma unroll
            for (int i = 0; i < 6; ++i) { const int pc = i * 8 + wid; const int tl = pc / 24, ar = (pc % 24) / 6, o = pc % 6;
                *(LAS u32x4*)(lds + L_K + ar * 18432 + tl * 6144 + o * 1024 + lane * 16) = hv[i]; }
            BAR();
            STAMP(9);
            const int lo = rt >= 2 ? 0 : 4 - tg;
            for (int KT = 7 - tg; KT >= (lo > 1 ? lo : 1); --KT) tile(KT, false, 0);
            if (lo <= 0) tile(0, false, 2);
        }
    }
    STAMP(10);
    if (wid == 0) wait_flags(F_P + hp * 16 * 32, 16, epoch, lane);
    BAR();
    STAMP(11);
    if (tid < 380) {
        f32x4 s = {0, 0, 0, 0};
        u32x4 pv[16];
#pragma unroll
        for (int i = 0; i < 16; ++i) pv[i] = ld_sc1(wsr, (unsigned)(WS_PART + (size_t)(hp * 16 + i) * 6144 + tid * 16));
#pragma unroll
        for (int i = 0; i < 16; ++i) s += __builtin_bit_cast(f32x4, pv[i]);
        const int ph = tid / 190, e0 = tid % 190, d = e0 / 10, u = e0 % 10;
        *(LAS u32x2*)(lds + L_KVT + ph * 2016 + d * 96 + (u / 5) * 48 + (u % 5) * 8) = u32x2{cvtpk(s[0], s[1]), cvtpk(s[2], s[3])};
    } else if (tid < 380 + 2 * 40) {
        const int e0 = tid - 380, ph = e0 / 40, e1 = e0 % 40;
        if (e1 < 38) *(LAS u32x2*)(lds + L_KVT + ph * 2016 + (e1 >> 1) * 96 + (e1 & 1) * 48 + 40) = u32x2{0u, 0u};
    }
    if (tid >= 500) { const int e0 = tid - 500; *(LAS u32x4*)(lds + L_KVT + (e0 / 6) * 2016 + 20 * 96 + (e0 % 6) * 16) = u32x4{0, 0, 0, 0}; }
    BAR();
    STAMP(12);
    {
        f32x4 dd[3];
        const int eo = (r16 >> 2) & 1;
        const bool mine = (r16 >> 3) == (g >> 1);
        const bf16x8 b0 = mk8(phq[0], phq[1], phq[2], phq[3]), b1 = mk8(phq[4], phq[5], phq[6], phq[7]), b2 = mk8(phq[8], 0u, 0u, 0u);
#pragma unroll
        for (int i = 0; i < 3; ++i) {
            const int sg = 4 * i + (r16 & 3);
            int d = eo ? (sg < 8 ? 10 + sg : 20) : (sg < 10 ? sg : 20); if (sg == 10) d = 18; if (!mine) d = 20;
            const lptr ap = lds + L_KVT + h * 2016 + d * 96 + (g & 1) * 48;
            f32x4 a = MFMA16(ld128(ap), b0, (f32x4{0, 0, 0, 0})); a = MFMA16(ld128(ap + 16), b1, a); a = MFMA16(ld128(ap + 32), b2, a);
            dd[i] = a;
        }
        const int tok = 32 * tg + 16 * (g >> 1) + r16;
        const float gate = *(const LAS float*)(lds + L_GATE + tok * 4);
        const float sc = gate * __builtin_amdgcn_rcpf(dd[2][2] + 1e-6f);
        const lptr op = lds + L_OST + tok * 144 + 72 + h * 36 + (g & 1) * 20;
        *(LAS unsigned*)(op) = cvtpk(dd[0][0] * sc, dd[0][1] * sc); *(LAS unsigned*)(op + 4) = cvtpk(dd[0][2] * sc, dd[0][3] * sc);
        *(LAS unsigned*)(op + 8) = cvtpk(dd[1][0] * sc, dd[1][1] * sc); *(LAS unsigned*)(op + 12) = cvtpk(dd[1][2] * sc, dd[1][3] * sc);
        if ((g & 1) == 0) *(LAS unsigned*)(op + 16) = cvtpk(dd[2][0] * sc, dd[2][1] * sc);
        const int tokb = 32 * tg + q32;
        auto sw = __builtin_amdgcn_permlane32_swap(__float_as_uint(oacc[10]), __float_as_uint(oacc[10]), false, false);
        const float gb = *(const LAS float*)(lds + L_GATE + tokb * 4);
        const float sb = (1.f - gb) * __builtin_amdgcn_rcpf(__uint_as_float(sw[0]));
        const lptr ob = lds + L_OST + tokb * 144;
        *(LAS u32x2*)(ob + h * 32 + 8 * hh) = u32x2{cvtpk(oacc[0] * sb, oacc[1] * sb), cvtpk(oacc[2] * sb, oacc[3] * sb)};
        *(LAS u32x2*)(ob + h * 32 + 16 + 8 * hh) = u32x2{cvtpk(oacc[4] * sb, oacc[5] * sb), cvtpk(oacc[6] * sb, oacc[7] * sb)};
        if (hh == 0) *(LAS unsigned*)(ob + 64 + h * 4) = cvtpk(oacc[8] * sb, oacc[9] * sb);
    }
    BAR();
#pragma unroll
    for (int i = 0; i < 3; ++i) {
        const int e0 = i * 512 + tid;
        if (e0 < 1152) { const int R = e0 / 144, e1 = e0 % 144, c = e1 >> 4, tk = e1 & 15;
            const u32x4 v = *(const LAS u32x4*)(lds + L_OST + (16 * R + tk) * 144 + c * 16);
            st_sc1(wsr, (unsigned)(WS_O + ((size_t)(rt * 8 + R) * 144 + 9 * hp) * 256 + e1 * 16), v); }
    }
    STAMP(13);
    publish(F_O + (rt * 16 + hp) * 32, epoch, tid);
    STAMP(14);

    {
        const int cg = hp & 7, rh = hp >> 3, kh = wid >> 2, r = wid & 3;
        const char* wot = ws + WS_WOT + (size_t)cg * 165888;
        auto issue3 = [&](int s, int slot) {
            const unsigned p = wid * 1024 + lane * 16;
            const unsigned src = p < 4608 ? s * 4608 + p : 18 * 4608 + s * 4608 + (p - 4608);
            glds16(src, wot, lds0 + slot * G3_SLOT + wid * 1024);
            if (wid == 0) glds16(18 * 4608 + s * 4608 + (8192 - 4608) + lane * 16, wot, lds0 + slot * G3_SLOT + 8192);
        };
        for (int s = 0; s < G3_NSLOT; ++s) issue3(s, s);
        if (wid == 0) wait_flags(F_O + rt * 16 * 32, 16, epoch, lane);
        BAR();
        STAMP(15);
        u32x4 of[18];
        const unsigned ob = (unsigned)(WS_O + ((size_t)(rt * 8 + 4 * rh + r) * 144 + 72 * kh) * 256 + lane * 16);
#pragma unroll
        for (int s = 0; s < 18; ++s) of[s] = ld_sc1(wsr, ob + s * 1024);
        f32x4 a3[5];
#pragma unroll
        for (int t = 0; t < 5; ++t) a3[t] = f32x4{0, 0, 0, 0};
        WAITV(18);
        BAR();
        const int wro = kh * 4608 + lane * 16, wr4 = kh * 4608 + 4096 + g * 128 + (r16 & 7) * 16;
#pragma unroll
        for (int s = 0; s < 18; ++s) {
            if (s == 3) { BAR(); for (int s2 = G3_NSLOT; s2 < 18; ++s2) issue3(s2, s2 - G3_NSLOT); }
            if (s == G3_NSLOT) { WAITV(0); BAR(); }
            const lptr sp = lds + (s < G3_NSLOT ? s : s - G3_NSLOT) * G3_SLOT;
            const bf16x8 bo = __builtin_bit_cast(bf16x8, of[s]);
#pragma unroll
            for (int t = 0; t < 4; ++t) a3[t] = MFMA16(ld128(sp + wro + t * 1024), bo, a3[t]);
            a3[4] = MFMA16(ld128(sp + wr4), bo, a3[4]);
        }
        BAR();
        STAMP(16);
        const lptr rp = lds + kh * 19456 + (16 * r + r16) * 304 + g * 16;
#pragma unroll
        for (int t = 0; t < 4; ++t) *(LAS f32x4*)(rp + t * 64) = a3[t];
        if (g < 2) *(LAS f32x4*)(rp + 256) = a3[4];
        BAR();
        for (int e0 = tid; e0 < 576; e0 += 512) {
            const int tk = e0 / 9, c = e0 % 9;
            const lptr p0 = lds + tk * 304 + c * 32;
            const f32x4 u0 = *(const LAS f32x4*)(p0) + *(const LAS f32x4*)(p0 + 19456), u1 = *(const LAS f32x4*)(p0 + 16) + *(const LAS f32x4*)(p0 + 19456 + 16);
            const u32x4 v = {cvtpk(u0[0], u0[1]), cvtpk(u0[2], u0[3]), cvtpk(u1[0], u1[1]), cvtpk(u1[2], u1[3])};
            *(u32x4*)((char*)out + (size_t)(rt * 128 + 64 * rh + tk) * 1152 + cg * 144 + c * 16) = v;
        }
        STAMP(17);
    }
}

extern "C" void kernel_launch(void* const* d_in, const int* in_sizes, int n_in, void* d_out, int out_size, void* d_ws, size_t ws_size, hipStream_t stream) {
    const u16* x = (const u16*)d_in[0]; const u16* wA = (const u16*)d_in[1]; const u16* wphi = (const u16*)d_in[2]; const u16* woA = (const u16*)d_in[3];
    const u16* wB = (const u16*)d_in[5]; const u16* woB = (const u16*)d_in[6]; const u16* gw = (const u16*)d_in[7];
    constexpr int NTOT = 16 * 9 * 28 * 64 + 16 * 12 * 64 + 8 * 36 * 288 + 768 * 8;
    prep_kernel<<<(NTOT + 255) / 256, 256, 0, stream>>>(wA, wB, wphi, woA, woB, gw, (char*)d_ws);
    (void)hipFuncSetAttribute((const void*)main_kernel, hipFuncAttributeMaxDynamicSharedMemorySize, LDS_BYTES);
    main_kernel<<<256, 512, LDS_BYTES, stream>>>((const char*)x, (char*)d_ws, (u16*)d_out);
}
```

```cpp
#include <hip/hip_runtime.h>
#include <hip/hip_bf16.h>
#include <stdint.h>

typedef unsigned short u16;
typedef short bf16x8 __attribute__((ext_vector_type(8)));
typedef short s16x4 __attribute__((ext_vector_type(4)));
typedef float f32x4 __attribute__((ext_vector_type(4)));
typedef float f32x16 __attribute__((ext_vector_type(16)));
typedef unsigned u32x4 __attribute__((ext_vector_type(4)));
typedef unsigned u32x2 __attribute__((ext_vector_type(2)));
typedef __bf16 bf16x2_t __attribute__((ext_vector_type(2)));
typedef float f32x2 __attribute__((ext_vector_type(2)));
#define LAS __attribute__((address_space(3)))
#define GAS __attribute__((address_space(1)))
typedef LAS char* lptr;
typedef GAS unsigned gu32;

constexpr int DM = 576;
constexpr size_t WS_EPOCH = 0;
constexpr size_t WS_FLAG = 4096;
constexpr size_t WS_WT   = 4096 + 4 * 256 * 128;
constexpr size_t WS_WOT  = WS_WT + 16 * 9 * 28672;
constexpr size_t WS_KV   = WS_WOT + 8 * 165888;
constexpr size_t WS_PART = WS_KV + 256 * 24576;
constexpr size_t WS_O    = WS_PART + 256 * 6144;
constexpr size_t WS_DBG  = WS_O + 16 * 8 * 36864;

constexpr int STAGE = 16384 + 28672;
constexpr int L_WP = 3 * STAGE;
constexpr int L_WOP = L_WP + 19 * 1024;
constexpr int L_PHI = L_WOP + 6144;
constexpr int L_GW = L_PHI + 2048;
constexpr int L_K = 0;
constexpr int L_Q = 73728;
constexpr int L_VA = 86016;
constexpr int L_PK = 98304;
constexpr int L_GATE = 118784;
constexpr int L_KVT = 119296;
constexpr int L_OST = 123392;
constexpr int LDS_BYTES = 160 * 1024;
constexpr int G3_SLOT = 9216, G3_NSLOT = 15;
constexpr unsigned SPIN_CAP = 1u << 18;
constexpr float LOG2E = 1.4426950408889634f;
constexpr float QSCALE = 0.23570226039551584f * LOG2E;
constexpr float THR = 8.f;

__device__ __forceinline__ unsigned cvtpk(float lo, float hi) { const f32x2 v = {lo, hi}; return __builtin_bit_cast(unsigned, __builtin_convertvector(v, bf16x2_t)); }
__device__ __forceinline__ void glds16(unsigned voff, const void* sbase, unsigned lds_addr) {
    unsigned sv;
    asm volatile("s_mov_b32 %0, m0\n\ts_mov_b32 m0, %3\n\ts_nop 0\n\tglobal_load_lds_dwordx4 %1, %2\n\ts_mov_b32 m0, %0" : "=&s"(sv) : "v"(voff), "s"(sbase), "s"(lds_addr) : "memory");
}
__device__ __forceinline__ void glds16_dev(unsigned voff, const void* sbase, unsigned lds_addr) {
    unsigned sv;
    asm volatile("s_mov_b32 %0, m0\n\ts_mov_b32 m0, %3\n\ts_nop 0\n\tglobal_load_lds_dwordx4 %1, %2 sc1\n\ts_mov_b32 m0, %0" : "=&s"(sv) : "v"(voff), "s"(sbase), "s"(lds_addr) : "memory");
}
__device__ __forceinline__ void glds16_ptr(const void* g, unsigned lds_addr) {
    unsigned sv;
    asm volatile("s_mov_b32 %0, m0\n\ts_mov_b32 m0, %2\n\ts_nop 0\n\tglobal_load_lds_dwordx4 %1, off\n\ts_mov_b32 m0, %0" : "=&s"(sv) : "v"(g), "s"(lds_addr) : "memory");
}
#define WAITV(n) asm volatile("s_waitcnt vmcnt(" #n ")" ::: "memory")
#define WAITL() asm volatile("s_waitcnt lgkmcnt(0)" ::: "memory")
#define BAR() asm volatile("s_waitcnt lgkmcnt(0)\n\ts_barrier" ::: "memory")
#define DRAIN_BAR() asm volatile("s_waitcnt vmcnt(0) lgkmcnt(0)\n\ts_barrier" ::: "memory")
#define SB() __builtin_amdgcn_sched_barrier(0)
#define MFMA16(a, b, c) __builtin_amdgcn_mfma_f32_16x16x32_bf16(a, b, c, 0, 0, 0)
#define MFMA32(a, b, c) __builtin_amdgcn_mfma_f32_32x32x16_bf16(a, b, c, 0, 0, 0)
__device__ __forceinline__ bf16x8 ld128(lptr p) { return *(const LAS bf16x8*)p; }
__device__ __forceinline__ s16x4 trrd(lptr p) { typedef short v4i16_t __attribute__((ext_vector_type(4))); return __builtin_bit_cast(s16x4, __builtin_amdgcn_ds_read_tr16_b64_v4i16((LAS v4i16_t*)p)); }
__device__ __forceinline__ bf16x8 cat8(s16x4 a, s16x4 b) { return __builtin_shufflevector(a, b, 0, 1, 2, 3, 4, 5, 6, 7); }
__device__ __forceinline__ bf16x8 mk8(unsigned a, unsigned b, unsigned c, unsigned d) { u32x4 w = {a, b, c, d}; return __builtin_bit_cast(bf16x8, w); }
__device__ __forceinline__ float x16max(float x) { auto s = __builtin_amdgcn_permlane16_swap(__float_as_uint(x), __float_as_uint(x), false, false); return fmaxf(__uint_as_float(s[0]), __uint_as_float(s[1])); }
__device__ __forceinline__ float x16sum(float x) { auto s = __builtin_amdgcn_permlane16_swap(__float_as_uint(x), __float_as_uint(x), false, false); return __uint_as_float(s[0]) + __uint_as_float(s[1]); }
__device__ __forceinline__ float x32max(float x) { auto s = __builtin_amdgcn_permlane32_swap(__float_as_uint(x), __float_as_uint(x), false, false); return fmaxf(__uint_as_float(s[0]), __uint_as_float(s[1])); }
__device__ __forceinline__ void st_dev(const __amdgpu_buffer_rsrc_t rs, unsigned off, u32x4 v) { __builtin_amdgcn_raw_buffer_store_b128(v, rs, off, 0, 16); }
__device__ __forceinline__ u32x4 ld_dev(const __amdgpu_buffer_rsrc_t rs, unsigned off) { return __builtin_amdgcn_raw_buffer_load_b128(rs, off, 0, 16); }
__device__ __forceinline__ void wait_flag(gu32* p, unsigned tag) {
    for (unsigned spins = 0; spins < SPIN_CAP; ++spins) {
        const unsigned v = __hip_atomic_load(p, __ATOMIC_RELAXED, __HIP_MEMORY_SCOPE_AGENT);
        if (__all(v == tag)) break;
        __builtin_amdgcn_s_sleep(1);
    }
}
__device__ __forceinline__ void set_flag(gu32* p, unsigned tag) { __hip_atomic_store(p, tag, __ATOMIC_RELAXED, __HIP_MEMORY_SCOPE_AGENT); }
__device__ __forceinline__ u32x4 gather8(lptr p, int stride) {
    unsigned w[4];
#pragma unroll
    for (int j = 0; j < 4; ++j) w[j] = (unsigned)*(const LAS u16*)(p + 2 * j * stride) | ((unsigned)*(const LAS u16*)(p + (2 * j + 1) * stride) << 16);
    return u32x4{w[0], w[1], w[2], w[3]};
}
__device__ __forceinline__ int piece_start(int j) { return (9 * j + 1) >> 1; }

#define STAMP(i) do {} while (0)

__device__ __forceinline__ void feature_map(const float (&p)[9], unsigned (&o)[9]) {
    float mx = fmaxf(fmaxf(p[0], p[1]), p[2]), mn = fminf(fminf(p[0], p[1]), p[2]);
#pragma unroll
    for (int i = 3; i < 9; i += 2) { mx = fmaxf(fmaxf(mx, p[i]), p[i + 1]); mn = fminf(fminf(mn, p[i]), p[i + 1]); }
    mx = x16max(mx); mn = -x16max(-mn);
    const float mxl = -mx * LOG2E, mnl = mn * LOG2E;
    float a[9], b[9], sa = 0.f, sb = 0.f;
#pragma unroll
    for (int i = 0; i < 9; ++i) { a[i] = __builtin_amdgcn_exp2f(__builtin_fmaf(p[i], LOG2E, mxl)); b[i] = __builtin_amdgcn_exp2f(__builtin_fmaf(p[i], -LOG2E, mnl)); sa += a[i]; sb += b[i]; }
    sa = x16sum(sa); sb = x16sum(sb);
    const float ra = __builtin_amdgcn_rcpf(sa), rb = __builtin_amdgcn_rcpf(sb);
#pragma unroll
    for (int i = 0; i < 9; ++i) { a[i] *= ra; b[i] *= rb; }
    o[0] = cvtpk(a[0], a[1]); o[1] = cvtpk(a[2], a[3]); o[2] = cvtpk(a[4], a[5]); o[3] = cvtpk(a[6], a[7]); o[4] = cvtpk(a[8], b[0]);
    o[5] = cvtpk(b[1], b[2]); o[6] = cvtpk(b[3], b[4]); o[7] = cvtpk(b[5], b[6]); o[8] = cvtpk(b[7], b[8]);
}

__global__ __launch_bounds__(512, 2) void fused_kernel(const char* __restrict__ x, const char* __restrict__ wA, const char* __restrict__ wB, const char* __restrict__ wphi,
                                                       const char* __restrict__ woA, const char* __restrict__ woB, const char* __restrict__ gw,
                                                       char* __restrict__ ws, u16* __restrict__ out) {
    extern __shared__ __attribute__((aligned(16))) char lds_raw[];
    const lptr lds = (lptr)lds_raw;
    const unsigned lds0 = (unsigned)(uintptr_t)lds_raw;
    const int tid = threadIdx.x, lane = tid & 63, wid = __builtin_amdgcn_readfirstlane(tid >> 6);
    const int b = blockIdx.x, hp = (b & 7) * 2 + ((b >> 3) & 1), rt = b >> 4;
    const int h = wid >> 2, tg = h ? 3 - (wid & 3) : (wid & 3);
    const int r16 = lane & 15, g = lane >> 4;
    gu32* flags = (gu32*)(ws + WS_FLAG);
    gu32* F_W = flags;
    gu32* F_KV = flags + 256 * 32;
    gu32* F_P = flags + 512 * 32;
    gu32* F_O = flags + 768 * 32;
    const unsigned tag = __builtin_amdgcn_readfirstlane(__hip_atomic_load((gu32*)(ws + WS_EPOCH), __ATOMIC_RELAXED, __HIP_MEMORY_SCOPE_AGENT)) + 1u;
    const __amdgpu_buffer_rsrc_t wsr = __builtin_amdgcn_make_buffer_rsrc(ws, 0, 0x2000000, 0x00020000);
    STAMP(0);

    const int G0 = piece_start(rt), nG = piece_start(rt + 1) - G0;
    const int j2 = b >> 3, cgp = b & 7, C0 = piece_start(j2), nC = piece_start(j2 + 1) - C0;
    unsigned xo[2];
    {
        const int nch = nG * 240;
#pragma unroll
        for (int i = 0; i < 3; ++i) {
            const int pc = wid + 8 * i;
            if (pc < 19) { int c = pc * 64 + lane; if (c >= nch) c = 0;
                const int row = c / 30, ch = c % 30, sg = ch / 5, cc = ch % 5;
                const char* src = (sg < 3 ? wA + sg * 1152 : wB + (sg - 3) * 1152) + (size_t)(8 * G0 + row) * 3456 + ((72 * hp) & ~15) + cc * 16;
                glds16_ptr(src, lds0 + L_WP + pc * 1024); }
        }
        if (wid < 6) { int c = wid * 64 + lane; if (c >= nC * 72) c = 0;
            const int row = c / 9, cc = c % 9, C = C0 + (row >> 3), hq = C / 9, p = 8 * (C % 9) + (row & 7);
            int br, head, d;
            if (p < 32) { br = 1; head = p >> 4; d = p & 15; } else if (p < 36) { br = 1; head = (p >> 1) & 1; d = 16 + (p & 1); } else { br = 0; head = p >= 54; d = p - 36 - 18 * head; }
            glds16_ptr((br ? woB : woA) + ((2 * hq + head) * 18 + d) * 1152 + cgp * 144 + cc * 16, lds0 + L_WOP + wid * 1024);
        } else if (wid == 6) { glds16(hp * 1296 + lane * 16, wphi, lds0 + L_PHI); }
        else { glds16(hp * 1296 + 1024 + (lane < 17 ? lane : 0) * 16, wphi, lds0 + L_PHI + 1024); glds16(G0 * 16 + (lane < nG ? lane : 0) * 16, gw, lds0 + L_GW); }
#pragma unroll
        for (int i = 0; i < 2; ++i) { const int row = 16 * wid + 8 * i + (lane >> 3); xo[i] = (unsigned)(rt * 128 + row) * 1152u + (unsigned)(((lane & 7) ^ ((row >> 1) & 7)) << 4); }
#pragma unroll
        for (int s = 0; s < 3; ++s) { glds16(xo[0], x + s * 128, lds0 + s * STAGE + wid * 2048); glds16(xo[1], x + s * 128, lds0 + s * STAGE + wid * 2048 + 1024); }
        WAITV(6);
        BAR();
        STAMP(1);
        const int dlt = 8 * (hp & 1);
#pragma unroll
        for (int i = 0; i < 3; ++i) {
            const int e = i * 512 + tid;
            if (e < nG * 224) {
                const int r = e & 15, q = e >> 4, Gi = q / 14, ht = q % 14, hh2 = ht / 7, t = ht % 7;
                int sg, d;
                if (t < 4) { sg = t == 0 ? 2 : t + 2; d = r; } else if (t == 4) { sg = 0; d = r; } else if (t == 5) { sg = r < 2 ? 0 : 1; d = r < 2 ? 16 + r : r - 2; }
                else { sg = r < 4 ? 1 : (r >> 1); d = r < 4 ? 14 + r : 16 + (r & 1); }
                u32x4 v;
                if (t == 6 && r >= 12) { v = u32x4{0, 0, 0, 0}; if (r == 12) v = *(const LAS u32x4*)(lds + L_GW + Gi * 16); }
                else v = gather8(lds + L_WP + Gi * 8 * 480 + sg * 80 + dlt + hh2 * 36 + d * 2, 480);
                const int G = G0 + Gi;
                st_dev(wsr, (unsigned)(WS_WT + (size_t)hp * 9 * 28672) + (G >> 3) * 28672 + (ht * 2 + ((G >> 2) & 1)) * 1024 + ((G & 3) * 16 + r) * 16, v);
            }
        }
        if (tid < nC * 72) {
            const int Ci = tid / 72, col = tid % 72, C = C0 + Ci, kh = C / 72, s = (C % 72) >> 2, gg = C & 3, t = col >> 4, r = col & 15;
            const u32x4 v = gather8(lds + L_WOP + Ci * 8 * 144 + col * 2, 144);
            st_dev(wsr, (unsigned)(WS_WOT + (size_t)cgp * 165888) + (kh * 18 + s) * 4608 + (t < 4 ? t * 1024 + (gg * 16 + r) * 16 : 4096 + (gg * 8 + r) * 16), v);
        }
        DRAIN_BAR();
        if (tid == 0) set_flag(F_W + (hp * 16 + rt) * 32, tag);
        STAMP(2);
#pragma unroll
        for (int i = 0; i < 2; ++i) {
            const int e = i * 512 + tid;
            if (e < 768) {
                const int ln = e & 63, f = e >> 6, ii = f % 3, X = (f / 3) & 1, hh2 = f / 6, r = ln & 15, gg = ln >> 4, sgm = 4 * ii + (r & 3), ff = 9 * ((r >> 2) & 1) + sgm;
                unsigned w[4];
#pragma unroll
                for (int jp = 0; jp < 4; ++jp) { unsigned pr = 0;
#pragma unroll
                    for (int u = 0; u < 2; ++u) { const int j = 2 * jp + u, rho = 4 * gg + (j & 3); int d;
                        if (X == 0) d = j < 4 ? rho : (rho < 2 ? 16 + rho : -1); else d = j < 4 ? (rho >= 2 ? rho - 2 : -1) : (rho < 4 ? 14 + rho : -1);
                        const unsigned v = (d >= 0 && sgm < 9) ? *(const LAS u16*)(lds + L_PHI + hh2 * 648 + (d * 18 + ff) * 2) : 0u;
                        pr |= v << (16 * u); }
                    w[jp] = pr; }
                *(LAS u32x4*)(lds + L_WP + e * 16) = u32x4{w[0], w[1], w[2], w[3]};
            }
        }
    }
    if (wid == 0) wait_flag(F_W + (hp * 16 + r16) * 32, tag);
    BAR();
    STAMP(3);

    f32x4 acc[7][2];
#pragma unroll
    for (int t = 0; t < 7; ++t) { acc[t][0] = f32x4{0, 0, 0, 0}; acc[t][1] = f32x4{0, 0, 0, 0}; }
    {
        const char* wt = ws + WS_WT + (size_t)hp * 9 * 28672;
        const unsigned wo = (unsigned)(wid * 1024 + lane * 16);
        auto issue_w = [&](int kb, unsigned sb) {
            const char* wk = wt + kb * 28672;
            glds16_dev(wo, wk, sb + 16384 + wid * 1024); glds16_dev(wo, wk + 8192, sb + 16384 + 8192 + wid * 1024); glds16_dev(wo, wk + 16384, sb + 16384 + 16384 + wid * 1024);
            if (wid < 4) glds16_dev(wo, wk + 24576, sb + 16384 + 24576 + wid * 1024);
        };
        issue_w(0, lds0); issue_w(1, lds0 + STAGE); issue_w(2, lds0 + 2 * STAGE);
        const int xr = (32 * tg + r16) * 128 + ((g ^ (r16 >> 1)) << 4);
        const int wr = 16384 + h * 14336 + lane * 16;
        WAITV(6);
        BAR();
        STAMP(4);
        bf16x8 wa[7], xa[2], wb[7], xb[2];
        xa[0] = ld128(lds + xr); xa[1] = ld128(lds + xr + 2048);
#pragma unroll
        for (int t = 0; t < 7; ++t) wa[t] = ld128(lds + wr + t * 2048);
        int slot = 0;
        for (int kb = 0; kb < 9; ++kb) {
            const lptr sb = lds + slot * STAGE;
            SB();
            xb[0] = ld128(sb + (xr ^ 64)); xb[1] = ld128(sb + (xr ^ 64) + 2048);
#pragma unroll
            for (int t = 0; t < 7; ++t) { wb[t] = ld128(sb + wr + t * 2048 + 1024); SB(); acc[t][0] = MFMA16(wa[t], xa[0], acc[t][0]); acc[t][1] = MFMA16(wa[t], xa[1], acc[t][1]); SB(); }
            if (kb == 0) WAITV(3); else if (kb < 7) WAITV(5); else WAITV(0);
            BAR();
            if (kb < 6) {
                const unsigned sa = lds0 + slot * STAGE;
                glds16(xo[0], x + (kb + 3) * 128, sa + wid * 2048); glds16(xo[1], x + (kb + 3) * 128, sa + wid * 2048 + 1024);
                issue_w(kb + 3, sa);
            }
            slot = slot == 2 ? 0 : slot + 1;
            const lptr sn = lds + slot * STAGE;
            SB();
            if (kb < 8) {
                xa[0] = ld128(sn + xr); xa[1] = ld128(sn + xr + 2048);
#pragma unroll
                for (int t = 0; t < 7; ++t) { const bf16x8 nw = ld128(sn + wr + t * 2048); SB(); acc[t][0] = MFMA16(wb[t], xb[0], acc[t][0]); acc[t][1] = MFMA16(wb[t], xb[1], acc[t][1]); SB(); wa[t] = nw; }
            } else {
#pragma unroll
                for (int t = 0; t < 7; ++t) { acc[t][0] = MFMA16(wb[t], xb[0], acc[t][0]); acc[t][1] = MFMA16(wb[t], xb[1], acc[t][1]); }
            }
        }
    }
    STAMP(5);
    unsigned pk45[2][3][2];
    {
        const lptr Kb = lds + L_K + h * 36864, Vb = Kb + 18432, Qb = lds + L_Q + h * 6144, VAb = lds + L_VA + h * 6144;
#pragma unroll
        for (int tt = 0; tt < 2; ++tt) {
            const int tok = 32 * tg + 16 * tt + r16;
            *(LAS u32x2*)(Kb + (256 + tok) * 48 + 8 * g) = u32x2{cvtpk(acc[2][tt][0], acc[2][tt][1]), cvtpk(acc[2][tt][2], acc[2][tt][3])};
            *(LAS u32x2*)(Vb + (256 + tok) * 48 + 8 * g) = u32x2{cvtpk(acc[3][tt][0], acc[3][tt][1]), cvtpk(acc[3][tt][2], acc[3][tt][3])};
#pragma unroll
            for (int t = 0; t < 3; ++t) { pk45[tt][t][0] = cvtpk(acc[4 + t][tt][0], acc[4 + t][tt][1]); pk45[tt][t][1] = cvtpk(acc[4 + t][tt][2], acc[4 + t][tt][3]); }
            if (g == 2) {
                *(LAS u32x4*)(Kb + (256 + tok) * 48 + 32) = u32x4{pk45[tt][2][0], 0u, 0u, 0u};
                *(LAS u32x4*)(Vb + (256 + tok) * 48 + 32) = u32x4{pk45[tt][2][1], 0x00003f80u, 0u, 0u};
            }
        }
        WAITL();
        const unsigned kvo = (unsigned)(WS_KV + (size_t)((rt * 16 + hp) * 4 + h * 2) * 6144 + tg * 1536);
        const u32x4 k0 = *(const LAS u32x4*)(Kb + (256 + 32 * tg) * 48 + lane * 16);
        const u32x4 v0 = *(const LAS u32x4*)(Vb + (256 + 32 * tg) * 48 + lane * 16);
        const u32x4 k1 = *(const LAS u32x4*)(Kb + (256 + 32 * tg) * 48 + 1024 + (lane & 31) * 16);
        const u32x4 v1 = *(const LAS u32x4*)(Vb + (256 + 32 * tg) * 48 + 1024 + (lane & 31) * 16);
#pragma unroll
        for (int tt = 0; tt < 2; ++tt) {
            const int tok = 32 * tg + 16 * tt + r16;
            *(LAS u32x2*)(VAb + tok * 48 + 8 * g) = u32x2{cvtpk(acc[0][tt][0], acc[0][tt][1]), cvtpk(acc[0][tt][2], acc[0][tt][3])};
            *(LAS u32x2*)(Qb + tok * 48 + 8 * g) = u32x2{cvtpk(acc[1][tt][0] * QSCALE, acc[1][tt][1] * QSCALE), cvtpk(acc[1][tt][2] * QSCALE, acc[1][tt][3] * QSCALE)};
            if (g == 1) {
                *(LAS u32x2*)(VAb + tok * 48 + 32) = u32x2{pk45[tt][2][0], 0x00003f80u};
                *(LAS unsigned*)(Qb + tok * 48 + 32) = cvtpk(acc[6][tt][2] * QSCALE, acc[6][tt][3] * QSCALE);
            } else if (g == 3 && h == 0) {
                *(LAS float*)(lds + L_GATE + tok * 4) = __builtin_amdgcn_rcpf(1.f + __builtin_amdgcn_exp2f(-acc[6][tt][0] * LOG2E));
            }
        }
        st_dev(wsr, kvo + lane * 16, k0); st_dev(wsr, kvo + 6144 + lane * 16, v0);
        if (lane < 32) st_dev(wsr, kvo + 1024 + lane * 16, k1); else st_dev(wsr, kvo + 6144 + 1024 + (lane & 31) * 16, v1);
    }
    STAMP(6);
    unsigned phq[9];
    {
        const bool lo = r16 < 8;
        unsigned phk[9];
        float p[2][9];
#pragma unroll
        for (int X = 0; X < 2; ++X) {
            const bf16x8 bA = mk8(pk45[0][X][0], pk45[0][X][1], pk45[0][X + 1][0], pk45[0][X + 1][1]);
            const bf16x8 bB = mk8(pk45[1][X][0], pk45[1][X][1], pk45[1][X + 1][0], pk45[1][X + 1][1]);
#pragma unroll
            for (int i = 0; i < 3; ++i) {
                const u32x4 F = *(const LAS u32x4*)(lds + L_WP + ((h * 2 + X) * 3 + i) * 1024 + lane * 16);
                const bf16x8 Flo = mk8(lo ? F[0] : 0u, lo ? F[1] : 0u, lo ? F[2] : 0u, lo ? F[3] : 0u), Fhi = mk8(lo ? 0u : F[0], lo ? 0u : F[1], lo ? 0u : F[2], lo ? 0u : F[3]);
                f32x4 d = MFMA16(Flo, bA, (f32x4{0, 0, 0, 0})); d = MFMA16(Fhi, bB, d);
                p[X][4 * i] = d[0]; if (i < 2) { p[X][4 * i + 1] = d[1]; p[X][4 * i + 2] = d[2]; p[X][4 * i + 3] = d[3]; }
            }
        }
        feature_map(p[1], phk);
        const lptr pkp = lds + L_PK + h * 10240 + (32 * tg + 16 * (g >> 1) + r16) * 80 + (g & 1) * 40;
        *(LAS u32x2*)(pkp) = u32x2{phk[0], phk[1]}; *(LAS u32x2*)(pkp + 8) = u32x2{phk[2], phk[3]}; *(LAS u32x2*)(pkp + 16) = u32x2{phk[4], phk[5]};
        *(LAS u32x2*)(pkp + 24) = u32x2{phk[6], phk[7]}; *(LAS u32x2*)(pkp + 32) = u32x2{phk[8], 0u};
        feature_map(p[0], phq);
    }
    DRAIN_BAR();
    if (tid == 0) set_flag(F_KV + (hp * 16 + rt) * 32, tag);
    STAMP(7);
    if (wid != 3 && wid != 4) {
        const int w6 = wid < 3 ? wid : wid - 2, hh = w6 / 3, mt = w6 % 3, q4 = r16 >> 2, p4 = r16 & 3;
        int u = 4 * mt + p4; if (u > 9) u = 9;
        const lptr pa = lds + L_PK + hh * 10240 + (8 * g + q4) * 80 + u * 8;
        const lptr pb0 = lds + L_VA + hh * 6144 + (8 * g + q4) * 48 + p4 * 8;
        const lptr pb1 = lds + L_VA + hh * 6144 + (8 * g + q4) * 48 + 32;
        f32x4 d0 = {0, 0, 0, 0}, d1 = {0, 0, 0, 0};
        bf16x8 a[4], b0[4], b1[4];
#pragma unroll
        for (int ks = 0; ks < 4; ++ks) {
            a[ks] = cat8(trrd(pa + ks * 32 * 80), trrd(pa + ks * 32 * 80 + 4 * 80));
            b0[ks] = cat8(trrd(pb0 + ks * 32 * 48), trrd(pb0 + ks * 32 * 48 + 4 * 48));
            b1[ks] = cat8(trrd(pb1 + ks * 32 * 48), trrd(pb1 + ks * 32 * 48 + 4 * 48));
        }
        SB();
#pragma unroll
        for (int ks = 0; ks < 4; ++ks) { d0 = MFMA16(a[ks], b0[ks], d0); d1 = MFMA16(a[ks], b1[ks], d1); }
        const int uu = 4 * mt + g;
        const unsigned po = (unsigned)(WS_PART + (size_t)(hp * 16 + rt) * 6144 + hh * 3040);
        if (uu < 10) {
            st_dev(wsr, po + (r16 * 10 + uu) * 16, __builtin_bit_cast(u32x4, d0));
            if (r16 < 3) st_dev(wsr, po + ((16 + r16) * 10 + uu) * 16, __builtin_bit_cast(u32x4, d1));
        }
    }
    const int q32 = lane & 31, hh = lane >> 5;
    f32x16 oacc;
    u32x4 pv[16];
    {
        const lptr Kb = lds + L_K + h * 36864, Vb = Kb + 18432, Qb = lds + L_Q + h * 6144;
        const bf16x8 qf0 = ld128(Qb + (32 * tg + q32) * 48 + 16 * hh);
        const unsigned q16 = *(const LAS unsigned*)(Qb + (32 * tg + q32) * 48 + 32);
        const bf16x8 qf1 = mk8(hh ? 0u : q16, 0u, 0u, 0u);
        const lptr kp = Kb + (32 * tg + q32) * 48;
        const int i16 = lane & 15, dhalf = (lane >> 4) & 1;
        const lptr vp = Vb + (32 * tg + 4 * hh + (i16 >> 2)) * 48 + (dhalf ? 32 + ((i16 & 3) ? 8 : 0) : (i16 & 3) * 8);
        f32x16 negm;
#pragma unroll
        for (int r = 0; r < 16; ++r) { oacc[r] = 0.f; negm[r] = 0.f; }
        bf16x8 k0, k1;
        auto tile = [&](int KT, int KN, bool first, int mask) {
            SB();
            f32x16 s = MFMA32(k0, qf0, negm); s = MFMA32(k1, qf1, s);
            SB();
            const s16x4 va = trrd(vp + (KT * 32) * 48), vb = trrd(vp + (KT * 32 + 8) * 48), vc = trrd(vp + (KT * 32 + 16) * 48), vd = trrd(vp + (KT * 32 + 24) * 48);
            k0 = ld128(kp + KN * 32 * 48 + 16 * hh); k1 = ld128(kp + KN * 32 * 48 + 32);
            SB();
            if (mask) {
#pragma unroll
                for (int r = 0; r < 16; ++r) { const int jj = (r & 3) + 8 * (r >> 2) + 4 * hh; const bool ok = mask == 1 ? jj <= q32 : jj > q32; s[r] = ok ? s[r] : -INFINITY; }
            }
            float m = fmaxf(fmaxf(s[0], s[1]), s[2]);
#pragma unroll
            for (int r = 3; r < 15; r += 2) m = fmaxf(fmaxf(m, s[r]), s[r + 1]);
            m = x32max(fmaxf(m, s[15]));
            if (first || __any(m > THR)) {
                const float dl = first ? m : fmaxf(m, 0.f);
                const float f = first ? 1.f : __builtin_amdgcn_exp2f(-dl);
#pragma unroll
                for (int r = 0; r < 16; ++r) { s[r] -= dl; negm[r] -= dl; if (!first) oacc[r] *= f; }
            }
            unsigned pw[8];
#pragma unroll
            for (int r = 0; r < 8; ++r) pw[r] = cvtpk(__builtin_amdgcn_exp2f(s[2 * r]), __builtin_amdgcn_exp2f(s[2 * r + 1]));
            oacc = MFMA32(cat8(va, vb), mk8(pw[0], pw[1], pw[2], pw[3]), oacc);
            oacc = MFMA32(cat8(vc, vd), mk8(pw[4], pw[5], pw[6], pw[7]), oacc);
        };
        k0 = ld128(kp + 8 * 32 * 48 + 16 * hh); k1 = ld128(kp + 8 * 32 * 48 + 32);
        tile(8, 7, true, 1);
        for (int KT = 7; KT >= 8 - tg; --KT) tile(KT, KT - 1, false, 0);
        STAMP(8);
        if (wid == 0) {
            const int cg = hp & 7;
            gu32* fp = lane < 32 ? F_W + ((2 * cg + (lane & 1)) * 16 + (lane >> 1)) * 32 : F_KV + (hp * 16 + (rt > 1 ? rt - 2 + (lane & 1) : 0)) * 32;
            if (lane >= 32 && rt == 0) fp = F_W + (hp * 16 + rt) * 32;
            wait_flag(fp, tag);
        }
        DRAIN_BAR();
        if (tid == 0) set_flag(F_P + (hp * 16 + rt) * 32, tag);
        STAMP(9);
        if (rt > 0) {
            u32x4 hv[6];
#pragma unroll
            for (int i = 0; i < 6; ++i) { const int pc = i * 8 + wid;
                const int tl = pc / 24, ar = (pc % 24) / 6, o = pc % 6; const int srt = rt - 2 + tl;
                hv[i] = (srt >= 0) ? ld_dev(wsr, (unsigned)(WS_KV + (size_t)((srt * 16 + hp) * 4 + ar) * 6144 + o * 1024 + lane * 16)) : u32x4{0, 0, 0, 0}; }
#pragma unroll
            for (int i = 0; i < 6; ++i) { const int pc = i * 8 + wid; const int tl = pc / 24, ar = (pc % 24) / 6, o = pc % 6;
                *(LAS u32x4*)(lds + L_K + ar * 18432 + tl * 6144 + o * 1024 + lane * 16) = hv[i]; }
            BAR();
            STAMP(10);
            const int lo = rt >= 2 ? 0 : 4 - tg;
            const int last = lo > 1 ? lo : 1;
            { const int KT = 7 - tg; k0 = ld128(kp + KT * 32 * 48 + 16 * hh); k1 = ld128(kp + KT * 32 * 48 + 32); }
            for (int KT = 7 - tg; KT >= last; --KT) tile(KT, KT > 0 ? KT - 1 : 0, false, 0);
            if (lo <= 0) tile(0, 0, false, 2);
        }
    }
    STAMP(11);
    if (wid == 0) wait_flag(F_P + (hp * 16 + r16) * 32, tag);
    BAR();
    STAMP(12);
    if (tid < 380) {
        f32x4 s = {0, 0, 0, 0};
#pragma unroll
        for (int i = 0; i < 16; ++i) pv[i] = ld_dev(wsr, (unsigned)(WS_PART + (size_t)(hp * 16 + i) * 6144 + tid * 16));
#pragma unroll
        for (int i = 0; i < 16; ++i) s += __builtin_bit_cast(f32x4, pv[i]);
        const int ph = tid / 190, e0 = tid % 190, d = e0 / 10, u = e0 % 10;
        *(LAS u32x2*)(lds + L_KVT + ph * 2016 + d * 96 + (u / 5) * 48 + (u % 5) * 8) = u32x2{cvtpk(s[0], s[1]), cvtpk(s[2], s[3])};
    } else if (tid < 380 + 2 * 40) {
        const int e0 = tid - 380, ph = e0 / 40, e1 = e0 % 40;
        if (e1 < 38) *(LAS u32x2*)(lds + L_KVT + ph * 2016 + (e1 >> 1) * 96 + (e1 & 1) * 48 + 40) = u32x2{0u, 0u};
    }
    if (tid >= 500) { const int e0 = tid - 500; *(LAS u32x4*)(lds + L_KVT + (e0 / 6) * 2016 + 20 * 96 + (e0 % 6) * 16) = u32x4{0, 0, 0, 0}; }
    BAR();
    STAMP(13);
    {
        f32x4 dd[3];
        const int eo = (r16 >> 2) & 1;
        const bool mine = (r16 >> 3) == (g >> 1);
        const bf16x8 b0 = mk8(phq[0], phq[1], phq[2], phq[3]), b1 = mk8(phq[4], phq[5], phq[6], phq[7]), b2 = mk8(phq[8], 0u, 0u, 0u);
        bf16x8 af[3][3];
#pragma unroll
        for (int i = 0; i < 3; ++i) {
            const int sg = 4 * i + (r16 & 3);
            int d = eo ? (sg < 8 ? 10 + sg : 20) : (sg < 10 ? sg : 20); if (sg == 10) d = 18; if (!mine) d = 20;
            const lptr ap = lds + L_KVT + h * 2016 + d * 96 + (g & 1) * 48;
            af[i][0] = ld128(ap); af[i][1] = ld128(ap + 16); af[i][2] = ld128(ap + 32);
        }
        const int tok = 32 * tg + 16 * (g >> 1) + r16, tokb = 32 * tg + q32;
        const float gate = *(const LAS float*)(lds + L_GATE + tok * 4), gb = *(const LAS float*)(lds + L_GATE + tokb * 4);
        SB();
#pragma unroll
        for (int i = 0; i < 3; ++i) { f32x4 a = MFMA16(af[i][0], b0, (f32x4{0, 0, 0, 0})); a = MFMA16(af[i][1], b1, a); dd[i] = MFMA16(af[i][2], b2, a); }
        auto sw = __builtin_amdgcn_permlane32_swap(__float_as_uint(oacc[10]), __float_as_uint(oacc[10]), false, false);
        const float sb = (1.f - gb) * __builtin_amdgcn_rcpf(__uint_as_float(sw[0]));
        const lptr ob = lds + L_OST + tokb * 144;
        *(LAS u32x2*)(ob + h * 32 + 8 * hh) = u32x2{cvtpk(oacc[0] * sb, oacc[1] * sb), cvtpk(oacc[2] * sb, oacc[3] * sb)};
        *(LAS u32x2*)(ob + h * 32 + 16 + 8 * hh) = u32x2{cvtpk(oacc[4] * sb, oacc[5] * sb), cvtpk(oacc[6] * sb, oacc[7] * sb)};
        if (hh == 0) *(LAS unsigned*)(ob + 64 + h * 4) = cvtpk(oacc[8] * sb, oacc[9] * sb);
        const float sc = gate * __builtin_amdgcn_rcpf(dd[2][2] + 1e-6f);
        const lptr op = lds + L_OST + tok * 144 + 72 + h * 36 + (g & 1) * 20;
        *(LAS unsigned*)(op) = cvtpk(dd[0][0] * sc, dd[0][1] * sc); *(LAS unsigned*)(op + 4) = cvtpk(dd[0][2] * sc, dd[0][3] * sc);
        *(LAS unsigned*)(op + 8) = cvtpk(dd[1][0] * sc, dd[1][1] * sc); *(LAS unsigned*)(op + 12) = cvtpk(dd[1][2] * sc, dd[1][3] * sc);
        if ((g & 1) == 0) *(LAS unsigned*)(op + 16) = cvtpk(dd[2][0] * sc, dd[2][1] * sc);
    }
    BAR();
    {
        u32x4 ov[3];
#pragma unroll
        for (int i = 0; i < 3; ++i) { const int e0 = i * 512 + tid, e = e0 < 1152 ? e0 : 0, R = e / 144, e1 = e % 144;
            ov[i] = *(const LAS u32x4*)(lds + L_OST + (16 * R + (e1 & 15)) * 144 + (e1 >> 4) * 16); }
#pragma unroll
        for (int i = 0; i < 3; ++i) { const int e0 = i * 512 + tid, R = e0 / 144, e1 = e0 % 144;
            if (e0 < 1152) st_dev(wsr, (unsigned)(WS_O + ((size_t)(rt * 8 + R) * 144 + 9 * hp) * 256 + e1 * 16), ov[i]); }
    }
    STAMP(14);
    {
        const int cg = hp & 7, rh = hp >> 3, kh = wid >> 2, r = wid & 3;
        const char* wot = ws + WS_WOT + (size_t)cg * 165888;
        const unsigned p3 = wid * 1024 + lane * 16;
        const unsigned src0 = p3 < 4608 ? p3 : 18 * 4608 + (p3 - 4608), src1 = 18 * 4608 + (8192 - 4608) + lane * 16;
        auto issue3 = [&](int s, int slot) {
            glds16_dev(src0, wot + s * 4608, lds0 + slot * G3_SLOT + wid * 1024);
            if (wid == (s & 7)) glds16_dev(src1, wot + s * 4608, lds0 + slot * G3_SLOT + 8192);
        };
        DRAIN_BAR();
        if (tid == 0) set_flag(F_O + (rt * 16 + hp) * 32, tag);
        for (int s = 0; s < G3_NSLOT; ++s) issue3(s, s);
        STAMP(15);
        if (wid == 0) wait_flag(F_O + (rt * 16 + r16) * 32, tag);
        BAR();
        STAMP(16);
        u32x4 of[18];
        const unsigned ob = (unsigned)(WS_O + ((size_t)(rt * 8 + 4 * rh + r) * 144 + 72 * kh) * 256 + lane * 16);
#pragma unroll
        for (int s = 0; s < 18; ++s) of[s] = ld_dev(wsr, ob + s * 1024);
        f32x4 a3[5];
#pragma unroll
        for (int t = 0; t < 5; ++t) a3[t] = f32x4{0, 0, 0, 0};
        WAITV(18);
        BAR();
        const int wro = kh * 4608 + lane * 16, wr4 = kh * 4608 + 4096 + g * 128 + (r16 & 7) * 16;
        bf16x8 wf[5];
#pragma unroll
        for (int t = 0; t < 4; ++t) wf[t] = ld128(lds + wro + t * 1024);
        wf[4] = ld128(lds + wr4);
#pragma unroll
        for (int s = 0; s < 18; ++s) {
            if (s == 3) { BAR(); for (int s2 = G3_NSLOT; s2 < 18; ++s2) issue3(s2, s2 - G3_NSLOT); }
            if (s == G3_NSLOT - 1) { WAITV(0); BAR(); }
            const int sn = s + 1 < G3_NSLOT ? s + 1 : s + 1 - G3_NSLOT;
            const lptr sp = lds + sn * G3_SLOT;
            const bf16x8 bo = __builtin_bit_cast(bf16x8, of[s]);
            SB();
#pragma unroll
            for (int t = 0; t < 5; ++t) {
                bf16x8 nw = wf[t];
                if (s + 1 < 18) nw = ld128(sp + (t < 4 ? wro + t * 1024 : wr4));
                SB();
                a3[t] = MFMA16(wf[t], bo, a3[t]);
                SB();
                wf[t] = nw;
            }
        }
        BAR();
        STAMP(17);
        const lptr rp = lds + r * 5120 + lane * 16;
        if (kh == 1) {
#pragma unroll
            for (int t = 0; t < 5; ++t) *(LAS f32x4*)(rp + t * 1024) = a3[t];
        }
        BAR();
        if (kh == 0) {
            char* orow = (char*)out + (size_t)(rt * 128 + 64 * rh + 16 * r + r16) * 1152 + cg * 144 + g * 8;
#pragma unroll
            for (int t = 0; t < 5; ++t) {
                const f32x4 u = a3[t] + *(const LAS f32x4*)(rp + t * 1024);
                if (t < 4 || g < 2) *(u32x2*)(orow + t * 32) = u32x2{cvtpk(u[0], u[1]), cvtpk(u[2], u[3])};
            }
        }
        STAMP(18);
        if (b == 0 && tid == 0) set_flag((gu32*)(ws + WS_EPOCH), tag);
    }
}

extern "C" void kernel_launch(void* const* d_in, const int* in_sizes, int n_in, void* d_out, int out_size, void* d_ws, size_t ws_size, hipStream_t stream) {
    (void)hipFuncSetAttribute((const void*)fused_kernel, hipFuncAttributeMaxDynamicSharedMemorySize, LDS_BYTES);
    fused_kernel<<<256, 512, LDS_BYTES, stream>>>((const char*)d_in[0], (const char*)d_in[1], (const char*)d_in[5], (const char*)d_in[2], (const char*)d_in[3],
                                                  (const char*)d_in[6], (const char*)d_in[7], (char*)d_ws, (u16*)d_out);
}
```

```cpp
#include <hip/hip_runtime.h>
#include <hip/hip_bf16.h>
#include <stdint.h>

typedef unsigned short u16;
typedef short bf16x8 __attribute__((ext_vector_type(8)));
typedef short s16x4 __attribute__((ext_vector_type(4)));
typedef float f32x4 __attribute__((ext_vector_type(4)));
typedef float f32x16 __attribute__((ext_vector_type(16)));
typedef unsigned u32x4 __attribute__((ext_vector_type(4)));
typedef unsigned u32x2 __attribute__((ext_vector_type(2)));
typedef __bf16 bf16x2_t __attribute__((ext_vector_type(2)));
typedef float f32x2 __attribute__((ext_vector_type(2)));
#define LAS __attribute__((address_space(3)))
#define GAS __attribute__((address_space(1)))
typedef LAS char* lptr;
typedef GAS unsigned gu32;

constexpr size_t WS_EPOCH = 0;
constexpr size_t WS_FLAG = 4096;
constexpr size_t WS_WT   = 4096 + 5 * 256 * 128;
constexpr size_t WS_WOT  = WS_WT + 16 * 9 * 20480;
constexpr size_t WS_KV   = WS_WOT + 8 * 165888;
constexpr size_t WS_PART = WS_KV + 256 * 24576;
constexpr size_t WS_O    = WS_PART + 256 * 6144;
constexpr size_t WS_DBG  = WS_O + 16 * 8 * 36864;

constexpr int XS = 16384, WS1 = 20480;
constexpr int L_X = 0, L_W = 3 * XS;
constexpr int L_STG = L_W + 3 * WS1;
constexpr int L_WP = L_STG;
constexpr int L_GW = L_WP + 15 * 1024;
constexpr int L_PHI = L_STG + 24576;
constexpr int L_WOP = L_PHI + 2048;
constexpr int L_PKX = 118784;
constexpr int L_K = 0;
constexpr int L_Q = 73728;
constexpr int L_VA = 86016;
constexpr int L_PK = 98304;
constexpr int L_GATE = 147456;
constexpr int L_KVT = 147968;
constexpr int L_OST = 73728;
constexpr int LDS_BYTES = 160 * 1024;
constexpr int G3_SLOT = 18432;
__device__ __forceinline__ constexpr int g3_slot(int p) { return (p < 3 ? 5 + p : p < 8 ? p - 3 : 5) * G3_SLOT; }
constexpr unsigned SPIN_CAP = 1u << 18;
constexpr float LOG2E = 1.4426950408889634f;
constexpr float QSCALE = 0.23570226039551584f * LOG2E;
constexpr float THR = 8.f;
constexpr float NEGBIG = -1e30f;

__device__ __forceinline__ unsigned cvtpk(float lo, float hi) { const f32x2 v = {lo, hi}; return __builtin_bit_cast(unsigned, __builtin_convertvector(v, bf16x2_t)); }
__device__ __forceinline__ void glds16(unsigned voff, const void* sbase, unsigned lds_addr) {
    unsigned sv;
    asm volatile("s_mov_b32 %0, m0\n\ts_mov_b32 m0, %3\n\ts_nop 0\n\tglobal_load_lds_dwordx4 %1, %2\n\ts_mov_b32 m0, %0" : "=&s"(sv) : "v"(voff), "s"(sbase), "s"(lds_addr) : "memory");
}
__device__ __forceinline__ void glds16_dev(unsigned voff, const void* sbase, unsigned lds_addr) {
    unsigned sv;
    asm volatile("s_mov_b32 %0, m0\n\ts_mov_b32 m0, %3\n\ts_nop 0\n\tglobal_load_lds_dwordx4 %1, %2 sc1\n\ts_mov_b32 m0, %0" : "=&s"(sv) : "v"(voff), "s"(sbase), "s"(lds_addr) : "memory");
}
__device__ __forceinline__ void glds16_ptr(const void* g, unsigned lds_addr) {
    unsigned sv;
    asm volatile("s_mov_b32 %0, m0\n\ts_mov_b32 m0, %2\n\ts_nop 0\n\tglobal_load_lds_dwordx4 %1, off\n\ts_mov_b32 m0, %0" : "=&s"(sv) : "v"(g), "s"(lds_addr) : "memory");
}
#define WAITV(n) asm volatile("s_waitcnt vmcnt(" #n ")" ::: "memory")
#define WAITL() asm volatile("s_waitcnt lgkmcnt(0)" ::: "memory")
#define LGKM0() do { __builtin_amdgcn_s_waitcnt(0xC07F); asm volatile("" ::: "memory"); } while (0)
#define BAR() do { asm volatile("" ::: "memory"); __builtin_amdgcn_s_waitcnt(0xC07F); __builtin_amdgcn_s_barrier(); asm volatile("" ::: "memory"); } while (0)
#define DRAIN_BAR() do { asm volatile("s_waitcnt vmcnt(0)" ::: "memory"); BAR(); } while (0)
#define SB() __builtin_amdgcn_sched_barrier(0)
#define MFMA16(a, b, c) __builtin_amdgcn_mfma_f32_16x16x32_bf16(a, b, c, 0, 0, 0)
#define MFMA32(a, b, c) __builtin_amdgcn_mfma_f32_32x32x16_bf16(a, b, c, 0, 0, 0)
__device__ __forceinline__ bf16x8 ld128(lptr p) { return *(const LAS bf16x8*)p; }
__device__ __forceinline__ s16x4 trrd(lptr p) { typedef short v4i16_t __attribute__((ext_vector_type(4))); return __builtin_bit_cast(s16x4, __builtin_amdgcn_ds_read_tr16_b64_v4i16((LAS v4i16_t*)p)); }
__device__ __forceinline__ bf16x8 cat8(s16x4 a, s16x4 b) { return __builtin_shufflevector(a, b, 0, 1, 2, 3, 4, 5, 6, 7); }
__device__ __forceinline__ bf16x8 mk8(unsigned a, unsigned b, unsigned c, unsigned d) { u32x4 w = {a, b, c, d}; return __builtin_bit_cast(bf16x8, w); }
__device__ __forceinline__ float x16max(float x) { auto s = __builtin_amdgcn_permlane16_swap(__float_as_uint(x), __float_as_uint(x), false, false); return fmaxf(__uint_as_float(s[0]), __uint_as_float(s[1])); }
__device__ __forceinline__ float x16sum(float x) { auto s = __builtin_amdgcn_permlane16_swap(__float_as_uint(x), __float_as_uint(x), false, false); return __uint_as_float(s[0]) + __uint_as_float(s[1]); }
__device__ __forceinline__ float x32max(float x) { auto s = __builtin_amdgcn_permlane32_swap(__float_as_uint(x), __float_as_uint(x), false, false); return fmaxf(__uint_as_float(s[0]), __uint_as_float(s[1])); }
__device__ __forceinline__ void st_dev(const __amdgpu_buffer_rsrc_t rs, unsigned off, u32x4 v) { __builtin_amdgcn_raw_buffer_store_b128(v, rs, off, 0, 16); }
__device__ __forceinline__ u32x4 ld_dev(const __amdgpu_buffer_rsrc_t rs, unsigned off) { return __builtin_amdgcn_raw_buffer_load_b128(rs, off, 0, 16); }
__device__ __forceinline__ void wait_flag(gu32* p, unsigned tag) {
    for (unsigned spins = 0; spins < SPIN_CAP; ++spins) {
        const unsigned v = __hip_atomic_load(p, __ATOMIC_RELAXED, __HIP_MEMORY_SCOPE_AGENT);
        if (__all(v == tag)) break;
        __builtin_amdgcn_s_sleep(1);
    }
}
__device__ __forceinline__ void set_flag(gu32* p, unsigned tag) { __hip_atomic_store(p, tag, __ATOMIC_RELAXED, __HIP_MEMORY_SCOPE_AGENT); }
__device__ __forceinline__ u32x4 gather8(lptr p, int stride) {
    unsigned w[4];
#pragma unroll
    for (int j = 0; j < 4; ++j) w[j] = (unsigned)*(const LAS u16*)(p + 2 * j * stride) | ((unsigned)*(const LAS u16*)(p + (2 * j + 1) * stride) << 16);
    return u32x4{w[0], w[1], w[2], w[3]};
}
__device__ __forceinline__ int piece_start(int j) { return (9 * j + 1) >> 1; }

__device__ __forceinline__ unsigned flag_request(gu32* p) { unsigned v; asm volatile("global_load_dword %0, %1, off sc1" : "=v"(v) : "v"(p) : "memory"); return v; }
__device__ __forceinline__ void flag_confirm(unsigned v, gu32* p, unsigned tag) {
    asm volatile("" : "+v"(v));
    if (!__all(v == tag)) wait_flag(p, tag);
}

#define STAMP(i) do {} while (0)
#define WSTAMP(i) do {} while (0)

__device__ __forceinline__ void feature_map(const float (&p)[9], unsigned (&o)[9]) {
    float mx = fmaxf(fmaxf(p[0], p[1]), p[2]), mn = fminf(fminf(p[0], p[1]), p[2]);
#pragma unroll
    for (int i = 3; i < 9; i += 2) { mx = fmaxf(fmaxf(mx, p[i]), p[i + 1]); mn = fminf(fminf(mn, p[i]), p[i + 1]); }
    mx = x16max(mx); mn = -x16max(-mn);
    const float mxl = -mx * LOG2E, mnl = mn * LOG2E;
    float a[9], b[9], sa = 0.f, sb = 0.f;
#pragma unroll
    for (int i = 0; i < 9; ++i) { a[i] = __builtin_amdgcn_exp2f(__builtin_fmaf(p[i], LOG2E, mxl)); b[i] = __builtin_amdgcn_exp2f(__builtin_fmaf(p[i], -LOG2E, mnl)); sa += a[i]; sb += b[i]; }
    sa = x16sum(sa); sb = x16sum(sb);
    const float ra = __builtin_amdgcn_rcpf(sa), rb = __builtin_amdgcn_rcpf(sb);
#pragma unroll
    for (int i = 0; i < 9; ++i) { a[i] *= ra; b[i] *= rb; }
    o[0] = cvtpk(a[0], a[1]); o[1] = cvtpk(a[2], a[3]); o[2] = cvtpk(a[4], a[5]); o[3] = cvtpk(a[6], a[7]); o[4] = cvtpk(a[8], b[0]);
    o[5] = cvtpk(b[1], b[2]); o[6] = cvtpk(b[3], b[4]); o[7] = cvtpk(b[5], b[6]); o[8] = cvtpk(b[7], b[8]);
}

__global__ __launch_bounds__(512, 2) void fused_kernel(const char* __restrict__ x, const char* __restrict__ wA, const char* __restrict__ wB, const char* __restrict__ wphi,
                                                       const char* __restrict__ woA, const char* __restrict__ woB, const char* __restrict__ gw,
                                                       char* __restrict__ ws, u16* __restrict__ out) {
    extern __shared__ __attribute__((aligned(16))) char lds_raw[];
    const lptr lds = (lptr)lds_raw;
    const unsigned lds0 = (unsigned)(uintptr_t)lds_raw;
    const int tid = threadIdx.x, lane = tid & 63, wid = __builtin_amdgcn_readfirstlane(tid >> 6);
    const int b = blockIdx.x, hp = (b & 3) * 4 + ((b >> 3) & 3), rt = ((b >> 2) & 1) * 8 + (b >> 5);
    const bool loader = wid >= 4;
    const int lw = wid & 3;
    const int h = lw >> 1, tg = 2 * (lw & 1) + (wid >> 2);
    const int r16 = lane & 15, g = lane >> 4;
    gu32* flags = (gu32*)(ws + WS_FLAG);
    gu32* F_W = flags;
    gu32* F_WO = flags + 256 * 32;
    gu32* F_KV = flags + 512 * 32;
    gu32* F_P = flags + 768 * 32;
    gu32* F_O = flags + 1024 * 32;
    STAMP(0);
    unsigned tagv = flag_request((gu32*)(ws + WS_EPOCH));
    const __amdgpu_buffer_rsrc_t wsr = __builtin_amdgcn_make_buffer_rsrc(ws, 0, 0x2000000, 0x00020000);

    const int G0 = piece_start(rt), nG = piece_start(rt + 1) - G0;
    const int j2 = b >> 3, cgp = b & 7, C0 = piece_start(j2), nC = piece_start(j2 + 1) - C0;
#pragma unroll
    for (int i = 0; i < 2; ++i) {
        const int pc = wid + 8 * i;
        if (pc < 15) { int c = pc * 64 + lane; if (c >= nG * 192) c = 0;
            const int row = c / 24, ch = c % 24, w = ch / 3, cc = ch % 3, sg = w >> 1;
            const char* src = (sg < 3 ? wA + sg * 1152 : wB) + (size_t)(8 * G0 + row) * 3456 + (2 * hp + (w & 1)) * 36 + (cc == 2 ? 20 : cc * 16);
            glds16_ptr(src, lds0 + L_WP + pc * 1024); }
        else glds16(G0 * 16 + (lane < nG ? lane : 0) * 16, gw, lds0 + L_GW);
    }
    unsigned xo[4], w0o[3];
    const char* w0base = wB + 1152 + (hp >> 3) * 1152 + (hp & 7) * 144;
    auto issue_x = [&](int kb, int slot) {
#pragma unroll
        for (int i = 0; i < 4; ++i) glds16(xo[i], x + kb * 128, lds0 + L_X + slot * XS + lw * 4096 + i * 1024);
    };
    auto issue_w0 = [&](int kb, int slot) {
        const char* src = w0base + (size_t)kb * 64 * 3456;
        glds16(w0o[0], src, lds0 + L_W + slot * WS1 + lw * 1024); glds16(w0o[1], src, lds0 + L_W + slot * WS1 + (lw + 4) * 1024);
        if (lw == 0) glds16(w0o[2], src, lds0 + L_W + slot * WS1 + 8192);
    };
    if (loader) {
#pragma unroll
        for (int i = 0; i < 4; ++i) { const int row = 32 * lw + 8 * i + (lane >> 3); xo[i] = (unsigned)(rt * 128 + row) * 1152u + (unsigned)(((lane & 7) ^ ((row >> 1) & 7)) << 4); }
#pragma unroll
        for (int i = 0; i < 3; ++i) { int c = (lw + 4 * i) * 64 + lane; if (c >= 576) c = 0;
            const int rho = c / 9, cc = c % 9, k = (rho & 32) + 8 * ((rho & 15) >> 2) + (rho & 3) + 4 * ((rho >> 4) & 1); w0o[i] = (unsigned)k * 3456u + cc * 16; }
        issue_x(0, 0); issue_w0(0, 0);
        issue_x(1, 1); issue_w0(1, 1);
        WAITV(6);
    } else WAITV(0);
    BAR();
    asm volatile("" : "+v"(tagv));
    const unsigned tag = __builtin_amdgcn_readfirstlane(tagv) + 1u;
    STAMP(1);
    if (loader) { issue_x(2, 2); issue_w0(2, 2); }
    auto w1_units = [&](int i) {
        const int q4 = r16 >> 2, p4 = r16 & 3;
        const int U0 = i * 16 + lw * 4 + g, U = U0 < nG * 10 ? U0 : 0, Gi = (U * 26) >> 8, ht = U - 10 * Gi, hh2 = ht >= 5, t = ht - 5 * hh2;
        const int sg = t < 4 ? (0x1032 >> (4 * t)) & 3 : p4;
        const lptr p = lds + L_WP + (8 * Gi + q4) * 384 + (sg * 2 + hh2) * 48 + (t < 4 ? p4 * 8 : 40);
        const s16x4 ra = trrd(p), rb = trrd(p + 4 * 384);
        const u32x4 gv = *(const LAS u32x4*)(lds + L_GW + Gi * 16);
        u32x4 v = __builtin_bit_cast(u32x4, cat8(ra, rb));
        const bool isg = t == 4 && r16 == 12;
        v = u32x4{isg ? gv[0] : v[0], isg ? gv[1] : v[1], isg ? gv[2] : v[2], isg ? gv[3] : v[3]};
        const int G = G0 + Gi;
        const unsigned off = (unsigned)(WS_WT + (size_t)hp * 9 * 20480) + (G >> 3) * 20480 + (ht * 2 + ((G >> 2) & 1)) * 1024 + ((G & 3) * 16 + r16) * 16;
        st_dev(wsr, U0 < nG * 10 ? off : 0xfffffff0u, v);
    };
    const char* wt = ws + WS_WT + (size_t)hp * 9 * 20480;
    auto issue_w1 = [&](int kb, int slot) {
#pragma unroll
        for (int i = 0; i < 5; ++i) glds16_dev((unsigned)(lw * 1024 + lane * 16), wt + kb * 20480 + i * 4096, lds0 + L_W + slot * WS1 + i * 4096 + lw * 1024);
    };
    unsigned pk[3][2][2];
    if (!loader) {
        {
            f32x4 a0[5][2];
#pragma unroll
            for (int t = 0; t < 5; ++t) { a0[t][0] = f32x4{0, 0, 0, 0}; a0[t][1] = f32x4{0, 0, 0, 0}; }
            const int xr = L_X + (32 * lw + r16) * 128 + ((g ^ (r16 >> 1)) << 4);
            const int q4 = r16 >> 2, p4 = r16 & 3;
            const int wr = L_W + (4 * g + q4) * 144 + p4 * 8, wr4 = L_W + (4 * g + q4) * 144 + 128 + (p4 & 1) * 8;
            bf16x8 wa[5], xa[2], wb[5], xb[2];
            unsigned fw = 0;
            xa[0] = ld128(lds + xr); xa[1] = ld128(lds + xr + 2048);
#pragma unroll
            for (int t = 0; t < 5; ++t) { const lptr p = lds + (t < 4 ? wr + t * 32 : wr4); wa[t] = cat8(trrd(p), trrd(p + 16 * 144)); }
            int slot = 0;
            for (int kb = 0; kb < 8; ++kb) {
                const lptr sx = lds + slot * XS, sw = lds + slot * WS1 + 32 * 144;
                LGKM0();
                SB();
                xb[0] = ld128(sx + (xr ^ 64)); xb[1] = ld128(sx + (xr ^ 64) + 2048);
#pragma unroll
                for (int t = 0; t < 5; ++t) { const lptr p = sw + (t < 4 ? wr + t * 32 : wr4); wb[t] = cat8(trrd(p), trrd(p + 16 * 144)); }
                SB();
#pragma unroll
                for (int t = 0; t < 5; ++t) { a0[t][0] = MFMA16(wa[t], xa[0], a0[t][0]); a0[t][1] = MFMA16(wa[t], xa[1], a0[t][1]); }
                SB();
                if (kb == 6) WAITV(0);
                if (kb == 6 && lw == 0) flag_confirm(fw, F_W + (hp * 16 + r16) * 32, tag);
                BAR();
                if (kb == 5 && lw == 0) fw = flag_request(F_W + (hp * 16 + r16) * 32);
                slot = slot == 2 ? 0 : slot + 1;
                const lptr nx = lds + slot * XS, nw = lds + slot * WS1;
                SB();
                bf16x8 xn[2], wn[5];
                xn[0] = ld128(nx + xr); xn[1] = ld128(nx + xr + 2048);
#pragma unroll
                for (int t = 0; t < 5; ++t) { const lptr p = nw + (t < 4 ? wr + t * 32 : wr4); wn[t] = cat8(trrd(p), trrd(p + 16 * 144)); }
                SB();
#pragma unroll
                for (int t = 0; t < 5; ++t) { a0[t][0] = MFMA16(wb[t], xb[0], a0[t][0]); a0[t][1] = MFMA16(wb[t], xb[1], a0[t][1]); }
                SB();
                xa[0] = xn[0]; xa[1] = xn[1];
#pragma unroll
                for (int t = 0; t < 5; ++t) wa[t] = wn[t];
            }
            {
                const lptr sx = lds + 2 * XS, sw = lds + 2 * WS1 + 32 * 144;
                LGKM0();
                SB();
                xb[0] = ld128(sx + (xr ^ 64)); xb[1] = ld128(sx + (xr ^ 64) + 2048);
#pragma unroll
                for (int t = 0; t < 5; ++t) { const lptr p = sw + (t < 4 ? wr + t * 32 : wr4); wb[t] = cat8(trrd(p), trrd(p + 16 * 144)); }
                SB();
#pragma unroll
                for (int t = 0; t < 5; ++t) { a0[t][0] = MFMA16(wa[t], xa[0], a0[t][0]); a0[t][1] = MFMA16(wa[t], xa[1], a0[t][1]); }
                SB();
                BAR();
#pragma unroll
                for (int t = 0; t < 5; ++t) { a0[t][0] = MFMA16(wb[t], xb[0], a0[t][0]); a0[t][1] = MFMA16(wb[t], xb[1], a0[t][1]); }
            }
            STAMP(2);
            const unsigned padv = hp >= 8 ? 0x00003f80u : 0u;
#pragma unroll
            for (int tt = 0; tt < 2; ++tt) {
                const int tok = 32 * lw + 16 * tt + r16;
#pragma unroll
                for (int t = 0; t < 5; ++t) {
                    const int col = 16 * t + 4 * g;
                    if (t < 4 || g < 2) {
                        const int i0 = col / 18, i1 = (col + 2) / 18;
                        *(LAS unsigned*)(lds + L_STG + i0 * 6144 + tok * 48 + (col - 18 * i0) * 2) = cvtpk(a0[t][tt][0], a0[t][tt][1]);
                        *(LAS unsigned*)(lds + L_STG + i1 * 6144 + tok * 48 + (col + 2 - 18 * i1) * 2) = cvtpk(a0[t][tt][2], a0[t][tt][3]);
                    }
                }
                *(LAS unsigned*)(lds + L_STG + g * 6144 + tok * 48 + 36) = padv;
                *(LAS u32x2*)(lds + L_STG + g * 6144 + tok * 48 + 40) = u32x2{0u, 0u};
            }
            WAITL();
            u32x4 kvv[6];
#pragma unroll
            for (int i = 0; i < 6; ++i) { const int e = i * 64 + lane, hd = e / 96, o = e % 96; kvv[i] = *(const LAS u32x4*)(lds + L_STG + hd * 6144 + 32 * lw * 48 + o * 16); }
#pragma unroll
            for (int i = 0; i < 6; ++i) { const int e = i * 64 + lane, hd = e / 96, o = e % 96;
                st_dev(wsr, (unsigned)(WS_KV + (size_t)((rt * 16 + 2 * (hp & 7) + (hd >> 1)) * 4 + (hd & 1) * 2 + (hp >> 3)) * 6144) + 32 * lw * 48 + o * 16, kvv[i]); }
        }
        BAR();
        STAMP(3);
        f32x4 acc[5][4];
#pragma unroll
        for (int t = 0; t < 5; ++t)
#pragma unroll
            for (int u = 0; u < 4; ++u) acc[t][u] = f32x4{0, 0, 0, 0};
        const int th = lw & 1;
        const int xr = L_X + (64 * th + r16) * 128 + ((g ^ (r16 >> 1)) << 4), wr = L_W + h * 10240 + lane * 16;
        bf16x8 wa[5], xa[4], wb[5], xb[4];
        unsigned fk = 0;
        gu32* fkp = F_KV + (((lane & 1) * 8 + (hp >> 1)) * 16 + (rt - (lane >> 1) % 3 >= 0 ? rt - (lane >> 1) % 3 : rt)) * 32;
#pragma unroll
        for (int u = 0; u < 4; ++u) xa[u] = ld128(lds + xr + u * 2048);
#pragma unroll
        for (int t = 0; t < 5; ++t) wa[t] = ld128(lds + wr + t * 2048);
        int slot = 0;
        for (int i = 0; i < 8; ++i) {
            const lptr sx = lds + slot * XS, sw = lds + slot * WS1;
            SB();
#pragma unroll
            for (int u = 0; u < 4; ++u) xb[u] = ld128(sx + (xr ^ 64) + u * 2048);
#pragma unroll
            for (int t = 0; t < 5; ++t) { wb[t] = ld128(sw + wr + t * 2048 + 1024); SB();
#pragma unroll
                for (int u = 0; u < 4; ++u) acc[t][u] = MFMA16(wa[t], xa[u], acc[t][u]);
                SB(); }
            if (i == 1 || i == 4) WAITV(0);
            if (i == 4 && lw == 0) flag_confirm(fk, fkp, tag);
            BAR();
            if (i == 1 && tid == 0) set_flag(F_KV + (hp * 16 + rt) * 32, tag);
            if (i == 3 && lw == 0) fk = flag_request(fkp);
            slot = slot == 2 ? 0 : slot + 1;
            const lptr nx = lds + slot * XS, nw = lds + slot * WS1;
            SB();
#pragma unroll
            for (int u = 0; u < 4; ++u) xa[u] = ld128(nx + xr + u * 2048);
#pragma unroll
            for (int t = 0; t < 5; ++t) { const bf16x8 n = ld128(nw + wr + t * 2048); SB();
#pragma unroll
                for (int u = 0; u < 4; ++u) acc[t][u] = MFMA16(wb[t], xb[u], acc[t][u]);
                SB(); wa[t] = n; }
        }
        {
            const lptr sx = lds + 2 * XS, sw = lds + 2 * WS1;
            SB();
#pragma unroll
            for (int u = 0; u < 4; ++u) xb[u] = ld128(sx + (xr ^ 64) + u * 2048);
#pragma unroll
            for (int t = 0; t < 5; ++t) { wb[t] = ld128(sw + wr + t * 2048 + 1024); SB();
#pragma unroll
                for (int u = 0; u < 4; ++u) acc[t][u] = MFMA16(wa[t], xa[u], acc[t][u]);
                SB(); }
            BAR();
#pragma unroll
            for (int t = 0; t < 5; ++t)
#pragma unroll
                for (int u = 0; u < 4; ++u) acc[t][u] = MFMA16(wb[t], xb[u], acc[t][u]);
        }
        STAMP(4);
        const lptr Qb = lds + L_Q + h * 6144, VAb = lds + L_VA + h * 6144;
#pragma unroll
        for (int u = 0; u < 4; ++u) {
            const int tok = 64 * th + 16 * u + r16;
            unsigned p2[3][2];
#pragma unroll
            for (int t = 0; t < 3; ++t) { p2[t][0] = cvtpk(acc[2 + t][u][0], acc[2 + t][u][1]); p2[t][1] = cvtpk(acc[2 + t][u][2], acc[2 + t][u][3]); }
            if (u < 2) {
#pragma unroll
                for (int t = 0; t < 3; ++t) { pk[t][u][0] = p2[t][0]; pk[t][u][1] = p2[t][1]; }
            } else {
#pragma unroll
                for (int t = 0; t < 3; ++t) *(LAS u32x2*)(lds + L_PKX + (lw * 3 + t) * 1024 + lane * 16 + (u - 2) * 8) = u32x2{p2[t][0], p2[t][1]};
            }
            *(LAS u32x2*)(VAb + tok * 48 + 8 * g) = u32x2{cvtpk(acc[0][u][0], acc[0][u][1]), cvtpk(acc[0][u][2], acc[0][u][3])};
            *(LAS u32x2*)(Qb + tok * 48 + 8 * g) = u32x2{cvtpk(acc[1][u][0] * QSCALE, acc[1][u][1] * QSCALE), cvtpk(acc[1][u][2] * QSCALE, acc[1][u][3] * QSCALE)};
            if (g == 2) *(LAS u32x2*)(VAb + tok * 48 + 32) = u32x2{p2[2][1], 0x00003f80u};
            else if (g == 3) {
                *(LAS unsigned*)(Qb + tok * 48 + 32) = cvtpk(acc[4][u][2] * QSCALE, acc[4][u][3] * QSCALE);
                if (h == 0) *(LAS float*)(lds + L_GATE + tok * 4) = __builtin_amdgcn_rcpf(1.f + __builtin_amdgcn_exp2f(-acc[4][u][0] * LOG2E));
            }
        }
    } else {
        for (int kb = 0; kb < 9; ++kb) {
            if (kb == 1) WAITV(8); else if (kb == 2) WAITV(10); else if (kb == 3) WAITV(8); else if (kb < 7) WAITV(6); else if (kb == 7) WAITV(5);
            BAR();
            if (kb == 4 && tid == 256) set_flag(F_W + (hp * 16 + rt) * 32, tag);
            if (kb == 6) {
#pragma unroll
                for (int i = 0; i < 2; ++i) { const int pc = lw + 4 * i;
                    if (pc < 6) { int c = pc * 64 + lane; if (c >= nC * 72) c = 0;
                        const int row = c / 9, cc = c % 9, C = C0 + (row >> 3), hq = C / 9, p = 8 * (C % 9) + (row & 7);
                        int br, head, d;
                        if (p < 32) { br = 1; head = p >> 4; d = p & 15; } else if (p < 36) { br = 1; head = (p >> 1) & 1; d = 16 + (p & 1); } else { br = 0; head = p >= 54; d = p - 36 - 18 * head; }
                        glds16_ptr((br ? woB : woA) + ((2 * hq + head) * 18 + d) * 1152 + cgp * 144 + cc * 16, lds0 + L_WOP + pc * 1024); }
                    else { int c = (pc - 6) * 64 + lane; if (c >= 108) c = 0; const int row = c / 3, cc = c % 3;
                        glds16((unsigned)(hp * 1296 + row * 36 + (cc == 2 ? 20 : cc * 16)), wphi, lds0 + L_PHI + (pc - 6) * 1024); } }
            }
            if (kb < 6) { issue_x(kb + 3, kb % 3); issue_w0(kb + 3, kb % 3); }
            else issue_w1(kb, kb - 6);
            if (kb < 2) { w1_units(2 * kb); w1_units(2 * kb + 1); }
        }
        WAITV(10);
        BAR();
        u32x4 hv[18];
        auto kvload = [&](int n) { const int pc = n * 4 + lw, tl = pc / 24, ar = (pc % 24) / 6, o = pc % 6, srt = rt - 2 + tl;
            return ld_dev(wsr, (unsigned)(WS_KV + (size_t)(((srt >= 0 ? srt : rt) * 16 + hp) * 4 + ar) * 6144) + o * 1024 + lane * 16); };
        for (int i = 0; i < 5; ++i) {
            if (i == 0) WAITV(5); else if (i == 1) WAITV(10); else if (i == 2) WAITV(11); else if (i == 3) WAITV(10); else WAITV(9);
            BAR();
            if (i == 4 && tid == 256) set_flag(F_WO + b * 32, tag);
            issue_x(i, i % 3); issue_w1(i, i % 3);
            if (i < 2) {
                const int q4 = r16 >> 2, p4 = r16 & 3;
                const int U0 = i * 16 + lw * 4 + g, U = U0 < nC * 5 ? U0 : 0, Ci = (U * 52) >> 8, t = U - 5 * Ci, C = C0 + Ci, kq = C / 36, s = (C % 36) >> 2, gg = C & 3;
                const lptr p = lds + L_WOP + (8 * Ci + q4) * 144 + (t < 4 ? t * 32 + p4 * 8 : 128 + (p4 & 1) * 8);
                const u32x4 v = __builtin_bit_cast(u32x4, cat8(trrd(p), trrd(p + 4 * 144)));
                const unsigned off = (unsigned)(WS_WOT + (size_t)cgp * 165888) + (s * 4 + kq) * 4608 + (t < 4 ? t * 1024 + (gg * 16 + r16) * 16 : 4096 + (gg * 8 + r16) * 16);
                st_dev(wsr, U0 < nC * 5 && (t < 4 || r16 < 8) ? off : 0xfffffff0u, v);
            }
        }
        WAITV(9); BAR();
        issue_x(5, 2); issue_w1(5, 2);
#pragma unroll
        for (int n = 0; n < 6; ++n) hv[n] = kvload(n);
        WAITV(15); BAR();
#pragma unroll
        for (int n = 6; n < 12; ++n) hv[n] = kvload(n);
        WAITV(12); BAR();
#pragma unroll
        for (int n = 12; n < 18; ++n) hv[n] = kvload(n);
        BAR();
#pragma unroll
        for (int n = 0; n < 18; ++n) { const int pc = n * 4 + lw, tl = pc / 24, ar = (pc % 24) / 6, o = pc % 6;
            *(LAS u32x4*)(lds + L_K + ar * 18432 + tl * 6144 + o * 1024 + lane * 16) = hv[n]; }
    }
    BAR();
    STAMP(5);
    const int cg = (b >> 3) & 7, r64 = 4 * (b & 7) + (b >> 6);
    unsigned fwo = 0;
    gu32* fwop = F_WO + (cg + 8 * (lane & 31)) * 32;
    if (wid == 0) fwo = flag_request(fwop);
    if (loader) {
#pragma unroll
        for (int t = 0; t < 3; ++t) { const u32x4 v = *(const LAS u32x4*)(lds + L_PKX + (lw * 3 + t) * 1024 + lane * 16); pk[t][0][0] = v[0]; pk[t][0][1] = v[1]; pk[t][1][0] = v[2]; pk[t][1][1] = v[3]; }
    }
    unsigned phq[9];
    {
        const bool lo = r16 < 8;
        unsigned phk[9];
        float p[2][9];
        s16x4 R1[3], R2[3];
        {
            const int q4 = r16 >> 2, p4 = r16 & 3;
            const lptr pb = lds + L_PHI + h * 864 + q4 * 48;
#pragma unroll
            for (int i = 0; i < 3; ++i) { const int off = i < 2 ? 16 * (p4 & 1) + 8 * i : 40; R1[i] = trrd(pb + 4 * g * 48 + off); R2[i] = trrd(pb + 14 * 48 + off); }
        }
#pragma unroll
        for (int X = 0; X < 2; ++X) {
            const bf16x8 bA = mk8(pk[X][0][0], pk[X][0][1], pk[2][0][0], pk[2][0][1]);
            const bf16x8 bB = mk8(pk[X][1][0], pk[X][1][1], pk[2][1][0], pk[2][1][1]);
            f32x4 d[3];
#pragma unroll
            for (int i = 0; i < 3; ++i) {
                const u32x2 a = __builtin_bit_cast(u32x2, R1[i]), c = __builtin_bit_cast(u32x2, R2[i]);
                const unsigned f3 = g == X ? c[1] : 0u;
                const bf16x8 Flo = mk8(lo ? a[0] : 0u, lo ? a[1] : 0u, 0u, lo ? f3 : 0u), Fhi = mk8(lo ? 0u : a[0], lo ? 0u : a[1], 0u, lo ? 0u : f3);
                d[i] = MFMA16(Flo, bA, (f32x4{0, 0, 0, 0})); d[i] = MFMA16(Fhi, bB, d[i]);
            }
#pragma unroll
            for (int j = 0; j < 4; ++j) { p[X][j] = d[0][j]; p[X][4 + j] = d[1][j]; }
            p[X][8] = (g & 1) ? d[2][3] : d[2][2];
        }
        feature_map(p[1], phk);
        const lptr pkp = lds + L_PK + h * 10240 + (32 * tg + 16 * (g >> 1) + r16) * 80 + (g & 1) * 40;
        *(LAS u32x2*)(pkp) = u32x2{phk[0], phk[1]}; *(LAS u32x2*)(pkp + 8) = u32x2{phk[2], phk[3]}; *(LAS u32x2*)(pkp + 16) = u32x2{phk[4], phk[5]};
        *(LAS u32x2*)(pkp + 24) = u32x2{phk[6], phk[7]}; *(LAS u32x2*)(pkp + 32) = u32x2{phk[8], 0u};
        feature_map(p[0], phq);
    }
    BAR();
    STAMP(6);
    if (wid >= 2) {
        const int w6 = wid - 2, hh = w6 / 3, mt = w6 % 3, q4 = r16 >> 2, p4 = r16 & 3;
        int u = 4 * mt + p4; if (u > 9) u = 9;
        const lptr pa = lds + L_PK + hh * 10240 + (8 * g + q4) * 80 + u * 8;
        const lptr pb0 = lds + L_VA + hh * 6144 + (8 * g + q4) * 48 + p4 * 8;
        const lptr pb1 = lds + L_VA + hh * 6144 + (8 * g + q4) * 48 + 32;
        f32x4 d0 = {0, 0, 0, 0}, d1 = {0, 0, 0, 0};
        bf16x8 a[4], b0[4], b1[4];
#pragma unroll
        for (int ks = 0; ks < 4; ++ks) {
            a[ks] = cat8(trrd(pa + ks * 32 * 80), trrd(pa + ks * 32 * 80 + 4 * 80));
            b0[ks] = cat8(trrd(pb0 + ks * 32 * 48), trrd(pb0 + ks * 32 * 48 + 4 * 48));
            b1[ks] = cat8(trrd(pb1 + ks * 32 * 48), trrd(pb1 + ks * 32 * 48 + 4 * 48));
        }
        SB();
#pragma unroll
        for (int ks = 0; ks < 4; ++ks) { d0 = MFMA16(a[ks], b0[ks], d0); d1 = MFMA16(a[ks], b1[ks], d1); }
        const int uu = 4 * mt + g;
        const unsigned po = (unsigned)(WS_PART + (size_t)(hp * 16 + rt) * 3072 + hh * 1520);
        if (uu < 10) {
            __builtin_amdgcn_raw_buffer_store_b64(u32x2{cvtpk(d0[0], d0[1]), cvtpk(d0[2], d0[3])}, wsr, po + (r16 * 10 + uu) * 8, 0, 16);
            if (r16 < 3) __builtin_amdgcn_raw_buffer_store_b64(u32x2{cvtpk(d1[0], d1[1]), cvtpk(d1[2], d1[3])}, wsr, po + ((16 + r16) * 10 + uu) * 8, 0, 16);
        }
    }
    const int q32 = lane & 31, hh = lane >> 5;
    const char* wot = ws + WS_WOT + (size_t)cg * 165888;
    auto g3_step = [&](int p) { const int s = p + cg; return s >= 9 ? s - 9 : s; };
    f32x16 oacc;
    u32x4 pv[2][4];
    auto pv_load = [&]() {
        if (wid < 6) {
#pragma unroll
            for (int n = 0; n < 2; ++n) { const int gr = (2 * wid + n) * 16 + r16;
#pragma unroll
                for (int m = 0; m < 4; ++m) pv[n][m] = ld_dev(wsr, (unsigned)(WS_PART + (size_t)(hp * 16 + 4 * m + g) * 3072) + (gr < 190 ? gr : 0) * 16); }
        }
    };
    {
        const lptr Kb = lds + L_K + h * 36864, Vb = Kb + 18432, Qb = lds + L_Q + h * 6144;
        const bf16x8 qf0 = ld128(Qb + (32 * tg + q32) * 48 + 16 * hh);
        const unsigned q16 = *(const LAS unsigned*)(Qb + (32 * tg + q32) * 48 + 32);
        const bf16x8 qf1 = mk8(hh ? 0u : q16, 0u, 0u, 0u);
        const lptr kp = Kb + (32 * tg + q32) * 48;
        const int i16 = lane & 15, dhalf = (lane >> 4) & 1;
        const lptr vp = Vb + (32 * tg + 4 * hh + (i16 >> 2)) * 48 + (dhalf ? 32 + ((i16 & 3) ? 8 : 0) : (i16 & 3) * 8);
        f32x16 negm;
#pragma unroll
        for (int r = 0; r < 16; ++r) { oacc[r] = 0.f; negm[r] = 0.f; }
        bf16x8 k0, k1;
        auto tile = [&](int KT, int KN, bool first, int mask) {
            SB();
            f32x16 s = MFMA32(k0, qf0, negm); s = MFMA32(k1, qf1, s);
            SB();
            const s16x4 va = trrd(vp + (KT * 32) * 48), vb = trrd(vp + (KT * 32 + 8) * 48), vc = trrd(vp + (KT * 32 + 16) * 48), vd = trrd(vp + (KT * 32 + 24) * 48);
            k0 = ld128(kp + KN * 32 * 48 + 16 * hh); k1 = ld128(kp + KN * 32 * 48 + 32);
            SB();
            if (mask) {
#pragma unroll
                for (int r = 0; r < 16; ++r) { const int jj = (r & 3) + 8 * (r >> 2) + 4 * hh; const bool ok = mask == 1 ? jj <= q32 : jj > q32; s[r] = ok ? s[r] : NEGBIG; }
            }
            float m = fmaxf(fmaxf(s[0], s[1]), s[2]);
#pragma unroll
            for (int r = 3; r < 15; r += 2) m = fmaxf(fmaxf(m, s[r]), s[r + 1]);
            m = x32max(fmaxf(m, s[15]));
            if (first || __any(m > THR)) {
                const float dl = first ? m : fmaxf(m, 0.f);
                const float f = first ? 1.f : __builtin_amdgcn_exp2f(-dl);
#pragma unroll
                for (int r = 0; r < 16; ++r) { s[r] -= dl; negm[r] -= dl; if (!first) oacc[r] *= f; }
            }
            unsigned pw[8];
#pragma unroll
            for (int r = 0; r < 8; ++r) pw[r] = cvtpk(__builtin_amdgcn_exp2f(s[2 * r]), __builtin_amdgcn_exp2f(s[2 * r + 1]));
            oacc = MFMA32(cat8(va, vb), mk8(pw[0], pw[1], pw[2], pw[3]), oacc);
            oacc = MFMA32(cat8(vc, vd), mk8(pw[4], pw[5], pw[6], pw[7]), oacc);
        };
        auto wo_piece = [&](int n) { const int pc = n * 8 + wid; if (pc < 54) { const int p = pc / 18, o = pc % 18;
            glds16_dev((unsigned)(o * 1024 + lane * 16), wot + g3_step(p) * G3_SLOT, lds0 + g3_slot(0) + p * G3_SLOT + o * 1024); } };
        const int lo = rt >= 2 ? 0 : (rt == 1 ? 4 - tg : 8 - tg);
        const int last = lo > 1 ? lo : 1;
        k0 = ld128(kp + 8 * 32 * 48 + 16 * hh); k1 = ld128(kp + 8 * 32 * 48 + 32);
        tile(8, 7, true, 1);
        int KT = 7;
        for (; KT >= last && KT >= 6; --KT) tile(KT, KT - 1, false, 0);
        WAITV(0);
        if (wid == 0) flag_confirm(fwo, fwop, tag);
        BAR();
        if (tid == 0) set_flag(F_P + (hp * 16 + rt) * 32, tag);
        STAMP(7);
        gu32* fpp = F_P + (hp * 16 + r16) * 32;
        if (rt >= 2) {
            tile(5, 4, false, 0); wo_piece(0); wo_piece(1);
            tile(4, 3, false, 0); wo_piece(2); wo_piece(3);
            tile(3, 2, false, 0); wo_piece(4); wo_piece(5); wo_piece(6);
            unsigned fp = flag_request(fpp);
            tile(2, 1, false, 0);
            WAITV(0);
            flag_confirm(fp, fpp, tag);
            pv_load();
            tile(1, 0, false, 0);
            tile(0, 0, false, 2);
        } else {
            for (; KT >= last; --KT) tile(KT, KT - 1, false, 0);
#pragma unroll
            for (int n = 0; n < 7; ++n) wo_piece(n);
            wait_flag(fpp, tag);
            pv_load();
        }
    }
    STAMP(8);
    {
        if (wid < 6) {
            const unsigned one = (r16 < 8 && (r16 & 1) == 0) ? 0x00003f80u : (r16 < 8 ? 0x3f800000u : 0u);
            const bf16x8 idn = mk8((r16 >> 1) == 0 ? one : 0u, (r16 >> 1) == 1 ? one : 0u, (r16 >> 1) == 2 ? one : 0u, (r16 >> 1) == 3 ? one : 0u);
#pragma unroll
            for (int n = 0; n < 2; ++n) {
                f32x4 sm = {0, 0, 0, 0};
#pragma unroll
                for (int m = 0; m < 4; ++m) sm = MFMA16(idn, __builtin_bit_cast(bf16x8, pv[n][m]), sm);
                const int gr = (2 * wid + n) * 16 + r16, ph = gr >= 95, e0 = gr - 95 * ph, d = e0 / 5, u = 2 * (e0 - 5 * d) + g;
                if (gr < 190 && g < 2) *(LAS u32x2*)(lds + L_KVT + ph * 2016 + d * 96 + (u / 5) * 48 + (u % 5) * 8) = u32x2{cvtpk(sm[0], sm[1]), cvtpk(sm[2], sm[3])};
            }
        } else {
            const int e0 = (wid - 6) * 64 + lane;
            if (e0 < 76) *(LAS u32x2*)(lds + L_KVT + (e0 / 38) * 2016 + ((e0 % 38) >> 1) * 96 + (e0 & 1) * 48 + 40) = u32x2{0u, 0u};
            else if (e0 < 88) *(LAS u32x4*)(lds + L_KVT + ((e0 - 76) / 6) * 2016 + 20 * 96 + ((e0 - 76) % 6) * 16) = u32x4{0, 0, 0, 0};
        }
        const int tokb = 32 * tg + q32;
        const float gb = *(const LAS float*)(lds + L_GATE + tokb * 4);
        auto sw = __builtin_amdgcn_permlane32_swap(__float_as_uint(oacc[10]), __float_as_uint(oacc[10]), false, false);
        const float sb = (1.f - gb) * __builtin_amdgcn_rcpf(__uint_as_float(sw[0]));
        const lptr ob = lds + L_OST + tokb * 144;
        *(LAS u32x2*)(ob + h * 32 + 8 * hh) = u32x2{cvtpk(oacc[0] * sb, oacc[1] * sb), cvtpk(oacc[2] * sb, oacc[3] * sb)};
        *(LAS u32x2*)(ob + h * 32 + 16 + 8 * hh) = u32x2{cvtpk(oacc[4] * sb, oacc[5] * sb), cvtpk(oacc[6] * sb, oacc[7] * sb)};
        if (hh == 0) *(LAS unsigned*)(ob + 64 + h * 4) = cvtpk(oacc[8] * sb, oacc[9] * sb);
    }
    BAR();
    STAMP(9);
    {
        f32x4 dd[3];
        const int eo = (r16 >> 2) & 1;
        const bool mine = (r16 >> 3) == (g >> 1);
        const bf16x8 b0 = mk8(phq[0], phq[1], phq[2], phq[3]), b1 = mk8(phq[4], phq[5], phq[6], phq[7]), b2 = mk8(phq[8], 0u, 0u, 0u);
        bf16x8 af[3][3];
#pragma unroll
        for (int i = 0; i < 3; ++i) {
            const int sg = 4 * i + (r16 & 3);
            int d = eo ? (sg < 8 ? 10 + sg : 20) : (sg < 10 ? sg : 20); if (sg == 10) d = 18; if (!mine) d = 20;
            const lptr ap = lds + L_KVT + h * 2016 + d * 96 + (g & 1) * 48;
            af[i][0] = ld128(ap); af[i][1] = ld128(ap + 16); af[i][2] = ld128(ap + 32);
        }
        const int tok = 32 * tg + 16 * (g >> 1) + r16;
        const float gate = *(const LAS float*)(lds + L_GATE + tok * 4);
        SB();
#pragma unroll
        for (int i = 0; i < 3; ++i) { f32x4 a = MFMA16(af[i][0], b0, (f32x4{0, 0, 0, 0})); a = MFMA16(af[i][1], b1, a); dd[i] = MFMA16(af[i][2], b2, a); }
        const float sc = gate * __builtin_amdgcn_rcpf(dd[2][2] + 1e-6f);
        const lptr op = lds + L_OST + tok * 144 + 72 + h * 36 + (g & 1) * 20;
        *(LAS unsigned*)(op) = cvtpk(dd[0][0] * sc, dd[0][1] * sc); *(LAS unsigned*)(op + 4) = cvtpk(dd[0][2] * sc, dd[0][3] * sc);
        *(LAS unsigned*)(op + 8) = cvtpk(dd[1][0] * sc, dd[1][1] * sc); *(LAS unsigned*)(op + 12) = cvtpk(dd[1][2] * sc, dd[1][3] * sc);
        if ((g & 1) == 0) *(LAS unsigned*)(op + 16) = cvtpk(dd[2][0] * sc, dd[2][1] * sc);
    }
    BAR();
    {
        u32x4 ov[3];
#pragma unroll
        for (int i = 0; i < 3; ++i) { const int e0 = i * 512 + tid, e = e0 < 1152 ? e0 : 0, R = e / 144, e1 = e % 144;
            ov[i] = *(const LAS u32x4*)(lds + L_OST + (16 * R + (e1 & 15)) * 144 + (e1 >> 4) * 16); }
#pragma unroll
        for (int i = 0; i < 3; ++i) { const int e0 = i * 512 + tid, R = e0 / 144, e1 = e0 % 144;
            if (e0 < 1152) st_dev(wsr, (unsigned)(WS_O + ((size_t)(rt * 8 + R) * 144 + 9 * hp) * 256 + e1 * 16), ov[i]); }
    }
    STAMP(10);
    {
        const int rt3 = r64 >> 1, rh = r64 & 1, kq = wid >> 1, rp = wid & 1;
        auto issue3 = [&](int p) {
            if (wid > 0) {
#pragma unroll
                for (int i = 0; i < 3; ++i) { const int pc = (wid - 1) + 7 * i;
                    if (pc < 18) glds16_dev((unsigned)(pc * 1024 + lane * 16), wot + g3_step(p) * G3_SLOT, lds0 + g3_slot(p) + pc * 1024); }
            }
        };
        DRAIN_BAR();
        if (tid == 0) set_flag(F_O + (rt * 16 + hp) * 32, tag);
        issue3(3); issue3(4); issue3(5); issue3(6); issue3(7);
        STAMP(11);
        if (wid == 0) wait_flag(F_O + (rt3 * 16 + r16) * 32, tag);
        DRAIN_BAR();
        STAMP(12);
        u32x4 of[9][2];
        const unsigned ob = (unsigned)(WS_O + ((size_t)(rt3 * 8 + 4 * rh + 2 * rp) * 144 + 36 * kq) * 256 + lane * 16);
        auto o_load = [&](int p) { const unsigned o = ob + g3_step(p) * 1024; of[p][0] = ld_dev(wsr, o); of[p][1] = ld_dev(wsr, o + 36864); };
        o_load(0); o_load(1); o_load(2);
        f32x4 a3[5][2];
#pragma unroll
        for (int t = 0; t < 5; ++t) { a3[t][0] = f32x4{0, 0, 0, 0}; a3[t][1] = f32x4{0, 0, 0, 0}; }
        const int wro = kq * 4608 + lane * 16, wr4 = kq * 4608 + 4096 + g * 128 + (r16 & 7) * 16;
        bf16x8 wf[5];
#pragma unroll
        for (int t = 0; t < 4; ++t) wf[t] = ld128(lds + g3_slot(0) + wro + t * 1024);
        wf[4] = ld128(lds + g3_slot(0) + wr4);
#pragma unroll
        for (int p = 0; p < 9; ++p) {
            if (p == 1) { BAR(); issue3(8); }
            if (p == 7) { WAITV(2); BAR(); }
            if (p + 3 < 9) o_load(p + 3);
            const lptr sp = lds + g3_slot(p + 1 < 9 ? p + 1 : 8);
            const bf16x8 bo0 = __builtin_bit_cast(bf16x8, of[p][0]), bo1 = __builtin_bit_cast(bf16x8, of[p][1]);
            SB();
#pragma unroll
            for (int t = 0; t < 5; ++t) {
                bf16x8 nw = wf[t];
                if (p + 1 < 9) nw = ld128(sp + (t < 4 ? wro + t * 1024 : wr4));
                SB();
                a3[t][0] = MFMA16(wf[t], bo0, a3[t][0]); a3[t][1] = MFMA16(wf[t], bo1, a3[t][1]);
                SB();
                wf[t] = nw;
            }
        }
        BAR();
        STAMP(13);
        const lptr rp0 = lds + rp * 40960 + lane * 16;
#pragma unroll
        for (int n = 0; n < 10; ++n) *(LAS f32x4*)(rp0 + (n * 4 + kq) * 1024) = a3[n % 5][n / 5];
        BAR();
        f32x4 pr[3][4];
#pragma unroll
        for (int j = 0; j < 3; ++j) { const int n = kq + 4 * j < 10 ? kq + 4 * j : kq;
#pragma unroll
            for (int o = 0; o < 4; ++o) pr[j][o] = *(const LAS f32x4*)(rp0 + (n * 4 + o) * 1024); }
#pragma unroll
        for (int j = 0; j < 3; ++j) { const int n = kq + 4 * j, tt = n >= 5, t = n - 5 * tt;
            const f32x4 u = (pr[j][0] + pr[j][1]) + (pr[j][2] + pr[j][3]);
            char* orow = (char*)out + (size_t)(rt3 * 128 + 64 * rh + 32 * rp + 16 * tt + r16) * 1152 + cg * 144 + g * 8 + t * 32;
            if (n < 10 && (t < 4 || g < 2)) *(u32x2*)orow = u32x2{cvtpk(u[0], u[1]), cvtpk(u[2], u[3])};
        }
        STAMP(14);
        if (b == 0 && tid == 0) set_flag((gu32*)(ws + WS_EPOCH), tag);
    }
}

extern "C" void kernel_launch(void* const* d_in, const int* in_sizes, int n_in, void* d_out, int out_size, void* d_ws, size_t ws_size, hipStream_t stream) {
    (void)hipFuncSetAttribute((const void*)fused_kernel, hipFuncAttributeMaxDynamicSharedMemorySize, LDS_BYTES);
    fused_kernel<<<256, 512, LDS_BYTES, stream>>>((const char*)d_in[0], (const char*)d_in[1], (const char*)d_in[5], (const char*)d_in[2], (const char*)d_in[3],
                                                  (const char*)d_in[6], (const char*)d_in[7], (char*)d_ws, (u16*)d_out);
}
```

```cpp
#include <hip/hip_runtime.h>
#include <hip/hip_bf16.h>
#include <stdint.h>

typedef unsigned short u16;
typedef short bf16x8 __attribute__((ext_vector_type(8)));
typedef short s16x4 __attribute__((ext_vector_type(4)));
typedef float f32x4 __attribute__((ext_vector_type(4)));
typedef float f32x16 __attribute__((ext_vector_type(16)));
typedef unsigned u32x4 __attribute__((ext_vector_type(4)));
typedef unsigned u32x2 __attribute__((ext_vector_type(2)));
typedef __bf16 bf16x2_t __attribute__((ext_vector_type(2)));
typedef float f32x2 __attribute__((ext_vector_type(2)));
#define LAS __attribute__((address_space(3)))
#define GAS __attribute__((address_space(1)))
typedef LAS char* lptr;
typedef GAS unsigned gu32;

constexpr size_t WS_EPOCH = 0;
constexpr size_t WS_FLAG = 4096;
constexpr size_t WS_WT   = 4096 + 5 * 256 * 128;
constexpr size_t WS_WOT  = WS_WT + 16 * 9 * 20480;
constexpr size_t WS_KV   = WS_WOT + 8 * 165888;
constexpr size_t WS_PART = WS_KV + 256 * 24576;
constexpr size_t WS_O    = WS_PART + 256 * 6144;
constexpr size_t WS_DBG  = WS_O + 16 * 8 * 36864;

constexpr int XS = 16384, WS1 = 20480;
constexpr int L_X = 0, L_W = 3 * XS;
constexpr int L_STG = L_W + 3 * WS1;
constexpr int L_WP = L_STG;
constexpr int L_GW = L_WP + 15 * 1024;
constexpr int L_PHI = L_STG + 24576;
constexpr int L_WOP = L_PHI + 2048;
constexpr int L_PKX = 118784;
constexpr int L_K = 0;
constexpr int L_Q = 73728;
constexpr int L_VA = 86016;
constexpr int L_PK = 98304;
constexpr int L_GATE = 147456;
constexpr int L_KVT = 147968;
constexpr int L_OST = 73728;
constexpr int LDS_BYTES = 160 * 1024;
constexpr int G3_SLOT = 18432;
__device__ __forceinline__ constexpr int g3_slot(int p) { return (p < 3 ? 5 + p : p < 8 ? p - 3 : 5) * G3_SLOT; }
constexpr unsigned SPIN_CAP = 1u << 18;
constexpr float LOG2E = 1.4426950408889634f;
constexpr float QSCALE = 0.23570226039551584f * LOG2E;
constexpr float THR = 8.f;
constexpr float NEGBIG = -1e30f;

__device__ __forceinline__ unsigned cvtpk(float lo, float hi) { const f32x2 v = {lo, hi}; return __builtin_bit_cast(unsigned, __builtin_convertvector(v, bf16x2_t)); }
__device__ __forceinline__ void glds16(unsigned voff, const void* sbase, unsigned lds_addr) {
    unsigned sv;
    asm volatile("s_mov_b32 %0, m0\n\ts_mov_b32 m0, %3\n\ts_nop 0\n\tglobal_load_lds_dwordx4 %1, %2\n\ts_mov_b32 m0, %0" : "=&s"(sv) : "v"(voff), "s"(sbase), "s"(lds_addr) : "memory");
}
__device__ __forceinline__ void glds16_dev(unsigned voff, const void* sbase, unsigned lds_addr) {
    unsigned sv;
    asm volatile("s_mov_b32 %0, m0\n\ts_mov_b32 m0, %3\n\ts_nop 0\n\tglobal_load_lds_dwordx4 %1, %2 sc1\n\ts_mov_b32 m0, %0" : "=&s"(sv) : "v"(voff), "s"(sbase), "s"(lds_addr) : "memory");
}
__device__ __forceinline__ void glds16_ptr(const void* g, unsigned lds_addr) {
    unsigned sv;
    asm volatile("s_mov_b32 %0, m0\n\ts_mov_b32 m0, %2\n\ts_nop 0\n\tglobal_load_lds_dwordx4 %1, off\n\ts_mov_b32 m0, %0" : "=&s"(sv) : "v"(g), "s"(lds_addr) : "memory");
}
#define WAITV(n) asm volatile("s_waitcnt vmcnt(" #n ")" ::: "memory")
#define WAITL() asm volatile("s_waitcnt lgkmcnt(0)" ::: "memory")
#define LGKM0() do { __builtin_amdgcn_s_waitcnt(0xC07F); asm volatile("" ::: "memory"); } while (0)
#define BAR() do { asm volatile("" ::: "memory"); __builtin_amdgcn_s_waitcnt(0xC07F); __builtin_amdgcn_s_barrier(); asm volatile("" ::: "memory"); } while (0)
#define DRAIN_BAR() do { asm volatile("s_waitcnt vmcnt(0)" ::: "memory"); BAR(); } while (0)
#define SB() __builtin_amdgcn_sched_barrier(0)
#define MFMA16(a, b, c) __builtin_amdgcn_mfma_f32_16x16x32_bf16(a, b, c, 0, 0, 0)
#define MFMA32(a, b, c) __builtin_amdgcn_mfma_f32_32x32x16_bf16(a, b, c, 0, 0, 0)
__device__ __forceinline__ bf16x8 ld128(lptr p) { return *(const LAS bf16x8*)p; }
__device__ __forceinline__ s16x4 trrd(lptr p) { typedef short v4i16_t __attribute__((ext_vector_type(4))); return __builtin_bit_cast(s16x4, __builtin_amdgcn_ds_read_tr16_b64_v4i16((LAS v4i16_t*)p)); }
__device__ __forceinline__ bf16x8 cat8(s16x4 a, s16x4 b) { return __builtin_shufflevector(a, b, 0, 1, 2, 3, 4, 5, 6, 7); }
__device__ __forceinline__ bf16x8 mk8(unsigned a, unsigned b, unsigned c, unsigned d) { u32x4 w = {a, b, c, d}; return __builtin_bit_cast(bf16x8, w); }
__device__ __forceinline__ float x16max(float x) { auto s = __builtin_amdgcn_permlane16_swap(__float_as_uint(x), __float_as_uint(x), false, false); return fmaxf(__uint_as_float(s[0]), __uint_as_float(s[1])); }
__device__ __forceinline__ float x16sum(float x) { auto s = __builtin_amdgcn_permlane16_swap(__float_as_uint(x), __float_as_uint(x), false, false); return __uint_as_float(s[0]) + __uint_as_float(s[1]); }
__device__ __forceinline__ float x32max(float x) { auto s = __builtin_amdgcn_permlane32_swap(__float_as_uint(x), __float_as_uint(x), false, false); return fmaxf(__uint_as_float(s[0]), __uint_as_float(s[1])); }
__device__ __forceinline__ void st_dev(const __amdgpu_buffer_rsrc_t rs, unsigned off, u32x4 v) { __builtin_amdgcn_raw_buffer_store_b128(v, rs, off, 0, 16); }
__device__ __forceinline__ u32x4 ld_dev(const __amdgpu_buffer_rsrc_t rs, unsigned off) { return __builtin_amdgcn_raw_buffer_load_b128(rs, off, 0, 16); }
__device__ __forceinline__ void wait_flag(gu32* p, unsigned tag) {
    for (unsigned spins = 0; spins < SPIN_CAP; ++spins) {
        const unsigned v = __hip_atomic_load(p, __ATOMIC_RELAXED, __HIP_MEMORY_SCOPE_AGENT);
        if (__all(v == tag)) break;
        __builtin_amdgcn_s_sleep(1);
    }
}
__device__ __forceinline__ void set_flag(gu32* p, unsigned tag) { __hip_atomic_store(p, tag, __ATOMIC_RELAXED, __HIP_MEMORY_SCOPE_AGENT); }
__device__ __forceinline__ u32x4 gather8(lptr p, int stride) {
    unsigned w[4];
#pragma unroll
    for (int j = 0; j < 4; ++j) w[j] = (unsigned)*(const LAS u16*)(p + 2 * j * stride) | ((unsigned)*(const LAS u16*)(p + (2 * j + 1) * stride) << 16);
    return u32x4{w[0], w[1], w[2], w[3]};
}
__device__ __forceinline__ int piece_start(int j) { return (9 * j + 1) >> 1; }

__device__ __forceinline__ unsigned flag_request(gu32* p) { unsigned v; asm volatile("global_load_dword %0, %1, off sc1" : "=v"(v) : "v"(p) : "memory"); return v; }
__device__ __forceinline__ void flag_confirm(unsigned v, gu32* p, unsigned tag) {
    asm volatile("" : "+v"(v));
    if (!__all(v == tag)) wait_flag(p, tag);
}

#define STAMP(i) do {} while (0)
#define WSTAMP(i) do {} while (0)

__device__ __forceinline__ void feature_map(const float (&p)[9], unsigned (&o)[9]) {
    float mx = fmaxf(fmaxf(p[0], p[1]), p[2]), mn = fminf(fminf(p[0], p[1]), p[2]);
#pragma unroll
    for (int i = 3; i < 9; i += 2) { mx = fmaxf(fmaxf(mx, p[i]), p[i + 1]); mn = fminf(fminf(mn, p[i]), p[i + 1]); }
    mx = x16max(mx); mn = -x16max(-mn);
    const float mxl = -mx * LOG2E, mnl = mn * LOG2E;
    float a[9], b[9], sa = 0.f, sb = 0.f;
#pragma unroll
    for (int i = 0; i < 9; ++i) { a[i] = __builtin_amdgcn_exp2f(__builtin_fmaf(p[i], LOG2E, mxl)); b[i] = __builtin_amdgcn_exp2f(__builtin_fmaf(p[i], -LOG2E, mnl)); sa += a[i]; sb += b[i]; }
    sa = x16sum(sa); sb = x16sum(sb);
    const float ra = __builtin_amdgcn_rcpf(sa), rb = __builtin_amdgcn_rcpf(sb);
#pragma unroll
    for (int i = 0; i < 9; ++i) { a[i] *= ra; b[i] *= rb; }
    o[0] = cvtpk(a[0], a[1]); o[1] = cvtpk(a[2], a[3]); o[2] = cvtpk(a[4], a[5]); o[3] = cvtpk(a[6], a[7]); o[4] = cvtpk(a[8], b[0]);
    o[5] = cvtpk(b[1], b[2]); o[6] = cvtpk(b[3], b[4]); o[7] = cvtpk(b[5], b[6]); o[8] = cvtpk(b[7], b[8]);
}

__global__ __launch_bounds__(512, 2) void fused_kernel(const char* __restrict__ x, const char* __restrict__ wA, const char* __restrict__ wB, const char* __restrict__ wphi,
                                                       const char* __restrict__ woA, const char* __restrict__ woB, const char* __restrict__ gw,
                                                       char* __restrict__ ws, u16* __restrict__ out) {
    extern __shared__ __attribute__((aligned(16))) char lds_raw[];
    const lptr lds = (lptr)lds_raw;
    const unsigned lds0 = (unsigned)(uintptr_t)lds_raw;
    const int tid = threadIdx.x, lane = tid & 63, wid = __builtin_amdgcn_readfirstlane(tid >> 6);
    const int b = blockIdx.x, hp = (b & 3) * 4 + ((b >> 3) & 3), rt = ((b >> 2) & 1) * 8 + (b >> 5);
    const bool loader = wid >= 4;
    const int lw = wid & 3;
    const int h = lw >> 1, tg = 2 * (lw & 1) + (wid >> 2);
    const int r16 = lane & 15, g = lane >> 4;
    gu32* flags = (gu32*)(ws + WS_FLAG);
    gu32* F_W = flags;
    gu32* F_WO = flags + 256 * 32;
    gu32* F_KV = flags + 512 * 32;
    gu32* F_P = flags + 768 * 32;
    gu32* F_O = flags + 1024 * 32;
    {
        unsigned long long pc0; unsigned len;
        asm volatile("s_getpc_b64 %0\nfused_code_begin:" : "=s"(pc0));
        asm volatile("s_mov_b32 %0, fused_code_end-fused_code_begin" : "=s"(len));
        if (wid == 3) {
            const unsigned j = blockIdx.x >> 3;
            if ((j + 1) * 1024 <= len) glds16(j * 1024 + lane * 16, (const void*)pc0, lds0 + 160256);
            if ((j + 33) * 1024 <= len) glds16((j + 32) * 1024 + lane * 16, (const void*)pc0, lds0 + 160256);
        }
    }
    STAMP(0);
    unsigned tagv = flag_request((gu32*)(ws + WS_EPOCH));
    const __amdgpu_buffer_rsrc_t wsr = __builtin_amdgcn_make_buffer_rsrc(ws, 0, 0x2000000, 0x00020000);

    const int G0 = piece_start(rt), nG = piece_start(rt + 1) - G0;
    const int j2 = b >> 3, cgp = b & 7, C0 = piece_start(j2), nC = piece_start(j2 + 1) - C0;
#pragma unroll
    for (int i = 0; i < 2; ++i) {
        const int pc = wid + 8 * i;
        if (pc < 15) { int c = pc * 64 + lane; if (c >= nG * 192) c = 0;
            const int row = c / 24, ch = c % 24, w = ch / 3, cc = ch % 3, sg = w >> 1;
            const char* src = (sg < 3 ? wA + sg * 1152 : wB) + (size_t)(8 * G0 + row) * 3456 + (2 * hp + (w & 1)) * 36 + (cc == 2 ? 20 : cc * 16);
            glds16_ptr(src, lds0 + L_WP + pc * 1024); }
        else glds16(G0 * 16 + (lane < nG ? lane : 0) * 16, gw, lds0 + L_GW);
    }
    unsigned xo[4], w0o[3];
    const char* w0base = wB + 1152 + (hp >> 3) * 1152 + (hp & 7) * 144;
    auto issue_x = [&](int kb, int slot) {
#pragma unroll
        for (int i = 0; i < 4; ++i) glds16(xo[i], x + kb * 128, lds0 + L_X + slot * XS + lw * 4096 + i * 1024);
    };
    auto issue_w0 = [&](int kb, int slot) {
        const char* src = w0base + (size_t)kb * 64 * 3456;
        glds16(w0o[0], src, lds0 + L_W + slot * WS1 + lw * 1024); glds16(w0o[1], src, lds0 + L_W + slot * WS1 + (lw + 4) * 1024);
        if (lw == 0) glds16(w0o[2], src, lds0 + L_W + slot * WS1 + 8192);
    };
    if (loader) {
#pragma unroll
        for (int i = 0; i < 4; ++i) { const int row = 32 * lw + 8 * i + (lane >> 3); xo[i] = (unsigned)(rt * 128 + row) * 1152u + (unsigned)(((lane & 7) ^ ((row >> 1) & 7)) << 4); }
#pragma unroll
        for (int i = 0; i < 3; ++i) { int c = (lw + 4 * i) * 64 + lane; if (c >= 576) c = 0;
            const int rho = c / 9, cc = c % 9, k = (rho & 32) + 8 * ((rho & 15) >> 2) + (rho & 3) + 4 * ((rho >> 4) & 1); w0o[i] = (unsigned)k * 3456u + cc * 16; }
        issue_x(0, 0); issue_w0(0, 0);
        issue_x(1, 1); issue_w0(1, 1);
        WAITV(6);
    } else WAITV(0);
    BAR();
    asm volatile("" : "+v"(tagv));
    const unsigned tag = __builtin_amdgcn_readfirstlane(tagv) + 1u;
    STAMP(1);
    if (loader) { issue_x(2, 2); issue_w0(2, 2); }
    auto w1_units = [&](int i) {
        const int q4 = r16 >> 2, p4 = r16 & 3;
        const int U0 = i * 16 + lw * 4 + g, U = U0 < nG * 10 ? U0 : 0, Gi = (U * 26) >> 8, ht = U - 10 * Gi, hh2 = ht >= 5, t = ht - 5 * hh2;
        const int sg = t < 4 ? (0x1032 >> (4 * t)) & 3 : p4;
        const lptr p = lds + L_WP + (8 * Gi + q4) * 384 + (sg * 2 + hh2) * 48 + (t < 4 ? p4 * 8 : 40);
        const s16x4 ra = trrd(p), rb = trrd(p + 4 * 384);
        const u32x4 gv = *(const LAS u32x4*)(lds + L_GW + Gi * 16);
        u32x4 v = __builtin_bit_cast(u32x4, cat8(ra, rb));
        const bool isg = t == 4 && r16 == 12;
        v = u32x4{isg ? gv[0] : v[0], isg ? gv[1] : v[1], isg ? gv[2] : v[2], isg ? gv[3] : v[3]};
        const int G = G0 + Gi;
        const unsigned off = (unsigned)(WS_WT + (size_t)hp * 9 * 20480) + (G >> 3) * 20480 + (ht * 2 + ((G >> 2) & 1)) * 1024 + ((G & 3) * 16 + r16) * 16;
        st_dev(wsr, U0 < nG * 10 ? off : 0xfffffff0u, v);
    };
    const char* wt = ws + WS_WT + (size_t)hp * 9 * 20480;
    auto issue_w1 = [&](int kb, int slot) {
#pragma unroll
        for (int i = 0; i < 5; ++i) glds16_dev((unsigned)(lw * 1024 + lane * 16), wt + kb * 20480 + i * 4096, lds0 + L_W + slot * WS1 + i * 4096 + lw * 1024);
    };
    unsigned pk[3][2][2];
    if (!loader) {
        {
            f32x4 a0[5][2];
#pragma unroll
            for (int t = 0; t < 5; ++t) { a0[t][0] = f32x4{0, 0, 0, 0}; a0[t][1] = f32x4{0, 0, 0, 0}; }
            const int xr = L_X + (32 * lw + r16) * 128 + ((g ^ (r16 >> 1)) << 4);
            const int q4 = r16 >> 2, p4 = r16 & 3;
            const int wr = L_W + (4 * g + q4) * 144 + p4 * 8, wr4 = L_W + (4 * g + q4) * 144 + 128 + (p4 & 1) * 8;
            bf16x8 wa[5], xa[2], wb[5], xb[2];
            unsigned fw = 0;
            xa[0] = ld128(lds + xr); xa[1] = ld128(lds + xr + 2048);
#pragma unroll
            for (int t = 0; t < 5; ++t) { const lptr p = lds + (t < 4 ? wr + t * 32 : wr4); wa[t] = cat8(trrd(p), trrd(p + 16 * 144)); }
            int slot = 0;
            for (int kb = 0; kb < 8; ++kb) {
                const lptr sx = lds + slot * XS, sw = lds + slot * WS1 + 32 * 144;
                LGKM0();
                SB();
                xb[0] = ld128(sx + (xr ^ 64)); xb[1] = ld128(sx + (xr ^ 64) + 2048);
#pragma unroll
                for (int t = 0; t < 5; ++t) { const lptr p = sw + (t < 4 ? wr + t * 32 : wr4); wb[t] = cat8(trrd(p), trrd(p + 16 * 144)); }
                SB();
#pragma unroll
                for (int t = 0; t < 5; ++t) { a0[t][0] = MFMA16(wa[t], xa[0], a0[t][0]); a0[t][1] = MFMA16(wa[t], xa[1], a0[t][1]); }
                SB();
                if (kb == 6) WAITV(0);
                if (kb == 6 && lw == 0) flag_confirm(fw, F_W + (hp * 16 + r16) * 32, tag);
                BAR();
                if (kb == 5 && lw == 0) fw = flag_request(F_W + (hp * 16 + r16) * 32);
                slot = slot == 2 ? 0 : slot + 1;
                const lptr nx = lds + slot * XS, nw = lds + slot * WS1;
                SB();
                bf16x8 xn[2], wn[5];
                xn[0] = ld128(nx + xr); xn[1] = ld128(nx + xr + 2048);
#pragma unroll
                for (int t = 0; t < 5; ++t) { const lptr p = nw + (t < 4 ? wr + t * 32 : wr4); wn[t] = cat8(trrd(p), trrd(p + 16 * 144)); }
                SB();
#pragma unroll
                for (int t = 0; t < 5; ++t) { a0[t][0] = MFMA16(wb[t], xb[0], a0[t][0]); a0[t][1] = MFMA16(wb[t], xb[1], a0[t][1]); }
                SB();
                xa[0] = xn[0]; xa[1] = xn[1];
#pragma unroll
                for (int t = 0; t < 5; ++t) wa[t] = wn[t];
            }
            {
                const lptr sx = lds + 2 * XS, sw = lds + 2 * WS1 + 32 * 144;
                LGKM0();
                SB();
                xb[0] = ld128(sx + (xr ^ 64)); xb[1] = ld128(sx + (xr ^ 64) + 2048);
#pragma unroll
                for (int t = 0; t < 5; ++t) { const lptr p = sw + (t < 4 ? wr + t * 32 : wr4); wb[t] = cat8(trrd(p), trrd(p + 16 * 144)); }
                SB();
#pragma unroll
                for (int t = 0; t < 5; ++t) { a0[t][0] = MFMA16(wa[t], xa[0], a0[t][0]); a0[t][1] = MFMA16(wa[t], xa[1], a0[t][1]); }
                SB();
                BAR();
#pragma unroll
                for (int t = 0; t < 5; ++t) { a0[t][0] = MFMA16(wb[t], xb[0], a0[t][0]); a0[t][1] = MFMA16(wb[t], xb[1], a0[t][1]); }
            }
            STAMP(2);
            const unsigned padv = hp >= 8 ? 0x00003f80u : 0u;
#pragma unroll
            for (int tt = 0; tt < 2; ++tt) {
                const int tok = 32 * lw + 16 * tt + r16;
#pragma unroll
                for (int t = 0; t < 5; ++t) {
                    const int col = 16 * t + 4 * g;
                    if (t < 4 || g < 2) {
                        const int i0 = col / 18, i1 = (col + 2) / 18;
                        *(LAS unsigned*)(lds + L_STG + i0 * 6144 + tok * 48 + (col - 18 * i0) * 2) = cvtpk(a0[t][tt][0], a0[t][tt][1]);
                        *(LAS unsigned*)(lds + L_STG + i1 * 6144 + tok * 48 + (col + 2 - 18 * i1) * 2) = cvtpk(a0[t][tt][2], a0[t][tt][3]);
                    }
                }
                *(LAS unsigned*)(lds + L_STG + g * 6144 + tok * 48 + 36) = padv;
                *(LAS u32x2*)(lds + L_STG + g * 6144 + tok * 48 + 40) = u32x2{0u, 0u};
            }
            WAITL();
            u32x4 kvv[6];
#pragma unroll
            for (int i = 0; i < 6; ++i) { const int e = i * 64 + lane, hd = e / 96, o = e % 96; kvv[i] = *(const LAS u32x4*)(lds + L_STG + hd * 6144 + 32 * lw * 48 + o * 16); }
#pragma unroll
            for (int i = 0; i < 6; ++i) { const int e = i * 64 + lane, hd = e / 96, o = e % 96;
                st_dev(wsr, (unsigned)(WS_KV + (size_t)((rt * 16 + 2 * (hp & 7) + (hd >> 1)) * 4 + (hd & 1) * 2 + (hp >> 3)) * 6144) + 32 * lw * 48 + o * 16, kvv[i]); }
        }
        BAR();
        STAMP(3);
        f32x4 acc[5][4];
#pragma unroll
        for (int t = 0; t < 5; ++t)
#pragma unroll
            for (int u = 0; u < 4; ++u) acc[t][u] = f32x4{0, 0, 0, 0};
        const int th = lw & 1;
        const int xr = L_X + (64 * th + r16) * 128 + ((g ^ (r16 >> 1)) << 4), wr = L_W + h * 10240 + lane * 16;
        bf16x8 wa[5], xa[4], wb[5], xb[4];
        unsigned fk = 0;
        gu32* fkp = F_KV + (((lane & 1) * 8 + (hp >> 1)) * 16 + (rt - (lane >> 1) % 3 >= 0 ? rt - (lane >> 1) % 3 : rt)) * 32;
#pragma unroll
        for (int u = 0; u < 4; ++u) xa[u] = ld128(lds + xr + u * 2048);
#pragma unroll
        for (int t = 0; t < 5; ++t) wa[t] = ld128(lds + wr + t * 2048);
        int slot = 0;
        for (int i = 0; i < 8; ++i) {
            const lptr sx = lds + slot * XS, sw = lds + slot * WS1;
            SB();
#pragma unroll
            for (int u = 0; u < 4; ++u) xb[u] = ld128(sx + (xr ^ 64) + u * 2048);
#pragma unroll
            for (int t = 0; t < 5; ++t) { wb[t] = ld128(sw + wr + t * 2048 + 1024); SB();
#pragma unroll
                for (int u = 0; u < 4; ++u) acc[t][u] = MFMA16(wa[t], xa[u], acc[t][u]);
                SB(); }
            if (i == 1 || i == 4) WAITV(0);
            if (i == 4 && lw == 0) flag_confirm(fk, fkp, tag);
            BAR();
            if (i == 1 && tid == 0) set_flag(F_KV + (hp * 16 + rt) * 32, tag);
            if (i == 3 && lw == 0) fk = flag_request(fkp);
            slot = slot == 2 ? 0 : slot + 1;
            const lptr nx = lds + slot * XS, nw = lds + slot * WS1;
            SB();
#pragma unroll
            for (int u = 0; u < 4; ++u) xa[u] = ld128(nx + xr + u * 2048);
#pragma unroll
            for (int t = 0; t < 5; ++t) { const bf16x8 n = ld128(nw + wr + t * 2048); SB();
#pragma unroll
                for (int u = 0; u < 4; ++u) acc[t][u] = MFMA16(wb[t], xb[u], acc[t][u]);
                SB(); wa[t] = n; }
        }
        {
            const lptr sx = lds + 2 * XS, sw = lds + 2 * WS1;
            SB();
#pragma unroll
            for (int u = 0; u < 4; ++u) xb[u] = ld128(sx + (xr ^ 64) + u * 2048);
#pragma unroll
            for (int t = 0; t < 5; ++t) { wb[t] = ld128(sw + wr + t * 2048 + 1024); SB();
#pragma unroll
                for (int u = 0; u < 4; ++u) acc[t][u] = MFMA16(wa[t], xa[u], acc[t][u]);
                SB(); }
            BAR();
#pragma unroll
            for (int t = 0; t < 5; ++t)
#pragma unroll
                for (int u = 0; u < 4; ++u) acc[t][u] = MFMA16(wb[t], xb[u], acc[t][u]);
        }
        STAMP(4);
        const lptr Qb = lds + L_Q + h * 6144, VAb = lds + L_VA + h * 6144;
#pragma unroll
        for (int u = 0; u < 4; ++u) {
            const int tok = 64 * th + 16 * u + r16;
            unsigned p2[3][2];
#pragma unroll
            for (int t = 0; t < 3; ++t) { p2[t][0] = cvtpk(acc[2 + t][u][0], acc[2 + t][u][1]); p2[t][1] = cvtpk(acc[2 + t][u][2], acc[2 + t][u][3]); }
            if (u < 2) {
#pragma unroll
                for (int t = 0; t < 3; ++t) { pk[t][u][0] = p2[t][0]; pk[t][u][1] = p2[t][1]; }
            } else {
#pragma unroll
                for (int t = 0; t < 3; ++t) *(LAS u32x2*)(lds + L_PKX + (lw * 3 + t) * 1024 + lane * 16 + (u - 2) * 8) = u32x2{p2[t][0], p2[t][1]};
            }
            *(LAS u32x2*)(VAb + tok * 48 + 8 * g) = u32x2{cvtpk(acc[0][u][0], acc[0][u][1]), cvtpk(acc[0][u][2], acc[0][u][3])};
            *(LAS u32x2*)(Qb + tok * 48 + 8 * g) = u32x2{cvtpk(acc[1][u][0] * QSCALE, acc[1][u][1] * QSCALE), cvtpk(acc[1][u][2] * QSCALE, acc[1][u][3] * QSCALE)};
            if (g == 2) *(LAS u32x2*)(VAb + tok * 48 + 32) = u32x2{p2[2][1], 0x00003f80u};
            else if (g == 3) {
                *(LAS unsigned*)(Qb + tok * 48 + 32) = cvtpk(acc[4][u][2] * QSCALE, acc[4][u][3] * QSCALE);
                if (h == 0) *(LAS float*)(lds + L_GATE + tok * 4) = __builtin_amdgcn_rcpf(1.f + __builtin_amdgcn_exp2f(-acc[4][u][0] * LOG2E));
            }
        }
    } else {
        for (int kb = 0; kb < 9; ++kb) {
            if (kb == 1) WAITV(8); else if (kb == 2) WAITV(10); else if (kb == 3) WAITV(8); else if (kb < 7) WAITV(6); else if (kb == 7) WAITV(5);
            BAR();
            if (kb == 4 && tid == 256) set_flag(F_W + (hp * 16 + rt) * 32, tag);
            if (kb == 6) {
#pragma unroll
                for (int i = 0; i < 2; ++i) { const int pc = lw + 4 * i;
                    if (pc < 6) { int c = pc * 64 + lane; if (c >= nC * 72) c = 0;
                        const int row = c / 9, cc = c % 9, C = C0 + (row >> 3), hq = C / 9, p = 8 * (C % 9) + (row & 7);
                        int br, head, d;
                        if (p < 32) { br = 1; head = p >> 4; d = p & 15; } else if (p < 36) { br = 1; head = (p >> 1) & 1; d = 16 + (p & 1); } else { br = 0; head = p >= 54; d = p - 36 - 18 * head; }
                        glds16_ptr((br ? woB : woA) + ((2 * hq + head) * 18 + d) * 1152 + cgp * 144 + cc * 16, lds0 + L_WOP + pc * 1024); }
                    else { int c = (pc - 6) * 64 + lane; if (c >= 108) c = 0; const int row = c / 3, cc = c % 3;
                        glds16((unsigned)(hp * 1296 + row * 36 + (cc == 2 ? 20 : cc * 16)), wphi, lds0 + L_PHI + (pc - 6) * 1024); } }
            }
            if (kb < 6) { issue_x(kb + 3, kb % 3); issue_w0(kb + 3, kb % 3); }
            else issue_w1(kb, kb - 6);
            if (kb < 2) { w1_units(2 * kb); w1_units(2 * kb + 1); }
        }
        WAITV(10);
        BAR();
        u32x4 hv[18];
        auto kvload = [&](int n) { const int pc = n * 4 + lw, tl = pc / 24, ar = (pc % 24) / 6, o = pc % 6, srt = rt - 2 + tl;
            return ld_dev(wsr, (unsigned)(WS_KV + (size_t)(((srt >= 0 ? srt : rt) * 16 + hp) * 4 + ar) * 6144) + o * 1024 + lane * 16); };
        for (int i = 0; i < 5; ++i) {
            if (i == 0) WAITV(5); else if (i == 1) WAITV(10); else if (i == 2) WAITV(11); else if (i == 3) WAITV(10); else WAITV(9);
            BAR();
            if (i == 4 && tid == 256) set_flag(F_WO + b * 32, tag);
            issue_x(i, i % 3); issue_w1(i, i % 3);
            if (i < 2) {
                const int q4 = r16 >> 2, p4 = r16 & 3;
                const int U0 = i * 16 + lw * 4 + g, U = U0 < nC * 5 ? U0 : 0, Ci = (U * 52) >> 8, t = U - 5 * Ci, C = C0 + Ci, kq = C / 36, s = (C % 36) >> 2, gg = C & 3;
                const lptr p = lds + L_WOP + (8 * Ci + q4) * 144 + (t < 4 ? t * 32 + p4 * 8 : 128 + (p4 & 1) * 8);
                const u32x4 v = __builtin_bit_cast(u32x4, cat8(trrd(p), trrd(p + 4 * 144)));
                const unsigned off = (unsigned)(WS_WOT + (size_t)cgp * 165888) + (s * 4 + kq) * 4608 + (t < 4 ? t * 1024 + (gg * 16 + r16) * 16 : 4096 + (gg * 8 + r16) * 16);
                st_dev(wsr, U0 < nC * 5 && (t < 4 || r16 < 8) ? off : 0xfffffff0u, v);
            }
        }
        WAITV(9); BAR();
        issue_x(5, 2); issue_w1(5, 2);
#pragma unroll
        for (int n = 0; n < 6; ++n) hv[n] = kvload(n);
        WAITV(15); BAR();
#pragma unroll
        for (int n = 6; n < 12; ++n) hv[n] = kvload(n);
        WAITV(12); BAR();
#pragma unroll
        for (int n = 12; n < 18; ++n) hv[n] = kvload(n);
        BAR();
#pragma unroll
        for (int n = 0; n < 18; ++n) { const int pc = n * 4 + lw, tl = pc / 24, ar = (pc % 24) / 6, o = pc % 6;
            *(LAS u32x4*)(lds + L_K + ar * 18432 + tl * 6144 + o * 1024 + lane * 16) = hv[n]; }
    }
    BAR();
    STAMP(5);
    const int cg = (b >> 3) & 7, r64 = 4 * (b & 7) + (b >> 6);
    unsigned fwo = 0;
    gu32* fwop = F_WO + (cg + 8 * (lane & 31)) * 32;
    if (wid == 0) fwo = flag_request(fwop);
    if (loader) {
#pragma unroll
        for (int t = 0; t < 3; ++t) { const u32x4 v = *(const LAS u32x4*)(lds + L_PKX + (lw * 3 + t) * 1024 + lane * 16); pk[t][0][0] = v[0]; pk[t][0][1] = v[1]; pk[t][1][0] = v[2]; pk[t][1][1] = v[3]; }
    }
    unsigned phq[9];
    {
        const bool lo = r16 < 8;
        unsigned phk[9];
        float p[2][9];
        s16x4 R1[3], R2[3];
        {
            const int q4 = r16 >> 2, p4 = r16 & 3;
            const lptr pb = lds + L_PHI + h * 864 + q4 * 48;
#pragma unroll
            for (int i = 0; i < 3; ++i) { const int off = i < 2 ? 16 * (p4 & 1) + 8 * i : 40; R1[i] = trrd(pb + 4 * g * 48 + off); R2[i] = trrd(pb + 14 * 48 + off); }
        }
#pragma unroll
        for (int X = 0; X < 2; ++X) {
            const bf16x8 bA = mk8(pk[X][0][0], pk[X][0][1], pk[2][0][0], pk[2][0][1]);
            const bf16x8 bB = mk8(pk[X][1][0], pk[X][1][1], pk[2][1][0], pk[2][1][1]);
            f32x4 d[3];
#pragma unroll
            for (int i = 0; i < 3; ++i) {
                const u32x2 a = __builtin_bit_cast(u32x2, R1[i]), c = __builtin_bit_cast(u32x2, R2[i]);
                const unsigned f3 = g == X ? c[1] : 0u;
                const bf16x8 Flo = mk8(lo ? a[0] : 0u, lo ? a[1] : 0u, 0u, lo ? f3 : 0u), Fhi = mk8(lo ? 0u : a[0], lo ? 0u : a[1], 0u, lo ? 0u : f3);
                d[i] = MFMA16(Flo, bA, (f32x4{0, 0, 0, 0})); d[i] = MFMA16(Fhi, bB, d[i]);
            }
#pragma unroll
            for (int j = 0; j < 4; ++j) { p[X][j] = d[0][j]; p[X][4 + j] = d[1][j]; }
            p[X][8] = (g & 1) ? d[2][3] : d[2][2];
        }
        feature_map(p[1], phk);
        const lptr pkp = lds + L_PK + h * 10240 + (32 * tg + 16 * (g >> 1) + r16) * 80 + (g & 1) * 40;
        *(LAS u32x2*)(pkp) = u32x2{phk[0], phk[1]}; *(LAS u32x2*)(pkp + 8) = u32x2{phk[2], phk[3]}; *(LAS u32x2*)(pkp + 16) = u32x2{phk[4], phk[5]};
        *(LAS u32x2*)(pkp + 24) = u32x2{phk[6], phk[7]}; *(LAS u32x2*)(pkp + 32) = u32x2{phk[8], 0u};
        feature_map(p[0], phq);
    }
    BAR();
    STAMP(6);
    if (wid >= 2) {
        const int w6 = wid - 2, hh = w6 / 3, mt = w6 % 3, q4 = r16 >> 2, p4 = r16 & 3;
        int u = 4 * mt + p4; if (u > 9) u = 9;
        const lptr pa = lds + L_PK + hh * 10240 + (8 * g + q4) * 80 + u * 8;
        const lptr pb0 = lds + L_VA + hh * 6144 + (8 * g + q4) * 48 + p4 * 8;
        const lptr pb1 = lds + L_VA + hh * 6144 + (8 * g + q4) * 48 + 32;
        f32x4 d0 = {0, 0, 0, 0}, d1 = {0, 0, 0, 0};
        bf16x8 a[4], b0[4], b1[4];
#pragma unroll
        for (int ks = 0; ks < 4; ++ks) {
            a[ks] = cat8(trrd(pa + ks * 32 * 80), trrd(pa + ks * 32 * 80 + 4 * 80));
            b0[ks] = cat8(trrd(pb0 + ks * 32 * 48), trrd(pb0 + ks * 32 * 48 + 4 * 48));
            b1[ks] = cat8(trrd(pb1 + ks * 32 * 48), trrd(pb1 + ks * 32 * 48 + 4 * 48));
        }
        SB();
#pragma unroll
        for (int ks = 0; ks < 4; ++ks) { d0 = MFMA16(a[ks], b0[ks], d0); d1 = MFMA16(a[ks], b1[ks], d1); }
        const int uu = 4 * mt + g;
        const unsigned po = (unsigned)(WS_PART + (size_t)(hp * 16 + rt) * 3072 + hh * 1520);
        if (uu < 10) {
            __builtin_amdgcn_raw_buffer_store_b64(u32x2{cvtpk(d0[0], d0[1]), cvtpk(d0[2], d0[3])}, wsr, po + (r16 * 10 + uu) * 8, 0, 16);
            if (r16 < 3) __builtin_amdgcn_raw_buffer_store_b64(u32x2{cvtpk(d1[0], d1[1]), cvtpk(d1[2], d1[3])}, wsr, po + ((16 + r16) * 10 + uu) * 8, 0, 16);
        }
    }
    const int q32 = lane & 31, hh = lane >> 5;
    const char* wot = ws + WS_WOT + (size_t)cg * 165888;
    auto g3_step = [&](int p) { const int s = p + cg; return s >= 9 ? s - 9 : s; };
    f32x16 oacc;
    u32x4 pv[2][4];
    auto pv_load = [&]() {
        if (wid < 6) {
#pragma unroll
            for (int n = 0; n < 2; ++n) { const int gr = (2 * wid + n) * 16 + r16;
#pragma unroll
                for (int m = 0; m < 4; ++m) pv[n][m] = ld_dev(wsr, (unsigned)(WS_PART + (size_t)(hp * 16 + 4 * m + g) * 3072) + (gr < 190 ? gr : 0) * 16); }
        }
    };
    {
        const lptr Kb = lds + L_K + h * 36864, Vb = Kb + 18432, Qb = lds + L_Q + h * 6144;
        const bf16x8 qf0 = ld128(Qb + (32 * tg + q32) * 48 + 16 * hh);
        const unsigned q16 = *(const LAS unsigned*)(Qb + (32 * tg + q32) * 48 + 32);
        const bf16x8 qf1 = mk8(hh ? 0u : q16, 0u, 0u, 0u);
        const lptr kp = Kb + (32 * tg + q32) * 48;
        const int i16 = lane & 15, dhalf = (lane >> 4) & 1;
        const lptr vp = Vb + (32 * tg + 4 * hh + (i16 >> 2)) * 48 + (dhalf ? 32 + ((i16 & 3) ? 8 : 0) : (i16 & 3) * 8);
        f32x16 negm;
#pragma unroll
        for (int r = 0; r < 16; ++r) { oacc[r] = 0.f; negm[r] = 0.f; }
        bf16x8 k0, k1;
        auto tile = [&](int KT, int KN, bool first, int mask) {
            SB();
            f32x16 s = MFMA32(k0, qf0, negm); s = MFMA32(k1, qf1, s);
            SB();
            const s16x4 va = trrd(vp + (KT * 32) * 48), vb = trrd(vp + (KT * 32 + 8) * 48), vc = trrd(vp + (KT * 32 + 16) * 48), vd = trrd(vp + (KT * 32 + 24) * 48);
            k0 = ld128(kp + KN * 32 * 48 + 16 * hh); k1 = ld128(kp + KN * 32 * 48 + 32);
            SB();
            if (mask) {
#pragma unroll
                for (int r = 0; r < 16; ++r) { const int jj = (r & 3) + 8 * (r >> 2) + 4 * hh; const bool ok = mask == 1 ? jj <= q32 : jj > q32; s[r] = ok ? s[r] : NEGBIG; }
            }
            float m = fmaxf(fmaxf(s[0], s[1]), s[2]);
#pragma unroll
            for (int r = 3; r < 15; r += 2) m = fmaxf(fmaxf(m, s[r]), s[r + 1]);
            m = x32max(fmaxf(m, s[15]));
            if (first || __any(m > THR)) {
                const float dl = first ? m : fmaxf(m, 0.f);
                const float f = first ? 1.f : __builtin_amdgcn_exp2f(-dl);
#pragma unroll
                for (int r = 0; r < 16; ++r) { s[r] -= dl; negm[r] -= dl; if (!first) oacc[r] *= f; }
            }
            unsigned pw[8];
#pragma unroll
            for (int r = 0; r < 8; ++r) pw[r] = cvtpk(__builtin_amdgcn_exp2f(s[2 * r]), __builtin_amdgcn_exp2f(s[2 * r + 1]));
            oacc = MFMA32(cat8(va, vb), mk8(pw[0], pw[1], pw[2], pw[3]), oacc);
            oacc = MFMA32(cat8(vc, vd), mk8(pw[4], pw[5], pw[6], pw[7]), oacc);
        };
        auto wo_piece = [&](int n) { const int pc = n * 8 + wid; if (pc < 54) { const int p = pc / 18, o = pc % 18;
            glds16_dev((unsigned)(o * 1024 + lane * 16), wot + g3_step(p) * G3_SLOT, lds0 + g3_slot(0) + p * G3_SLOT + o * 1024); } };
        const int lo = rt >= 2 ? 0 : (rt == 1 ? 4 - tg : 8 - tg);
        const int last = lo > 1 ? lo : 1;
        k0 = ld128(kp + 8 * 32 * 48 + 16 * hh); k1 = ld128(kp + 8 * 32 * 48 + 32);
        tile(8, 7, true, 1);
        int KT = 7;
        for (; KT >= last && KT >= 6; --KT) tile(KT, KT - 1, false, 0);
        WAITV(0);
        if (wid == 0) flag_confirm(fwo, fwop, tag);
        BAR();
        if (tid == 0) set_flag(F_P + (hp * 16 + rt) * 32, tag);
        STAMP(7);
        gu32* fpp = F_P + (hp * 16 + r16) * 32;
        if (rt >= 2) {
            tile(5, 4, false, 0); wo_piece(0); wo_piece(1);
            tile(4, 3, false, 0); wo_piece(2); wo_piece(3);
            tile(3, 2, false, 0); wo_piece(4); wo_piece(5); wo_piece(6);
            unsigned fp = flag_request(fpp);
            tile(2, 1, false, 0);
            WAITV(0);
            flag_confirm(fp, fpp, tag);
            pv_load();
            tile(1, 0, false, 0);
            tile(0, 0, false, 2);
        } else {
            for (; KT >= last; --KT) tile(KT, KT - 1, false, 0);
#pragma unroll
            for (int n = 0; n < 7; ++n) wo_piece(n);
            wait_flag(fpp, tag);
            pv_load();
        }
    }
    STAMP(8);
    {
        if (wid < 6) {
            const unsigned one = (r16 < 8 && (r16 & 1) == 0) ? 0x00003f80u : (r16 < 8 ? 0x3f800000u : 0u);
            const bf16x8 idn = mk8((r16 >> 1) == 0 ? one : 0u, (r16 >> 1) == 1 ? one : 0u, (r16 >> 1) == 2 ? one : 0u, (r16 >> 1) == 3 ? one : 0u);
#pragma unroll
            for (int n = 0; n < 2; ++n) {
                f32x4 sm = {0, 0, 0, 0};
#pragma unroll
                for (int m = 0; m < 4; ++m) sm = MFMA16(idn, __builtin_bit_cast(bf16x8, pv[n][m]), sm);
                const int gr = (2 * wid + n) * 16 + r16, ph = gr >= 95, e0 = gr - 95 * ph, d = e0 / 5, u = 2 * (e0 - 5 * d) + g;
                if (gr < 190 && g < 2) *(LAS u32x2*)(lds + L_KVT + ph * 2016 + d * 96 + (u / 5) * 48 + (u % 5) * 8) = u32x2{cvtpk(sm[0], sm[1]), cvtpk(sm[2], sm[3])};
            }
        } else {
            const int e0 = (wid - 6) * 64 + lane;
            if (e0 < 76) *(LAS u32x2*)(lds + L_KVT + (e0 / 38) * 2016 + ((e0 % 38) >> 1) * 96 + (e0 & 1) * 48 + 40) = u32x2{0u, 0u};
            else if (e0 < 88) *(LAS u32x4*)(lds + L_KVT + ((e0 - 76) / 6) * 2016 + 20 * 96 + ((e0 - 76) % 6) * 16) = u32x4{0, 0, 0, 0};
        }
        const int tokb = 32 * tg + q32;
        const float gb = *(const LAS float*)(lds + L_GATE + tokb * 4);
        auto sw = __builtin_amdgcn_permlane32_swap(__float_as_uint(oacc[10]), __float_as_uint(oacc[10]), false, false);
        const float sb = (1.f - gb) * __builtin_amdgcn_rcpf(__uint_as_float(sw[0]));
        const lptr ob = lds + L_OST + tokb * 144;
        *(LAS u32x2*)(ob + h * 32 + 8 * hh) = u32x2{cvtpk(oacc[0] * sb, oacc[1] * sb), cvtpk(oacc[2] * sb, oacc[3] * sb)};
        *(LAS u32x2*)(ob + h * 32 + 16 + 8 * hh) = u32x2{cvtpk(oacc[4] * sb, oacc[5] * sb), cvtpk(oacc[6] * sb, oacc[7] * sb)};
        if (hh == 0) *(LAS unsigned*)(ob + 64 + h * 4) = cvtpk(oacc[8] * sb, oacc[9] * sb);
    }
    BAR();
    STAMP(9);
    {
        f32x4 dd[3];
        const int eo = (r16 >> 2) & 1;
        const bool mine = (r16 >> 3) == (g >> 1);
        const bf16x8 b0 = mk8(phq[0], phq[1], phq[2], phq[3]), b1 = mk8(phq[4], phq[5], phq[6], phq[7]), b2 = mk8(phq[8], 0u, 0u, 0u);
        bf16x8 af[3][3];
#pragma unroll
        for (int i = 0; i < 3; ++i) {
            const int sg = 4 * i + (r16 & 3);
            int d = eo ? (sg < 8 ? 10 + sg : 20) : (sg < 10 ? sg : 20); if (sg == 10) d = 18; if (!mine) d = 20;
            const lptr ap = lds + L_KVT + h * 2016 + d * 96 + (g & 1) * 48;
            af[i][0] = ld128(ap); af[i][1] = ld128(ap + 16); af[i][2] = ld128(ap + 32);
        }
        const int tok = 32 * tg + 16 * (g >> 1) + r16;
        const float gate = *(const LAS float*)(lds + L_GATE + tok * 4);
        SB();
#pragma unroll
        for (int i = 0; i < 3; ++i) { f32x4 a = MFMA16(af[i][0], b0, (f32x4{0, 0, 0, 0})); a = MFMA16(af[i][1], b1, a); dd[i] = MFMA16(af[i][2], b2, a); }
        const float sc = gate * __builtin_amdgcn_rcpf(dd[2][2] + 1e-6f);
        const lptr op = lds + L_OST + tok * 144 + 72 + h * 36 + (g & 1) * 20;
        *(LAS unsigned*)(op) = cvtpk(dd[0][0] * sc, dd[0][1] * sc); *(LAS unsigned*)(op + 4) = cvtpk(dd[0][2] * sc, dd[0][3] * sc);
        *(LAS unsigned*)(op + 8) = cvtpk(dd[1][0] * sc, dd[1][1] * sc); *(LAS unsigned*)(op + 12) = cvtpk(dd[1][2] * sc, dd[1][3] * sc);
        if ((g & 1) == 0) *(LAS unsigned*)(op + 16) = cvtpk(dd[2][0] * sc, dd[2][1] * sc);
    }
    BAR();
    {
        u32x4 ov[3];
#pragma unroll
        for (int i = 0; i < 3; ++i) { const int e0 = i * 512 + tid, e = e0 < 1152 ? e0 : 0, R = e / 144, e1 = e % 144;
            ov[i] = *(const LAS u32x4*)(lds + L_OST + (16 * R + (e1 & 15)) * 144 + (e1 >> 4) * 16); }
#pragma unroll
        for (int i = 0; i < 3; ++i) { const int e0 = i * 512 + tid, R = e0 / 144, e1 = e0 % 144;
            if (e0 < 1152) st_dev(wsr, (unsigned)(WS_O + ((size_t)(rt * 8 + R) * 144 + 9 * hp) * 256 + e1 * 16), ov[i]); }
    }
    STAMP(10);
    {
        const int rt3 = r64 >> 1, rh = r64 & 1, kq = wid >> 1, rp = wid & 1;
        auto issue3 = [&](int p) {
            if (wid > 0) {
#pragma unroll
                for (int i = 0; i < 3; ++i) { const int pc = (wid - 1) + 7 * i;
                    if (pc < 18) glds16_dev((unsigned)(pc * 1024 + lane * 16), wot + g3_step(p) * G3_SLOT, lds0 + g3_slot(p) + pc * 1024); }
            }
        };
        DRAIN_BAR();
        if (tid == 0) set_flag(F_O + (rt * 16 + hp) * 32, tag);
        issue3(3); issue3(4); issue3(5); issue3(6); issue3(7);
        STAMP(11);
        if (wid == 0) wait_flag(F_O + (rt3 * 16 + r16) * 32, tag);
        DRAIN_BAR();
        STAMP(12);
        u32x4 of[9][2];
        const unsigned ob = (unsigned)(WS_O + ((size_t)(rt3 * 8 + 4 * rh + 2 * rp) * 144 + 36 * kq) * 256 + lane * 16);
        auto o_load = [&](int p) { const unsigned o = ob + g3_step(p) * 1024; of[p][0] = ld_dev(wsr, o); of[p][1] = ld_dev(wsr, o + 36864); };
        o_load(0); o_load(1); o_load(2);
        f32x4 a3[5][2];
#pragma unroll
        for (int t = 0; t < 5; ++t) { a3[t][0] = f32x4{0, 0, 0, 0}; a3[t][1] = f32x4{0, 0, 0, 0}; }
        const int wro = kq * 4608 + lane * 16, wr4 = kq * 4608 + 4096 + g * 128 + (r16 & 7) * 16;
        bf16x8 wf[5];
#pragma unroll
        for (int t = 0; t < 4; ++t) wf[t] = ld128(lds + g3_slot(0) + wro + t * 1024);
        wf[4] = ld128(lds + g3_slot(0) + wr4);
#pragma unroll
        for (int p = 0; p < 9; ++p) {
            if (p == 1) { BAR(); issue3(8); }
            if (p == 7) { WAITV(2); BAR(); }
            if (p + 3 < 9) o_load(p + 3);
            const lptr sp = lds + g3_slot(p + 1 < 9 ? p + 1 : 8);
            const bf16x8 bo0 = __builtin_bit_cast(bf16x8, of[p][0]), bo1 = __builtin_bit_cast(bf16x8, of[p][1]);
            SB();
#pragma unroll
            for (int t = 0; t < 5; ++t) {
                bf16x8 nw = wf[t];
                if (p + 1 < 9) nw = ld128(sp + (t < 4 ? wro + t * 1024 : wr4));
                SB();
                a3[t][0] = MFMA16(wf[t], bo0, a3[t][0]); a3[t][1] = MFMA16(wf[t], bo1, a3[t][1]);
                SB();
                wf[t] = nw;
            }
        }
        BAR();
        STAMP(13);
        const lptr rp0 = lds + rp * 40960 + lane * 16;
#pragma unroll
        for (int n = 0; n < 10; ++n) *(LAS f32x4*)(rp0 + (n * 4 + kq) * 1024) = a3[n % 5][n / 5];
        BAR();
        f32x4 pr[3][4];
#pragma unroll
        for (int j = 0; j < 3; ++j) { const int n = kq + 4 * j < 10 ? kq + 4 * j : kq;
#pragma unroll
            for (int o = 0; o < 4; ++o) pr[j][o] = *(const LAS f32x4*)(rp0 + (n * 4 + o) * 1024); }
#pragma unroll
        for (int j = 0; j < 3; ++j) { const int n = kq + 4 * j, tt = n >= 5, t = n - 5 * tt;
            const f32x4 u = (pr[j][0] + pr[j][1]) + (pr[j][2] + pr[j][3]);
            char* orow = (char*)out + (size_t)(rt3 * 128 + 64 * rh + 32 * rp + 16 * tt + r16) * 1152 + cg * 144 + g * 8 + t * 32;
            if (n < 10 && (t < 4 || g < 2)) *(u32x2*)orow = u32x2{cvtpk(u[0], u[1]), cvtpk(u[2], u[3])};
        }
        STAMP(14);
        asm volatile("fused_code_end:");
        if (b == 0 && tid == 0) set_flag((gu32*)(ws + WS_EPOCH), tag);
    }
}

extern "C" void kernel_launch(void* const* d_in, const int* in_sizes, int n_in, void* d_out, int out_size, void* d_ws, size_t ws_size, hipStream_t stream) {
    (void)hipFuncSetAttribute((const void*)fused_kernel, hipFuncAttributeMaxDynamicSharedMemorySize, LDS_BYTES);
    fused_kernel<<<256, 512, LDS_BYTES, stream>>>((const char*)d_in[0], (const char*)d_in[1], (const char*)d_in[5], (const char*)d_in[2], (const char*)d_in[3],
                                                  (const char*)d_in[6], (const char*)d_in[7], (char*)d_ws, (u16*)d_out);
}
```

```cpp
#include <hip/hip_runtime.h>
#include <hip/hip_bf16.h>
#include <stdint.h>

typedef unsigned short u16;
typedef short bf16x8 __attribute__((ext_vector_type(8)));
typedef short s16x4 __attribute__((ext_vector_type(4)));
typedef float f32x4 __attribute__((ext_vector_type(4)));
typedef float f32x16 __attribute__((ext_vector_type(16)));
typedef unsigned u32x4 __attribute__((ext_vector_type(4)));
typedef unsigned u32x2 __attribute__((ext_vector_type(2)));
typedef __bf16 bf16x2_t __attribute__((ext_vector_type(2)));
typedef float f32x2 __attribute__((ext_vector_type(2)));
#define LAS __attribute__((address_space(3)))
#define GAS __attribute__((address_space(1)))
typedef LAS char* lptr;
typedef GAS unsigned gu32;

constexpr size_t WS_EPOCH = 0;
constexpr size_t WS_FLAG = 4096;
constexpr size_t WS_WT   = 4096 + 5 * 256 * 128;
constexpr size_t WS_WOT  = WS_WT + 16 * 9 * 20480;
constexpr size_t WS_KV   = WS_WOT + 8 * 165888;
constexpr size_t WS_PART = WS_KV + 256 * 24576;
constexpr size_t WS_O    = WS_PART + 256 * 6144;
constexpr size_t WS_DBG  = WS_O + 16 * 8 * 36864;

constexpr int XS = 16384, WS1 = 20480;
constexpr int L_X = 0, L_W = 3 * XS;
constexpr int L_STG = L_W + 3 * WS1;
constexpr int L_WP = L_STG;
constexpr int L_GW = L_WP + 15 * 1024;
constexpr int L_PHI = L_STG + 24576;
constexpr int L_ZERO = L_PHI + 1792;
constexpr int L_WOP = L_PHI + 2048;
constexpr int L_PKX = 118784;
constexpr int L_K = 0;
constexpr int L_Q = 73728;
constexpr int L_VA = 86016;
constexpr int L_PK = 98304;
constexpr int L_GATE = 147456;
constexpr int L_KVT = 147968;
constexpr int L_OST = 73728;
constexpr int LDS_BYTES = 160 * 1024;
constexpr int G3_SLOT = 18432;
__device__ __forceinline__ constexpr int g3_slot(int p) { return (p < 3 ? 5 + p : p < 8 ? p - 3 : 5) * G3_SLOT; }
constexpr unsigned SPIN_CAP = 1u << 18;
constexpr float LOG2E = 1.4426950408889634f;
constexpr float QSCALE = 0.23570226039551584f * LOG2E;
constexpr float THR = 8.f;
constexpr float NEGBIG = -1e30f;

__device__ __forceinline__ unsigned cvtpk(float lo, float hi) { const f32x2 v = {lo, hi}; return __builtin_bit_cast(unsigned, __builtin_convertvector(v, bf16x2_t)); }
__device__ __forceinline__ void glds16(unsigned voff, const void* sbase, unsigned lds_addr) {
    unsigned sv;
    asm volatile("s_mov_b32 %0, m0\n\ts_mov_b32 m0, %3\n\ts_nop 0\n\tglobal_load_lds_dwordx4 %1, %2\n\ts_mov_b32 m0, %0" : "=&s"(sv) : "v"(voff), "s"(sbase), "s"(lds_addr) : "memory");
}
__device__ __forceinline__ void glds16_dev(unsigned voff, const void* sbase, unsigned lds_addr) {
    unsigned sv;
    asm volatile("s_mov_b32 %0, m0\n\ts_mov_b32 m0, %3\n\ts_nop 0\n\tglobal_load_lds_dwordx4 %1, %2 sc1\n\ts_mov_b32 m0, %0" : "=&s"(sv) : "v"(voff), "s"(sbase), "s"(lds_addr) : "memory");
}
__device__ __forceinline__ void glds16_ptr(const void* g, unsigned lds_addr) {
    unsigned sv;
    asm volatile("s_mov_b32 %0, m0\n\ts_mov_b32 m0, %2\n\ts_nop 0\n\tglobal_load_lds_dwordx4 %1, off\n\ts_mov_b32 m0, %0" : "=&s"(sv) : "v"(g), "s"(lds_addr) : "memory");
}
#define WAITV(n) asm volatile("s_waitcnt vmcnt(" #n ")" ::: "memory")
#define WAITL() asm volatile("s_waitcnt lgkmcnt(0)" ::: "memory")
#define LGKM0() do { __builtin_amdgcn_s_waitcnt(0xC07F); asm volatile("" ::: "memory"); } while (0)
#define BAR() do { asm volatile("" ::: "memory"); __builtin_amdgcn_s_waitcnt(0xC07F); __builtin_amdgcn_s_barrier(); asm volatile("" ::: "memory"); } while (0)
#define DRAIN_BAR() do { asm volatile("s_waitcnt vmcnt(0)" ::: "memory"); BAR(); } while (0)
#define SB() __builtin_amdgcn_sched_barrier(0)
#define MFMA16(a, b, c) __builtin_amdgcn_mfma_f32_16x16x32_bf16(a, b, c, 0, 0, 0)
#define MFMA32(a, b, c) __builtin_amdgcn_mfma_f32_32x32x16_bf16(a, b, c, 0, 0, 0)
__device__ __forceinline__ bf16x8 ld128(lptr p) { return *(const LAS bf16x8*)p; }
__device__ __forceinline__ s16x4 trrd(lptr p) { typedef short v4i16_t __attribute__((ext_vector_type(4))); return __builtin_bit_cast(s16x4, __builtin_amdgcn_ds_read_tr16_b64_v4i16((LAS v4i16_t*)p)); }
__device__ __forceinline__ bf16x8 cat8(s16x4 a, s16x4 b) { return __builtin_shufflevector(a, b, 0, 1, 2, 3, 4, 5, 6, 7); }
__device__ __forceinline__ bf16x8 mk8(unsigned a, unsigned b, unsigned c, unsigned d) { u32x4 w = {a, b, c, d}; return __builtin_bit_cast(bf16x8, w); }
__device__ __forceinline__ float x16max(float x) { auto s = __builtin_amdgcn_permlane16_swap(__float_as_uint(x), __float_as_uint(x), false, false); return fmaxf(__uint_as_float(s[0]), __uint_as_float(s[1])); }
__device__ __forceinline__ float x16sum(float x) { auto s = __builtin_amdgcn_permlane16_swap(__float_as_uint(x), __float_as_uint(x), false, false); return __uint_as_float(s[0]) + __uint_as_float(s[1]); }
__device__ __forceinline__ float x32max(float x) { auto s = __builtin_amdgcn_permlane32_swap(__float_as_uint(x), __float_as_uint(x), false, false); return fmaxf(__uint_as_float(s[0]), __uint_as_float(s[1])); }
__device__ __forceinline__ void st_dev(const __amdgpu_buffer_rsrc_t rs, unsigned off, u32x4 v) { __builtin_amdgcn_raw_buffer_store_b128(v, rs, off, 0, 16); }
__device__ __forceinline__ u32x4 ld_dev(const __amdgpu_buffer_rsrc_t rs, unsigned off) { return __builtin_amdgcn_raw_buffer_load_b128(rs, off, 0, 16); }
__device__ __forceinline__ void wait_flag(gu32* p, unsigned tag) {
    for (unsigned spins = 0; spins < SPIN_CAP; ++spins) {
        const unsigned v = __hip_atomic_load(p, __ATOMIC_RELAXED, __HIP_MEMORY_SCOPE_AGENT);
        if (__all(v == tag)) break;
        __builtin_amdgcn_s_sleep(1);
    }
}
__device__ __forceinline__ void set_flag(gu32* p, unsigned tag) { __hip_atomic_store(p, tag, __ATOMIC_RELAXED, __HIP_MEMORY_SCOPE_AGENT); }
__device__ __forceinline__ u32x4 gather8(lptr p, int stride) {
    unsigned w[4];
#pragma unroll
    for (int j = 0; j < 4; ++j) w[j] = (unsigned)*(const LAS u16*)(p + 2 * j * stride) | ((unsigned)*(const LAS u16*)(p + (2 * j + 1) * stride) << 16);
    return u32x4{w[0], w[1], w[2], w[3]};
}
__device__ __forceinline__ int piece_start(int j) { return (9 * j + 1) >> 1; }

__device__ __forceinline__ unsigned flag_request(gu32* p) { unsigned v; asm volatile("global_load_dword %0, %1, off sc1" : "=v"(v) : "v"(p) : "memory"); return v; }
__device__ __forceinline__ void flag_confirm(unsigned v, gu32* p, unsigned tag) {
    asm volatile("" : "+v"(v));
    if (!__all(v == tag)) wait_flag(p, tag);
}

#define STAMP(i) do {} while (0)
#define WSTAMP(i) do {} while (0)

__device__ __forceinline__ void feature_map(const float (&p)[9], unsigned (&o)[9]) {
    float mx = fmaxf(fmaxf(p[0], p[1]), p[2]), mn = fminf(fminf(p[0], p[1]), p[2]);
#pragma unroll
    for (int i = 3; i < 9; i += 2) { mx = fmaxf(fmaxf(mx, p[i]), p[i + 1]); mn = fminf(fminf(mn, p[i]), p[i + 1]); }
    mx = x16max(mx); mn = -x16max(-mn);
    const float mxl = -mx * LOG2E, mnl = mn * LOG2E;
    float a[9], b[9], sa = 0.f, sb = 0.f;
#pragma unroll
    for (int i = 0; i < 9; ++i) { a[i] = __builtin_amdgcn_exp2f(__builtin_fmaf(p[i], LOG2E, mxl)); b[i] = __builtin_amdgcn_exp2f(__builtin_fmaf(p[i], -LOG2E, mnl)); sa += a[i]; sb += b[i]; }
    sa = x16sum(sa); sb = x16sum(sb);
    const float ra = __builtin_amdgcn_rcpf(sa), rb = __builtin_amdgcn_rcpf(sb);
#pragma unroll
    for (int i = 0; i < 9; ++i) { a[i] *= ra; b[i] *= rb; }
    o[0] = cvtpk(a[0], a[1]); o[1] = cvtpk(a[2], a[3]); o[2] = cvtpk(a[4], a[5]); o[3] = cvtpk(a[6], a[7]); o[4] = cvtpk(a[8], b[0]);
    o[5] = cvtpk(b[1], b[2]); o[6] = cvtpk(b[3], b[4]); o[7] = cvtpk(b[5], b[6]); o[8] = cvtpk(b[7], b[8]);
}

__global__ __launch_bounds__(512, 2) void fused_kernel(const char* __restrict__ x, const char* __restrict__ wA, const char* __restrict__ wB, const char* __restrict__ wphi,
                                                       const char* __restrict__ woA, const char* __restrict__ woB, const char* __restrict__ gw,
                                                       char* __restrict__ ws, u16* __restrict__ out) {
    extern __shared__ __attribute__((aligned(16))) char lds_raw[];
    const lptr lds = (lptr)lds_raw;
    const unsigned lds0 = (unsigned)(uintptr_t)lds_raw;
    const int tid = threadIdx.x, lane = tid & 63, wid = __builtin_amdgcn_readfirstlane(tid >> 6);
    const int b = blockIdx.x, hp = (b & 3) * 4 + ((b >> 3) & 3), rt = ((b >> 2) & 1) * 8 + (b >> 5);
    const bool loader = wid >= 4;
    const int lw = wid & 3;
    const int h = lw >> 1, tg = 2 * (lw & 1) + (wid >> 2);
    const int r16 = lane & 15, g = lane >> 4;
    gu32* flags = (gu32*)(ws + WS_FLAG);
    gu32* F_W = flags;
    gu32* F_WO = flags + 256 * 32;
    gu32* F_KV = flags + 512 * 32;
    gu32* F_P = flags + 768 * 32;
    gu32* F_O = flags + 1024 * 32;
    {
        unsigned long long pc0; unsigned len;
        asm volatile("s_getpc_b64 %0\nfused_code_begin:" : "=s"(pc0));
        asm volatile("s_mov_b32 %0, fused_code_end-fused_code_begin" : "=s"(len));
        if (wid == 3) {
            const unsigned j = blockIdx.x >> 3;
            if ((j + 1) * 1024 <= len) glds16(j * 1024 + lane * 16, (const void*)pc0, lds0 + 160256);
            if ((j + 33) * 1024 <= len) glds16((j + 32) * 1024 + lane * 16, (const void*)pc0, lds0 + 160256);
        }
    }
    STAMP(0);
    unsigned tagv = flag_request((gu32*)(ws + WS_EPOCH));
    const __amdgpu_buffer_rsrc_t wsr = __builtin_amdgcn_make_buffer_rsrc(ws, 0, 0x2000000, 0x00020000);

    const int G0 = piece_start(rt), nG = piece_start(rt + 1) - G0;
    const int j2 = b >> 3, cgp = b & 7, C0 = piece_start(j2), nC = piece_start(j2 + 1) - C0;
#pragma unroll
    for (int i = 0; i < 2; ++i) {
        const int pc = wid + 8 * i;
        if (pc < 15) { int c = pc * 64 + lane; if (c >= nG * 192) c = 0;
            const int row = c / 24, ch = c % 24, w = ch / 3, cc = ch % 3, sg = w >> 1;
            const char* src = (sg < 3 ? wA + sg * 1152 : wB) + (size_t)(8 * G0 + row) * 3456 + (2 * hp + (w & 1)) * 36 + (cc == 2 ? 20 : cc * 16);
            glds16_ptr(src, lds0 + L_WP + pc * 1024); }
        else glds16(G0 * 16 + (lane < nG ? lane : 0) * 16, gw, lds0 + L_GW);
    }
    unsigned xo[4], w0o[3];
    const char* w0base = wB + 1152 + (hp >> 3) * 1152 + (hp & 7) * 144;
    auto issue_x = [&](int kb, int slot) {
#pragma unroll
        for (int i = 0; i < 4; ++i) glds16(xo[i], x + kb * 128, lds0 + L_X + slot * XS + lw * 4096 + i * 1024);
    };
    auto issue_w0 = [&](int kb, int slot) {
        const char* src = w0base + (size_t)kb * 64 * 3456;
        glds16(w0o[0], src, lds0 + L_W + slot * WS1 + lw * 1024); glds16(w0o[1], src, lds0 + L_W + slot * WS1 + (lw + 4) * 1024);
        if (lw == 0) glds16(w0o[2], src, lds0 + L_W + slot * WS1 + 8192);
    };
    if (loader) {
#pragma unroll
        for (int i = 0; i < 4; ++i) { const int row = 32 * lw + 8 * i + (lane >> 3); xo[i] = (unsigned)(rt * 128 + row) * 1152u + (unsigned)(((lane & 7) ^ ((row >> 1) & 7)) << 4); }
#pragma unroll
        for (int i = 0; i < 3; ++i) { int c = (lw + 4 * i) * 64 + lane; if (c >= 576) c = 0;
            const int rho = c / 9, cc = c % 9, k = (rho & 32) + 8 * ((rho & 15) >> 2) + (rho & 3) + 4 * ((rho >> 4) & 1); w0o[i] = (unsigned)k * 3456u + cc * 16; }
        issue_x(0, 0); issue_w0(0, 0);
        issue_x(1, 1); issue_w0(1, 1);
        WAITV(6);
    } else WAITV(0);
    BAR();
    asm volatile("" : "+v"(tagv));
    const unsigned tag = __builtin_amdgcn_readfirstlane(tagv) + 1u;
    STAMP(1);
    if (loader) { issue_x(2, 2); issue_w0(2, 2); }
    auto w1_units = [&](int i) {
        const int q4 = r16 >> 2, p4 = r16 & 3;
        const int U0 = i * 16 + lw * 4 + g, U = U0 < nG * 10 ? U0 : 0, Gi = (U * 26) >> 8, ht = U - 10 * Gi, hh2 = ht >= 5, t = ht - 5 * hh2;
        const int sg = t < 4 ? (0x1032 >> (4 * t)) & 3 : p4;
        const lptr p = lds + L_WP + (8 * Gi + q4) * 384 + (sg * 2 + hh2) * 48 + (t < 4 ? p4 * 8 : 40);
        const s16x4 ra = trrd(p), rb = trrd(p + 4 * 384);
        const u32x4 gv = *(const LAS u32x4*)(lds + L_GW + Gi * 16);
        u32x4 v = __builtin_bit_cast(u32x4, cat8(ra, rb));
        const bool isg = t == 4 && r16 == 12;
        v = u32x4{isg ? gv[0] : v[0], isg ? gv[1] : v[1], isg ? gv[2] : v[2], isg ? gv[3] : v[3]};
        const int G = G0 + Gi;
        const unsigned off = (unsigned)(WS_WT + (size_t)hp * 9 * 20480) + (G >> 3) * 20480 + (ht * 2 + ((G >> 2) & 1)) * 1024 + ((G & 3) * 16 + r16) * 16;
        st_dev(wsr, U0 < nG * 10 ? off : 0xfffffff0u, v);
    };
    const char* wt = ws + WS_WT + (size_t)hp * 9 * 20480;
    auto issue_w1 = [&](int kb, int slot) {
#pragma unroll
        for (int i = 0; i < 5; ++i) glds16_dev((unsigned)(lw * 1024 + lane * 16), wt + kb * 20480 + i * 4096, lds0 + L_W + slot * WS1 + i * 4096 + lw * 1024);
    };
    unsigned pk[3][2][2];
    if (!loader) {
        {
            f32x4 a0[5][2];
#pragma unroll
            for (int t = 0; t < 5; ++t) { a0[t][0] = f32x4{0, 0, 0, 0}; a0[t][1] = f32x4{0, 0, 0, 0}; }
            const int xr = L_X + (32 * lw + r16) * 128 + ((g ^ (r16 >> 1)) << 4);
            const int q4 = r16 >> 2, p4 = r16 & 3;
            const int wr = L_W + (4 * g + q4) * 144 + p4 * 8, wr4 = L_W + (4 * g + q4) * 144 + 128 + (p4 & 1) * 8;
            bf16x8 wa[5], xa[2], wb[5], xb[2];
            unsigned fw = 0;
            xa[0] = ld128(lds + xr); xa[1] = ld128(lds + xr + 2048);
#pragma unroll
            for (int t = 0; t < 5; ++t) { const lptr p = lds + (t < 4 ? wr + t * 32 : wr4); wa[t] = cat8(trrd(p), trrd(p + 16 * 144)); }
            int slot = 0;
            for (int kb = 0; kb < 8; ++kb) {
                const lptr sx = lds + slot * XS, sw = lds + slot * WS1 + 32 * 144;
                LGKM0();
                SB();
                xb[0] = ld128(sx + (xr ^ 64)); xb[1] = ld128(sx + (xr ^ 64) + 2048);
#pragma unroll
                for (int t = 0; t < 5; ++t) { const lptr p = sw + (t < 4 ? wr + t * 32 : wr4); wb[t] = cat8(trrd(p), trrd(p + 16 * 144)); }
                SB();
#pragma unroll
                for (int t = 0; t < 5; ++t) { a0[t][0] = MFMA16(wa[t], xa[0], a0[t][0]); a0[t][1] = MFMA16(wa[t], xa[1], a0[t][1]); }
                SB();
                if (kb == 6 && lw == 0) { WAITV(0); flag_confirm(fw, F_W + (hp * 16 + r16) * 32, tag); }
                BAR();
                if (kb == 5 && lw == 0) fw = flag_request(F_W + (hp * 16 + r16) * 32);
                slot = slot == 2 ? 0 : slot + 1;
                const lptr nx = lds + slot * XS, nw = lds + slot * WS1;
                SB();
                bf16x8 xn[2], wn[5];
                xn[0] = ld128(nx + xr); xn[1] = ld128(nx + xr + 2048);
#pragma unroll
                for (int t = 0; t < 5; ++t) { const lptr p = nw + (t < 4 ? wr + t * 32 : wr4); wn[t] = cat8(trrd(p), trrd(p + 16 * 144)); }
                SB();
#pragma unroll
                for (int t = 0; t < 5; ++t) { a0[t][0] = MFMA16(wb[t], xb[0], a0[t][0]); a0[t][1] = MFMA16(wb[t], xb[1], a0[t][1]); }
                SB();
                xa[0] = xn[0]; xa[1] = xn[1];
#pragma unroll
                for (int t = 0; t < 5; ++t) wa[t] = wn[t];
            }
            {
                const lptr sx = lds + 2 * XS, sw = lds + 2 * WS1 + 32 * 144;
                LGKM0();
                SB();
                xb[0] = ld128(sx + (xr ^ 64)); xb[1] = ld128(sx + (xr ^ 64) + 2048);
#pragma unroll
                for (int t = 0; t < 5; ++t) { const lptr p = sw + (t < 4 ? wr + t * 32 : wr4); wb[t] = cat8(trrd(p), trrd(p + 16 * 144)); }
                SB();
#pragma unroll
                for (int t = 0; t < 5; ++t) { a0[t][0] = MFMA16(wa[t], xa[0], a0[t][0]); a0[t][1] = MFMA16(wa[t], xa[1], a0[t][1]); }
                SB();
                BAR();
#pragma unroll
                for (int t = 0; t < 5; ++t) { a0[t][0] = MFMA16(wb[t], xb[0], a0[t][0]); a0[t][1] = MFMA16(wb[t], xb[1], a0[t][1]); }
            }
            STAMP(2);
            const unsigned padv = hp >= 8 ? 0x00003f80u : 0u;
#pragma unroll
            for (int tt = 0; tt < 2; ++tt) {
                const int tok = 32 * lw + 16 * tt + r16;
#pragma unroll
                for (int t = 0; t < 5; ++t) {
                    const int col = 16 * t + 4 * g;
                    if (t < 4 || g < 2) {
                        const int i0 = col / 18, i1 = (col + 2) / 18;
                        *(LAS unsigned*)(lds + L_STG + i0 * 6144 + tok * 48 + (col - 18 * i0) * 2) = cvtpk(a0[t][tt][0], a0[t][tt][1]);
                        *(LAS unsigned*)(lds + L_STG + i1 * 6144 + tok * 48 + (col + 2 - 18 * i1) * 2) = cvtpk(a0[t][tt][2], a0[t][tt][3]);
                    }
                }
                *(LAS unsigned*)(lds + L_STG + g * 6144 + tok * 48 + 36) = padv;
                *(LAS u32x2*)(lds + L_STG + g * 6144 + tok * 48 + 40) = u32x2{0u, 0u};
            }
            LGKM0();
            u32x4 kvv[6];
#pragma unroll
            for (int i = 0; i < 6; ++i) { const int e = i * 64 + lane, hd = e / 96, o = e % 96; kvv[i] = *(const LAS u32x4*)(lds + L_STG + hd * 6144 + 32 * lw * 48 + o * 16); }
#pragma unroll
            for (int i = 0; i < 6; ++i) { const int e = i * 64 + lane, hd = e / 96, o = e % 96;
                st_dev(wsr, (unsigned)(WS_KV + (size_t)((rt * 16 + 2 * (hp & 7) + (hd >> 1)) * 4 + (hd & 1) * 2 + (hp >> 3)) * 6144) + 32 * lw * 48 + o * 16, kvv[i]); }
        }
        BAR();
        STAMP(3);
        f32x4 acc[5][4];
#pragma unroll
        for (int t = 0; t < 5; ++t)
#pragma unroll
            for (int u = 0; u < 4; ++u) acc[t][u] = f32x4{0, 0, 0, 0};
        const int th = lw & 1;
        const int xr = L_X + (64 * th + r16) * 128 + ((g ^ (r16 >> 1)) << 4), wr = L_W + h * 10240 + lane * 16;
        bf16x8 wa[5], xa[4], wb[5], xb[4];
        unsigned fk = 0;
        gu32* fkp = F_KV + (((lane & 1) * 8 + (hp >> 1)) * 16 + (rt - (lane >> 1) % 3 >= 0 ? rt - (lane >> 1) % 3 : rt)) * 32;
#pragma unroll
        for (int u = 0; u < 4; ++u) xa[u] = ld128(lds + xr + u * 2048);
#pragma unroll
        for (int t = 0; t < 5; ++t) wa[t] = ld128(lds + wr + t * 2048);
        int slot = 0;
        for (int i = 0; i < 8; ++i) {
            const lptr sx = lds + slot * XS, sw = lds + slot * WS1;
            SB();
#pragma unroll
            for (int u = 0; u < 4; ++u) xb[u] = ld128(sx + (xr ^ 64) + u * 2048);
#pragma unroll
            for (int t = 0; t < 5; ++t) { wb[t] = ld128(sw + wr + t * 2048 + 1024); SB();
#pragma unroll
                for (int u = 0; u < 4; ++u) acc[t][u] = MFMA16(wa[t], xa[u], acc[t][u]);
                SB(); }
            if (i == 2) WAITV(0);
            if (i == 5 && lw == 0) { WAITV(0); flag_confirm(fk, fkp, tag); }
            BAR();
            if (i == 2 && tid == 0) set_flag(F_KV + (hp * 16 + rt) * 32, tag);
            if (i == 4 && lw == 0) fk = flag_request(fkp);
            slot = slot == 2 ? 0 : slot + 1;
            const lptr nx = lds + slot * XS, nw = lds + slot * WS1;
            SB();
#pragma unroll
            for (int u = 0; u < 4; ++u) xa[u] = ld128(nx + xr + u * 2048);
#pragma unroll
            for (int t = 0; t < 5; ++t) { const bf16x8 n = ld128(nw + wr + t * 2048); SB();
#pragma unroll
                for (int u = 0; u < 4; ++u) acc[t][u] = MFMA16(wb[t], xb[u], acc[t][u]);
                SB(); wa[t] = n; }
        }
        {
            const lptr sx = lds + 2 * XS, sw = lds + 2 * WS1;
            SB();
#pragma unroll
            for (int u = 0; u < 4; ++u) xb[u] = ld128(sx + (xr ^ 64) + u * 2048);
#pragma unroll
            for (int t = 0; t < 5; ++t) { wb[t] = ld128(sw + wr + t * 2048 + 1024); SB();
#pragma unroll
                for (int u = 0; u < 4; ++u) acc[t][u] = MFMA16(wa[t], xa[u], acc[t][u]);
                SB(); }
            BAR();
#pragma unroll
            for (int t = 0; t < 5; ++t)
#pragma unroll
                for (int u = 0; u < 4; ++u) acc[t][u] = MFMA16(wb[t], xb[u], acc[t][u]);
        }
        STAMP(4);
        const lptr Qb = lds + L_Q + h * 6144, VAb = lds + L_VA + h * 6144;
#pragma unroll
        for (int u = 0; u < 4; ++u) {
            const int tok = 64 * th + 16 * u + r16;
            unsigned p2[3][2];
#pragma unroll
            for (int t = 0; t < 3; ++t) { p2[t][0] = cvtpk(acc[2 + t][u][0], acc[2 + t][u][1]); p2[t][1] = cvtpk(acc[2 + t][u][2], acc[2 + t][u][3]); }
            if (u < 2) {
#pragma unroll
                for (int t = 0; t < 3; ++t) { pk[t][u][0] = p2[t][0]; pk[t][u][1] = p2[t][1]; }
            } else {
#pragma unroll
                for (int t = 0; t < 3; ++t) *(LAS u32x2*)(lds + L_PKX + (lw * 3 + t) * 1024 + lane * 16 + (u - 2) * 8) = u32x2{p2[t][0], p2[t][1]};
            }
            *(LAS u32x2*)(VAb + tok * 48 + 8 * g) = u32x2{cvtpk(acc[0][u][0], acc[0][u][1]), cvtpk(acc[0][u][2], acc[0][u][3])};
            *(LAS u32x2*)(Qb + tok * 48 + 8 * g) = u32x2{cvtpk(acc[1][u][0] * QSCALE, acc[1][u][1] * QSCALE), cvtpk(acc[1][u][2] * QSCALE, acc[1][u][3] * QSCALE)};
            if (g == 2) *(LAS u32x2*)(VAb + tok * 48 + 32) = u32x2{p2[2][1], 0x00003f80u};
            else if (g == 3) {
                *(LAS unsigned*)(Qb + tok * 48 + 32) = cvtpk(acc[4][u][2] * QSCALE, acc[4][u][3] * QSCALE);
                if (h == 0) *(LAS float*)(lds + L_GATE + tok * 4) = __builtin_amdgcn_rcpf(1.f + __builtin_amdgcn_exp2f(-acc[4][u][0] * LOG2E));
            }
        }
    } else {
        for (int kb = 0; kb < 9; ++kb) {
            if (kb == 1) WAITV(8); else if (kb == 2) WAITV(10); else if (kb == 3) WAITV(8); else if (kb < 7) WAITV(6); else if (kb == 7) WAITV(5);
            BAR();
            if (kb == 4 && tid == 256) set_flag(F_W + (hp * 16 + rt) * 32, tag);
            if (kb < 6) { issue_x(kb + 3, kb % 3); issue_w0(kb + 3, kb % 3); }
            else issue_w1(kb, kb - 6);
            if (kb < 2) { w1_units(2 * kb); w1_units(2 * kb + 1); }
            if (kb == 7) {
#pragma unroll
                for (int i = 0; i < 2; ++i) { const int pc = lw + 4 * i;
                    if (pc < 6) { int c = pc * 64 + lane; if (c >= nC * 72) c = 0;
                        const int row = c / 9, cc = c % 9, C = C0 + (row >> 3), hq = C / 9, p = 8 * (C % 9) + (row & 7);
                        int br, head, d;
                        if (p < 32) { br = 1; head = p >> 4; d = p & 15; } else if (p < 36) { br = 1; head = (p >> 1) & 1; d = 16 + (p & 1); } else { br = 0; head = p >= 54; d = p - 36 - 18 * head; }
                        glds16_ptr((br ? woB : woA) + ((2 * hq + head) * 18 + d) * 1152 + cgp * 144 + cc * 16, lds0 + L_WOP + pc * 1024); }
                    else { int c = (pc - 6) * 64 + lane; if (c >= 108) c = 0; const int row = c / 3, cc = c % 3;
                        glds16((unsigned)(hp * 1296 + row * 36 + (cc == 2 ? 20 : cc * 16)), wphi, lds0 + L_PHI + (pc - 6) * 1024); } }
            }
        }
        WAITV(12);
        BAR();
        for (int i = 0; i < 5; ++i) {
            if (i == 0) WAITV(5); else WAITV(9);
            if (i == 1 && lw == 0 && lane < 4) *(LAS u32x4*)(lds + L_ZERO + lane * 16) = u32x4{0, 0, 0, 0};
            BAR();
            issue_x(i, i % 3); issue_w1(i, i % 3);
        }
        auto kv_copy = [&](int u) { const int ar = u / 18, tl = (u % 18) / 6, o = u % 6, srt = rt - 2 + tl;
            glds16_dev((unsigned)(WS_KV + (size_t)(((srt >= 0 ? srt : rt) * 16 + hp) * 4 + ar) * 6144) + o * 1024 + lane * 16, ws, lds0 + L_K + u * 1024); };
        WAITV(9); BAR();
        issue_x(5, 2); issue_w1(5, 2);
        WAITV(9); BAR();
#pragma unroll
        for (int n = 0; n < 9; ++n) { const int j = 4 * n + lw; kv_copy(j < 16 ? j : j + 32); }
        WAITV(9); BAR();
#pragma unroll
        for (int n = 0; n < 5; ++n) { const int j = 4 * n + lw; kv_copy(j < 16 ? 16 + j : 52 + j); }
        BAR();
#pragma unroll
        for (int n = 0; n < 4; ++n) kv_copy(32 + 4 * n + lw);
    }
    BAR();
    STAMP(5);
    const int cg = (b >> 3) & 7, r64 = 4 * (b & 7) + (b >> 6);
    unsigned fwo = 0;
    gu32* fwop = F_WO + (cg + 8 * (lane & 31)) * 32;
    {
        const int q4 = r16 >> 2, p4 = r16 & 3;
        const int U0 = wid * 4 + g, U = U0 < nC * 5 ? U0 : 0, Ci = (U * 52) >> 8, t = U - 5 * Ci, C = C0 + Ci, kq = C / 36, s = (C % 36) >> 2, gg = C & 3;
        const lptr p = lds + L_WOP + (8 * Ci + q4) * 144 + (t < 4 ? t * 32 + p4 * 8 : 128 + (p4 & 1) * 8);
        const u32x4 v = __builtin_bit_cast(u32x4, cat8(trrd(p), trrd(p + 4 * 144)));
        const unsigned off = (unsigned)(WS_WOT + (size_t)cgp * 165888) + (s * 4 + kq) * 4608 + (t < 4 ? t * 1024 + (gg * 16 + r16) * 16 : 4096 + (gg * 8 + r16) * 16);
        st_dev(wsr, U0 < nC * 5 && (t < 4 || r16 < 8) ? off : 0xfffffff0u, v);
    }
    if (loader) {
#pragma unroll
        for (int t = 0; t < 3; ++t) { const u32x4 v = *(const LAS u32x4*)(lds + L_PKX + (lw * 3 + t) * 1024 + lane * 16); pk[t][0][0] = v[0]; pk[t][0][1] = v[1]; pk[t][1][0] = v[2]; pk[t][1][1] = v[3]; }
    }
    unsigned phq[9];
    {
        unsigned phk[9];
        float p[2][9];
        const int q4 = r16 >> 2, p4 = r16 & 3;
        const lptr pw0 = lds + L_PHI + h * 864, zp = lds + L_ZERO;
        s16x4 R1[2][3];
#pragma unroll
        for (int hf = 0; hf < 2; ++hf)
#pragma unroll
            for (int i = 0; i < 3; ++i) R1[hf][i] = trrd((p4 >> 1) == hf ? pw0 + (4 * g + q4) * 48 + (i < 2 ? 16 * (p4 & 1) + 8 * i : 40) : zp);
#pragma unroll
        for (int X = 0; X < 2; ++X) {
            const bf16x8 bA = mk8(pk[X][0][0], pk[X][0][1], pk[2][0][0], pk[2][0][1]);
            const bf16x8 bB = mk8(pk[X][1][0], pk[X][1][1], pk[2][1][0], pk[2][1][1]);
            s16x4 R2[2][3];
#pragma unroll
            for (int hf = 0; hf < 2; ++hf)
#pragma unroll
                for (int i = 0; i < 3; ++i) R2[hf][i] = trrd((p4 >> 1) == hf && g == X && q4 >= 2 ? pw0 + (14 + q4) * 48 + (i < 2 ? 16 * (p4 & 1) + 8 * i : 40) : zp);
            f32x4 d[3];
#pragma unroll
            for (int i = 0; i < 3; ++i) { d[i] = MFMA16(cat8(R1[0][i], R2[0][i]), bA, (f32x4{0, 0, 0, 0})); d[i] = MFMA16(cat8(R1[1][i], R2[1][i]), bB, d[i]); }
#pragma unroll
            for (int j = 0; j < 4; ++j) { p[X][j] = d[0][j]; p[X][4 + j] = d[1][j]; }
            p[X][8] = (g & 1) ? d[2][3] : d[2][2];
        }
        feature_map(p[1], phk);
        const lptr pkp = lds + L_PK + h * 10240 + (32 * tg + 16 * (g >> 1) + r16) * 80 + (g & 1) * 40;
        *(LAS u32x2*)(pkp) = u32x2{phk[0], phk[1]}; *(LAS u32x2*)(pkp + 8) = u32x2{phk[2], phk[3]}; *(LAS u32x2*)(pkp + 16) = u32x2{phk[4], phk[5]};
        *(LAS u32x2*)(pkp + 24) = u32x2{phk[6], phk[7]}; *(LAS u32x2*)(pkp + 32) = u32x2{phk[8], 0u};
        feature_map(p[0], phq);
    }
    DRAIN_BAR();
    if (tid == 0) set_flag(F_WO + b * 32, tag);
    STAMP(6);
    if (wid >= 2) {
        const int w6 = wid - 2, hh = w6 / 3, mt = w6 % 3, q4 = r16 >> 2, p4 = r16 & 3;
        int u = 4 * mt + p4; if (u > 9) u = 9;
        const lptr pa = lds + L_PK + hh * 10240 + (8 * g + q4) * 80 + u * 8;
        const lptr pb0 = lds + L_VA + hh * 6144 + (8 * g + q4) * 48 + p4 * 8;
        const lptr pb1 = lds + L_VA + hh * 6144 + (8 * g + q4) * 48 + 32;
        f32x4 d0 = {0, 0, 0, 0}, d1 = {0, 0, 0, 0};
        bf16x8 a[4], b0[4], b1[4];
#pragma unroll
        for (int ks = 0; ks < 4; ++ks) {
            a[ks] = cat8(trrd(pa + ks * 32 * 80), trrd(pa + ks * 32 * 80 + 4 * 80));
            b0[ks] = cat8(trrd(pb0 + ks * 32 * 48), trrd(pb0 + ks * 32 * 48 + 4 * 48));
            b1[ks] = cat8(trrd(pb1 + ks * 32 * 48), trrd(pb1 + ks * 32 * 48 + 4 * 48));
        }
        SB();
#pragma unroll
        for (int ks = 0; ks < 4; ++ks) { d0 = MFMA16(a[ks], b0[ks], d0); d1 = MFMA16(a[ks], b1[ks], d1); }
        const int uu = 4 * mt + g;
        const unsigned po = (unsigned)(WS_PART + (size_t)(hp * 16 + rt) * 3072 + hh * 1520);
        if (uu < 10) {
            __builtin_amdgcn_raw_buffer_store_b64(u32x2{cvtpk(d0[0], d0[1]), cvtpk(d0[2], d0[3])}, wsr, po + (r16 * 10 + uu) * 8, 0, 16);
            if (r16 < 3) __builtin_amdgcn_raw_buffer_store_b64(u32x2{cvtpk(d1[0], d1[1]), cvtpk(d1[2], d1[3])}, wsr, po + ((16 + r16) * 10 + uu) * 8, 0, 16);
        }
    }
    const int q32 = lane & 31, hh = lane >> 5;
    const char* wot = ws + WS_WOT + (size_t)cg * 165888;
    auto g3_step = [&](int p) { const int s = p + cg; return s >= 9 ? s - 9 : s; };
    f32x16 oacc;
    u32x4 pv[3][4];
    auto pv_load = [&]() {
#pragma unroll
        for (int n = 0; n < 3; ++n) { const int gr = (3 * lw + n) * 16 + r16;
#pragma unroll
            for (int m = 0; m < 4; ++m) pv[n][m] = ld_dev(wsr, (unsigned)(WS_PART + (size_t)(hp * 16 + 4 * m + g) * 3072) + (gr < 190 ? gr : 0) * 16); }
    };
    {
        const lptr Kb = lds + L_K + h * 36864, Vb = Kb + 18432, Qb = lds + L_Q + h * 6144;
        const bf16x8 qf0 = ld128(Qb + (32 * tg + q32) * 48 + 16 * hh);
        const unsigned q16 = *(const LAS unsigned*)(Qb + (32 * tg + q32) * 48 + 32);
        const bf16x8 qf1 = mk8(hh ? 0u : q16, 0u, 0u, 0u);
        const lptr kp = Kb + (32 * tg + q32) * 48;
        const int i16 = lane & 15, dhalf = (lane >> 4) & 1;
        const lptr vp = Vb + (32 * tg + 4 * hh + (i16 >> 2)) * 48 + (dhalf ? 32 + ((i16 & 3) ? 8 : 0) : (i16 & 3) * 8);
        f32x16 negm;
#pragma unroll
        for (int r = 0; r < 16; ++r) { oacc[r] = 0.f; negm[r] = 0.f; }
        bf16x8 k0, k1;
        auto tile = [&](int KT, int KN, bool first, int mask) {
            SB();
            f32x16 s = MFMA32(k0, qf0, negm); s = MFMA32(k1, qf1, s);
            SB();
            const s16x4 va = trrd(vp + (KT * 32) * 48), vb = trrd(vp + (KT * 32 + 8) * 48), vc = trrd(vp + (KT * 32 + 16) * 48), vd = trrd(vp + (KT * 32 + 24) * 48);
            k0 = ld128(kp + KN * 32 * 48 + 16 * hh); k1 = ld128(kp + KN * 32 * 48 + 32);
            SB();
            if (mask) {
#pragma unroll
                for (int r = 0; r < 16; ++r) { const int jj = (r & 3) + 8 * (r >> 2) + 4 * hh; const bool ok = mask == 1 ? jj <= q32 : jj > q32; s[r] = ok ? s[r] : NEGBIG; }
            }
            float m = fmaxf(fmaxf(s[0], s[1]), s[2]);
#pragma unroll
            for (int r = 3; r < 15; r += 2) m = fmaxf(fmaxf(m, s[r]), s[r + 1]);
            m = x32max(fmaxf(m, s[15]));
            if (first || __any(m > THR)) {
                const float dl = first ? m : fmaxf(m, 0.f);
                const float f = first ? 1.f : __builtin_amdgcn_exp2f(-dl);
#pragma unroll
                for (int r = 0; r < 16; ++r) { s[r] -= dl; negm[r] -= dl; if (!first) oacc[r] *= f; }
            }
            unsigned pw[8];
#pragma unroll
            for (int r = 0; r < 8; ++r) pw[r] = cvtpk(__builtin_amdgcn_exp2f(s[2 * r]), __builtin_amdgcn_exp2f(s[2 * r + 1]));
            oacc = MFMA32(cat8(va, vb), mk8(pw[0], pw[1], pw[2], pw[3]), oacc);
            oacc = MFMA32(cat8(vc, vd), mk8(pw[4], pw[5], pw[6], pw[7]), oacc);
        };
        auto wo_piece = [&](int n) { const int pc = n * 4 + lw; if (pc < 54) { const int p = pc / 18, o = pc % 18;
            glds16_dev((unsigned)(o * 1024 + lane * 16), wot + g3_step(p) * G3_SLOT, lds0 + g3_slot(0) + p * G3_SLOT + o * 1024); } };
        auto wo_pieces = [&](int n0, int n1) { if (!loader) for (int n = n0; n < n1; ++n) wo_piece(n); };
        const int lo = rt >= 2 ? 0 : (rt == 1 ? 4 - tg : 8 - tg);
        const int last = lo > 1 ? lo : 1;
        k0 = ld128(kp + 8 * 32 * 48 + 16 * hh); k1 = ld128(kp + 8 * 32 * 48 + 32);
        tile(8, 7, true, 1);
        if (wid == 0) fwo = flag_request(fwop);
        int KT = 7;
        for (; KT >= last && KT >= 6; --KT) tile(KT, KT - 1, false, 0);
        WAITV(0);
        if (wid == 0) flag_confirm(fwo, fwop, tag);
        BAR();
        if (tid == 0) set_flag(F_P + (hp * 16 + rt) * 32, tag);
        STAMP(7);
        gu32* fpp = F_P + (hp * 16 + r16) * 32;
        if (rt >= 2) {
            tile(5, 4, false, 0); wo_pieces(0, 5);
            tile(4, 3, false, 0); wo_pieces(5, 10);
            unsigned fp = 0;
            if (loader) fp = flag_request(fpp);
            tile(3, 2, false, 0); wo_pieces(10, 14);
            if (loader) { WAITV(0); flag_confirm(fp, fpp, tag); pv_load(); }
            tile(2, 1, false, 0);
            tile(1, 0, false, 0);
            tile(0, 0, false, 2);
        } else {
            for (; KT >= last; --KT) tile(KT, KT - 1, false, 0);
            wo_pieces(0, 14);
            if (loader) { wait_flag(fpp, tag); pv_load(); }
        }
    }
    STAMP(8);
    {
        if (loader) {
            const unsigned one = (r16 < 8 && (r16 & 1) == 0) ? 0x00003f80u : (r16 < 8 ? 0x3f800000u : 0u);
            const bf16x8 idn = mk8((r16 >> 1) == 0 ? one : 0u, (r16 >> 1) == 1 ? one : 0u, (r16 >> 1) == 2 ? one : 0u, (r16 >> 1) == 3 ? one : 0u);
#pragma unroll
            for (int n = 0; n < 3; ++n) {
                f32x4 sm = {0, 0, 0, 0};
#pragma unroll
                for (int m = 0; m < 4; ++m) sm = MFMA16(idn, __builtin_bit_cast(bf16x8, pv[n][m]), sm);
                const int gr = (3 * lw + n) * 16 + r16, ph = gr >= 95, e0 = gr - 95 * ph, d = e0 / 5, u = 2 * (e0 - 5 * d) + g;
                if (gr < 190 && g < 2) *(LAS u32x2*)(lds + L_KVT + ph * 2016 + d * 96 + (u / 5) * 48 + (u % 5) * 8) = u32x2{cvtpk(sm[0], sm[1]), cvtpk(sm[2], sm[3])};
            }
        } else {
            const int e0 = lw * 64 + lane;
            if (e0 < 76) *(LAS u32x2*)(lds + L_KVT + (e0 / 38) * 2016 + ((e0 % 38) >> 1) * 96 + (e0 & 1) * 48 + 40) = u32x2{0u, 0u};
            else if (e0 < 88) *(LAS u32x4*)(lds + L_KVT + ((e0 - 76) / 6) * 2016 + 20 * 96 + ((e0 - 76) % 6) * 16) = u32x4{0, 0, 0, 0};
        }
        const int tokb = 32 * tg + q32;
        const float gb = *(const LAS float*)(lds + L_GATE + tokb * 4);
        auto sw = __builtin_amdgcn_permlane32_swap(__float_as_uint(oacc[10]), __float_as_uint(oacc[10]), false, false);
        const float sb = (1.f - gb) * __builtin_amdgcn_rcpf(__uint_as_float(sw[0]));
        const lptr ob = lds + L_OST + tokb * 144;
        *(LAS u32x2*)(ob + h * 32 + 8 * hh) = u32x2{cvtpk(oacc[0] * sb, oacc[1] * sb), cvtpk(oacc[2] * sb, oacc[3] * sb)};
        *(LAS u32x2*)(ob + h * 32 + 16 + 8 * hh) = u32x2{cvtpk(oacc[4] * sb, oacc[5] * sb), cvtpk(oacc[6] * sb, oacc[7] * sb)};
        if (hh == 0) *(LAS unsigned*)(ob + 64 + h * 4) = cvtpk(oacc[8] * sb, oacc[9] * sb);
    }
    BAR();
    STAMP(9);
    {
        f32x4 dd[3];
        const int eo = (r16 >> 2) & 1;
        const bool mine = (r16 >> 3) == (g >> 1);
        const bf16x8 b0 = mk8(phq[0], phq[1], phq[2], phq[3]), b1 = mk8(phq[4], phq[5], phq[6], phq[7]), b2 = mk8(phq[8], 0u, 0u, 0u);
        bf16x8 af[3][3];
#pragma unroll
        for (int i = 0; i < 3; ++i) {
            const int sg = 4 * i + (r16 & 3);
            int d = eo ? (sg < 8 ? 10 + sg : 20) : (sg < 10 ? sg : 20); if (sg == 10) d = 18; if (!mine) d = 20;
            const lptr ap = lds + L_KVT + h * 2016 + d * 96 + (g & 1) * 48;
            af[i][0] = ld128(ap); af[i][1] = ld128(ap + 16); af[i][2] = ld128(ap + 32);
        }
        const int tok = 32 * tg + 16 * (g >> 1) + r16;
        const float gate = *(const LAS float*)(lds + L_GATE + tok * 4);
        SB();
#pragma unroll
        for (int i = 0; i < 3; ++i) { f32x4 a = MFMA16(af[i][0], b0, (f32x4{0, 0, 0, 0})); a = MFMA16(af[i][1], b1, a); dd[i] = MFMA16(af[i][2], b2, a); }
        const float sc = gate * __builtin_amdgcn_rcpf(dd[2][2] + 1e-6f);
        const lptr op = lds + L_OST + tok * 144 + 72 + h * 36 + (g & 1) * 20;
        *(LAS unsigned*)(op) = cvtpk(dd[0][0] * sc, dd[0][1] * sc); *(LAS unsigned*)(op + 4) = cvtpk(dd[0][2] * sc, dd[0][3] * sc);
        *(LAS unsigned*)(op + 8) = cvtpk(dd[1][0] * sc, dd[1][1] * sc); *(LAS unsigned*)(op + 12) = cvtpk(dd[1][2] * sc, dd[1][3] * sc);
        if ((g & 1) == 0) *(LAS unsigned*)(op + 16) = cvtpk(dd[2][0] * sc, dd[2][1] * sc);
    }
    BAR();
    {
        u32x4 ov[3];
#pragma unroll
        for (int i = 0; i < 3; ++i) { const int e0 = i * 512 + tid, e = e0 < 1152 ? e0 : 0, R = e / 144, e1 = e % 144;
            ov[i] = *(const LAS u32x4*)(lds + L_OST + (16 * R + (e1 & 15)) * 144 + (e1 >> 4) * 16); }
#pragma unroll
        for (int i = 0; i < 3; ++i) { const int e0 = i * 512 + tid, R = e0 / 144, e1 = e0 % 144;
            if (e0 < 1152) st_dev(wsr, (unsigned)(WS_O + ((size_t)(rt * 8 + R) * 144 + 9 * hp) * 256 + e1 * 16), ov[i]); }
    }
    STAMP(10);
    {
        const int rt3 = r64 >> 1, rh = r64 & 1, kq = wid >> 1, rp = wid & 1;
        auto issue3 = [&](int p) {
            if (wid > 0) {
#pragma unroll
                for (int i = 0; i < 3; ++i) { const int pc = (wid - 1) + 7 * i;
                    if (pc < 18) glds16_dev((unsigned)(pc * 1024 + lane * 16), wot + g3_step(p) * G3_SLOT, lds0 + g3_slot(p) + pc * 1024); }
            }
        };
        DRAIN_BAR();
        if (tid == 0) set_flag(F_O + (rt * 16 + hp) * 32, tag);
        issue3(3); issue3(4); issue3(5); issue3(6); issue3(7);
        STAMP(11);
        if (wid == 0) wait_flag(F_O + (rt3 * 16 + r16) * 32, tag);
        DRAIN_BAR();
        STAMP(12);
        u32x4 of[9][2];
        const unsigned ob = (unsigned)(WS_O + ((size_t)(rt3 * 8 + 4 * rh + 2 * rp) * 144 + 36 * kq) * 256 + lane * 16);
        auto o_load = [&](int p) { const unsigned o = ob + g3_step(p) * 1024; of[p][0] = ld_dev(wsr, o); of[p][1] = ld_dev(wsr, o + 36864); };
        o_load(0); o_load(1); o_load(2);
        f32x4 a3[5][2];
#pragma unroll
        for (int t = 0; t < 5; ++t) { a3[t][0] = f32x4{0, 0, 0, 0}; a3[t][1] = f32x4{0, 0, 0, 0}; }
        const int wro = kq * 4608 + lane * 16, wr4 = kq * 4608 + 4096 + g * 128 + (r16 & 7) * 16;
        bf16x8 wf[5];
#pragma unroll
        for (int t = 0; t < 4; ++t) wf[t] = ld128(lds + g3_slot(0) + wro + t * 1024);
        wf[4] = ld128(lds + g3_slot(0) + wr4);
#pragma unroll
        for (int p = 0; p < 9; ++p) {
            if (p == 1) { BAR(); issue3(8); }
            if (p == 7) { WAITV(2); BAR(); }
            if (p + 3 < 9) o_load(p + 3);
            const lptr sp = lds + g3_slot(p + 1 < 9 ? p + 1 : 8);
            const bf16x8 bo0 = __builtin_bit_cast(bf16x8, of[p][0]), bo1 = __builtin_bit_cast(bf16x8, of[p][1]);
            SB();
#pragma unroll
            for (int t = 0; t < 5; ++t) {
                bf16x8 nw = wf[t];
                if (p + 1 < 9) nw = ld128(sp + (t < 4 ? wro + t * 1024 : wr4));
                SB();
                a3[t][0] = MFMA16(wf[t], bo0, a3[t][0]); a3[t][1] = MFMA16(wf[t], bo1, a3[t][1]);
                SB();
                wf[t] = nw;
            }
        }
        BAR();
        STAMP(13);
        const lptr rp0 = lds + rp * 40960 + lane * 16;
#pragma unroll
        for (int n = 0; n < 10; ++n) *(LAS f32x4*)(rp0 + (n * 4 + kq) * 1024) = a3[n % 5][n / 5];
        BAR();
        f32x4 pr[3][4];
#pragma unroll
        for (int j = 0; j < 3; ++j) { const int n = kq + 4 * j < 10 ? kq + 4 * j : kq;
#pragma unroll
            for (int o = 0; o < 4; ++o) pr[j][o] = *(const LAS f32x4*)(rp0 + (n * 4 + o) * 1024); }
#pragma unroll
        for (int j = 0; j < 3; ++j) { const int n = kq + 4 * j, tt = n >= 5, t = n - 5 * tt;
            const f32x4 u = (pr[j][0] + pr[j][1]) + (pr[j][2] + pr[j][3]);
            char* orow = (char*)out + (size_t)(rt3 * 128 + 64 * rh + 32 * rp + 16 * tt + r16) * 1152 + cg * 144 + g * 8 + t * 32;
            if (n < 10 && (t < 4 || g < 2)) *(u32x2*)orow = u32x2{cvtpk(u[0], u[1]), cvtpk(u[2], u[3])};
        }
        STAMP(14);
        asm volatile("fused_code_end:");
        if (b == 0 && tid == 0) set_flag((gu32*)(ws + WS_EPOCH), tag);
    }
}

extern "C" void kernel_launch(void* const* d_in, const int* in_sizes, int n_in, void* d_out, int out_size, void* d_ws, size_t ws_size, hipStream_t stream) {
    (void)hipFuncSetAttribute((const void*)fused_kernel, hipFuncAttributeMaxDynamicSharedMemorySize, LDS_BYTES);
    fused_kernel<<<256, 512, LDS_BYTES, stream>>>((const char*)d_in[0], (const char*)d_in[1], (const char*)d_in[5], (const char*)d_in[2], (const char*)d_in[3],
                                                  (const char*)d_in[6], (const char*)d_in[7], (char*)d_ws, (u16*)d_out);
}
```

```cpp
#include <hip/hip_runtime.h>
#include <hip/hip_bf16.h>
#include <stdint.h>

typedef unsigned short u16;
typedef short bf16x8 __attribute__((ext_vector_type(8)));
typedef short s16x4 __attribute__((ext_vector_type(4)));
typedef float f32x4 __attribute__((ext_vector_type(4)));
typedef float f32x16 __attribute__((ext_vector_type(16)));
typedef unsigned u32x4 __attribute__((ext_vector_type(4)));
typedef unsigned u32x2 __attribute__((ext_vector_type(2)));
typedef __bf16 bf16x2_t __attribute__((ext_vector_type(2)));
typedef float f32x2 __attribute__((ext_vector_type(2)));
#define LAS __attribute__((address_space(3)))
#define GAS __attribute__((address_space(1)))
typedef LAS char* lptr;
typedef GAS unsigned gu32;

constexpr size_t WS_EPOCH = 0;
constexpr size_t WS_FLAG = 4096;
constexpr size_t WS_WT   = 4096 + 5 * 256 * 128;
constexpr size_t WS_WOT  = WS_WT + 16 * 9 * 20480;
constexpr size_t WS_KV   = WS_WOT + 8 * 165888;
constexpr size_t WS_PART = WS_KV + 256 * 24576;
constexpr size_t WS_O    = WS_PART + 256 * 6144;
constexpr size_t WS_DBG  = WS_O + 16 * 8 * 36864;

constexpr int XS = 16384, WS1 = 20480;
constexpr int L_X = 0, L_W = 3 * XS;
constexpr int L_STG = L_W + 3 * WS1;
constexpr int L_WP = L_STG;
constexpr int L_GW = L_WP + 15 * 1024;
constexpr int L_PHI = L_STG + 24576;
constexpr int L_ZERO = L_PHI + 1792;
constexpr int L_WOP = L_PHI + 2048;
constexpr int L_PKX = 118784;
constexpr int L_K = 0;
constexpr int L_Q = 73728;
constexpr int L_VA = 86016;
constexpr int L_PK = 98304;
constexpr int L_GATE = 147456;
constexpr int L_KVT = 147968;
constexpr int L_OST = 73728;
constexpr int LDS_BYTES = 160 * 1024;
constexpr int G3_SLOT = 18432;
__device__ __forceinline__ constexpr int g3_slot(int p) { return (p < 3 ? 5 + p : p < 8 ? p - 3 : 5) * G3_SLOT; }
constexpr unsigned SPIN_CAP = 1u << 18;
constexpr float LOG2E = 1.4426950408889634f;
constexpr float QSCALE = 0.23570226039551584f * LOG2E;
constexpr float THR = 8.f;
constexpr float NEGBIG = -1e30f;

__device__ __forceinline__ unsigned cvtpk(float lo, float hi) { const f32x2 v = {lo, hi}; return __builtin_bit_cast(unsigned, __builtin_convertvector(v, bf16x2_t)); }
__device__ __forceinline__ void glds16(unsigned voff, const void* sbase, unsigned lds_addr) {
    unsigned sv;
    asm volatile("s_mov_b32 %0, m0\n\ts_mov_b32 m0, %3\n\ts_nop 0\n\tglobal_load_lds_dwordx4 %1, %2\n\ts_mov_b32 m0, %0" : "=&s"(sv) : "v"(voff), "s"(sbase), "s"(lds_addr) : "memory");
}
__device__ __forceinline__ void glds16_dev(unsigned voff, const void* sbase, unsigned lds_addr) {
    unsigned sv;
    asm volatile("s_mov_b32 %0, m0\n\ts_mov_b32 m0, %3\n\ts_nop 0\n\tglobal_load_lds_dwordx4 %1, %2 sc1\n\ts_mov_b32 m0, %0" : "=&s"(sv) : "v"(voff), "s"(sbase), "s"(lds_addr) : "memory");
}
__device__ __forceinline__ void glds16_ptr(const void* g, unsigned lds_addr) {
    unsigned sv;
    asm volatile("s_mov_b32 %0, m0\n\ts_mov_b32 m0, %2\n\ts_nop 0\n\tglobal_load_lds_dwordx4 %1, off\n\ts_mov_b32 m0, %0" : "=&s"(sv) : "v"(g), "s"(lds_addr) : "memory");
}
#define WAITV(n) asm volatile("s_waitcnt vmcnt(" #n ")" ::: "memory")
#define WAITL() asm volatile("s_waitcnt lgkmcnt(0)" ::: "memory")
#define LGKM0() do { __builtin_amdgcn_s_waitcnt(0xC07F); asm volatile("" ::: "memory"); } while (0)
#define BAR() do { asm volatile("" ::: "memory"); __builtin_amdgcn_s_waitcnt(0xC07F); __builtin_amdgcn_s_barrier(); asm volatile("" ::: "memory"); } while (0)
#define DRAIN_BAR() do { asm volatile("s_waitcnt vmcnt(0)" ::: "memory"); BAR(); } while (0)
#define SB() __builtin_amdgcn_sched_barrier(0)
#define MFMA16(a, b, c) __builtin_amdgcn_mfma_f32_16x16x32_bf16(a, b, c, 0, 0, 0)
#define MFMA32(a, b, c) __builtin_amdgcn_mfma_f32_32x32x16_bf16(a, b, c, 0, 0, 0)
__device__ __forceinline__ bf16x8 ld128(lptr p) { return *(const LAS bf16x8*)p; }
__device__ __forceinline__ s16x4 trrd(lptr p) { typedef short v4i16_t __attribute__((ext_vector_type(4))); return __builtin_bit_cast(s16x4, __builtin_amdgcn_ds_read_tr16_b64_v4i16((LAS v4i16_t*)p)); }
__device__ __forceinline__ bf16x8 cat8(s16x4 a, s16x4 b) { return __builtin_shufflevector(a, b, 0, 1, 2, 3, 4, 5, 6, 7); }
__device__ __forceinline__ bf16x8 mk8(unsigned a, unsigned b, unsigned c, unsigned d) { u32x4 w = {a, b, c, d}; return __builtin_bit_cast(bf16x8, w); }
__device__ __forceinline__ float x16max(float x) { auto s = __builtin_amdgcn_permlane16_swap(__float_as_uint(x), __float_as_uint(x), false, false); return fmaxf(__uint_as_float(s[0]), __uint_as_float(s[1])); }
__device__ __forceinline__ float x16sum(float x) { auto s = __builtin_amdgcn_permlane16_swap(__float_as_uint(x), __float_as_uint(x), false, false); return __uint_as_float(s[0]) + __uint_as_float(s[1]); }
__device__ __forceinline__ float x32max(float x) { auto s = __builtin_amdgcn_permlane32_swap(__float_as_uint(x), __float_as_uint(x), false, false); return fmaxf(__uint_as_float(s[0]), __uint_as_float(s[1])); }
__device__ __forceinline__ void st_dev(const __amdgpu_buffer_rsrc_t rs, unsigned off, u32x4 v) { __builtin_amdgcn_raw_buffer_store_b128(v, rs, off, 0, 16); }
__device__ __forceinline__ u32x4 ld_dev(const __amdgpu_buffer_rsrc_t rs, unsigned off) { return __builtin_amdgcn_raw_buffer_load_b128(rs, off, 0, 16); }
__device__ __forceinline__ void wait_flag(gu32* p, unsigned tag) {
    for (unsigned spins = 0; spins < SPIN_CAP; ++spins) {
        const unsigned v = __hip_atomic_load(p, __ATOMIC_RELAXED, __HIP_MEMORY_SCOPE_AGENT);
        if (__all(v == tag)) break;
        __builtin_amdgcn_s_sleep(1);
    }
}
__device__ __forceinline__ void set_flag(gu32* p, unsigned tag) { __hip_atomic_store(p, tag, __ATOMIC_RELAXED, __HIP_MEMORY_SCOPE_AGENT); }
__device__ __forceinline__ u32x4 gather8(lptr p, int stride) {
    unsigned w[4];
#pragma unroll
    for (int j = 0; j < 4; ++j) w[j] = (unsigned)*(const LAS u16*)(p + 2 * j * stride) | ((unsigned)*(const LAS u16*)(p + (2 * j + 1) * stride) << 16);
    return u32x4{w[0], w[1], w[2], w[3]};
}
__device__ __forceinline__ int piece_start(int j) { return (9 * j + 1) >> 1; }

__device__ __forceinline__ unsigned flag_request(gu32* p) { unsigned v; asm volatile("global_load_dword %0, %1, off sc1" : "=v"(v) : "v"(p) : "memory"); return v; }
__device__ __forceinline__ void flag_confirm(unsigned v, gu32* p, unsigned tag) {
    asm volatile("" : "+v"(v));
    if (!__all(v == tag)) wait_flag(p, tag);
}

#define STAMP(i) do {} while (0)
#define WSTAMP(i) do {} while (0)

__device__ __forceinline__ void feature_map(const float (&p)[9], unsigned (&o)[9]) {
    float mx = fmaxf(fmaxf(p[0], p[1]), p[2]), mn = fminf(fminf(p[0], p[1]), p[2]);
#pragma unroll
    for (int i = 3; i < 9; i += 2) { mx = fmaxf(fmaxf(mx, p[i]), p[i + 1]); mn = fminf(fminf(mn, p[i]), p[i + 1]); }
    mx = x16max(mx); mn = -x16max(-mn);
    const float mxl = -mx * LOG2E, mnl = mn * LOG2E;
    float a[9], b[9], sa = 0.f, sb = 0.f;
#pragma unroll
    for (int i = 0; i < 9; ++i) { a[i] = __builtin_amdgcn_exp2f(__builtin_fmaf(p[i], LOG2E, mxl)); b[i] = __builtin_amdgcn_exp2f(__builtin_fmaf(p[i], -LOG2E, mnl)); sa += a[i]; sb += b[i]; }
    sa = x16sum(sa); sb = x16sum(sb);
    const float ra = __builtin_amdgcn_rcpf(sa), rb = __builtin_amdgcn_rcpf(sb);
#pragma unroll
    for (int i = 0; i < 9; ++i) { a[i] *= ra; b[i] *= rb; }
    o[0] = cvtpk(a[0], a[1]); o[1] = cvtpk(a[2], a[3]); o[2] = cvtpk(a[4], a[5]); o[3] = cvtpk(a[6], a[7]); o[4] = cvtpk(a[8], b[0]);
    o[5] = cvtpk(b[1], b[2]); o[6] = cvtpk(b[3], b[4]); o[7] = cvtpk(b[5], b[6]); o[8] = cvtpk(b[7], b[8]);
}

__global__ __launch_bounds__(512, 2) void fused_kernel(const char* __restrict__ x, const char* __restrict__ wA, const char* __restrict__ wB, const char* __restrict__ wphi,
                                                       const char* __restrict__ woA, const char* __restrict__ woB, const char* __restrict__ gw,
                                                       char* __restrict__ ws, u16* __restrict__ out) {
    extern __shared__ __attribute__((aligned(16))) char lds_raw[];
    const lptr lds = (lptr)lds_raw;
    const unsigned lds0 = (unsigned)(uintptr_t)lds_raw;
    const int tid = threadIdx.x, lane = tid & 63, wid = __builtin_amdgcn_readfirstlane(tid >> 6);
    const int b = blockIdx.x, hp = (b & 3) * 4 + ((b >> 3) & 3), rt = ((b >> 2) & 1) * 8 + (b >> 5);
    const bool loader = wid >= 4;
    const int lw = wid & 3;
    const int h = lw >> 1, tg = 2 * (lw & 1) + (wid >> 2);
    const int r16 = lane & 15, g = lane >> 4;
    gu32* flags = (gu32*)(ws + WS_FLAG);
    gu32* F_W = flags;
    gu32* F_WO = flags + 256 * 32;
    gu32* F_KV = flags + 512 * 32;
    gu32* F_P = flags + 768 * 32;
    gu32* F_O = flags + 1024 * 32;
    {
        unsigned long long pc0; unsigned len;
        asm volatile("s_getpc_b64 %0\nfused_code_begin:" : "=s"(pc0));
        asm volatile("s_mov_b32 %0, fused_code_end-fused_code_begin" : "=s"(len));
        if (wid == 3) {
            const unsigned j = blockIdx.x >> 3;
            if ((j + 1) * 1024 <= len) glds16(j * 1024 + lane * 16, (const void*)pc0, lds0 + 160256);
            if ((j + 33) * 1024 <= len) glds16((j + 32) * 1024 + lane * 16, (const void*)pc0, lds0 + 160256);
        }
    }
    if (wid == 2) {
        const char* tp = ws;
        if (lane < 10) tp = ws + (size_t)lane * 2097152;
        else if (lane == 10) tp = (const char*)out;
        else if (lane == 11) tp = (const char*)out + 2359280;
        else if (lane == 12) tp = woA;
        else if (lane == 13) tp = woB;
        else if (lane == 14) tp = wphi;
        else if (lane == 15) tp = woA + 663536;
        else if (lane == 16) tp = woB + 663536;
        glds16_ptr(tp, lds0 + 161280);
    }
    STAMP(0);
    unsigned tagv = flag_request((gu32*)(ws + WS_EPOCH));
    const __amdgpu_buffer_rsrc_t wsr = __builtin_amdgcn_make_buffer_rsrc(ws, 0, 0x2000000, 0x00020000);

    const int G0 = piece_start(rt), nG = piece_start(rt + 1) - G0;
    const int j2 = b >> 3, cgp = b & 7, C0 = piece_start(j2), nC = piece_start(j2 + 1) - C0;
#pragma unroll
    for (int i = 0; i < 2; ++i) {
        const int pc = wid + 8 * i;
        if (pc < 15) { int c = pc * 64 + lane; if (c >= nG * 192) c = 0;
            const int row = c / 24, ch = c % 24, w = ch / 3, cc = ch % 3, sg = w >> 1;
            const char* src = (sg < 3 ? wA + sg * 1152 : wB) + (size_t)(8 * G0 + row) * 3456 + (2 * hp + (w & 1)) * 36 + (cc == 2 ? 20 : cc * 16);
            glds16_ptr(src, lds0 + L_WP + pc * 1024); }
        else glds16(G0 * 16 + (lane < nG ? lane : 0) * 16, gw, lds0 + L_GW);
    }
    unsigned xo[4], w0o[3];
    const char* w0base = wB + 1152 + (hp >> 3) * 1152 + (hp & 7) * 144;
    auto issue_x = [&](int kb, int slot) {
#pragma unroll
        for (int i = 0; i < 4; ++i) glds16(xo[i], x + kb * 128, lds0 + L_X + slot * XS + lw * 4096 + i * 1024);
    };
    auto issue_w0 = [&](int kb, int slot) {
        const char* src = w0base + (size_t)kb * 64 * 3456;
        glds16(w0o[0], src, lds0 + L_W + slot * WS1 + lw * 1024); glds16(w0o[1], src, lds0 + L_W + slot * WS1 + (lw + 4) * 1024);
        if (lw == 0) glds16(w0o[2], src, lds0 + L_W + slot * WS1 + 8192);
    };
    if (loader) {
#pragma unroll
        for (int i = 0; i < 4; ++i) { const int row = 32 * lw + 8 * i + (lane >> 3); xo[i] = (unsigned)(rt * 128 + row) * 1152u + (unsigned)(((lane & 7) ^ ((row >> 1) & 7)) << 4); }
#pragma unroll
        for (int i = 0; i < 3; ++i) { int c = (lw + 4 * i) * 64 + lane; if (c >= 576) c = 0;
            const int rho = c / 9, cc = c % 9, k = (rho & 32) + 8 * ((rho & 15) >> 2) + (rho & 3) + 4 * ((rho >> 4) & 1); w0o[i] = (unsigned)k * 3456u + cc * 16; }
        issue_x(0, 0); issue_w0(0, 0);
        issue_x(1, 1); issue_w0(1, 1);
        WAITV(6);
    } else WAITV(0);
    BAR();
    asm volatile("" : "+v"(tagv));
    const unsigned tag = __builtin_amdgcn_readfirstlane(tagv) + 1u;
    STAMP(1);
    if (loader) { issue_x(2, 2); issue_w0(2, 2); }
    auto w1_units = [&](int i) {
        const int q4 = r16 >> 2, p4 = r16 & 3;
        const int U0 = i * 16 + lw * 4 + g, U = U0 < nG * 10 ? U0 : 0, Gi = (U * 26) >> 8, ht = U - 10 * Gi, hh2 = ht >= 5, t = ht - 5 * hh2;
        const int sg = t < 4 ? (0x1032 >> (4 * t)) & 3 : p4;
        const lptr p = lds + L_WP + (8 * Gi + q4) * 384 + (sg * 2 + hh2) * 48 + (t < 4 ? p4 * 8 : 40);
        const s16x4 ra = trrd(p), rb = trrd(p + 4 * 384);
        const u32x4 gv = *(const LAS u32x4*)(lds + L_GW + Gi * 16);
        u32x4 v = __builtin_bit_cast(u32x4, cat8(ra, rb));
        const bool isg = t == 4 && r16 == 12;
        v = u32x4{isg ? gv[0] : v[0], isg ? gv[1] : v[1], isg ? gv[2] : v[2], isg ? gv[3] : v[3]};
        const int G = G0 + Gi;
        const unsigned off = (unsigned)(WS_WT + (size_t)hp * 9 * 20480) + (G >> 3) * 20480 + (ht * 2 + ((G >> 2) & 1)) * 1024 + ((G & 3) * 16 + r16) * 16;
        st_dev(wsr, U0 < nG * 10 ? off : 0xfffffff0u, v);
    };
    const char* wt = ws + WS_WT + (size_t)hp * 9 * 20480;
    auto issue_w1 = [&](int kb, int slot) {
#pragma unroll
        for (int i = 0; i < 5; ++i) glds16_dev((unsigned)(lw * 1024 + lane * 16), wt + kb * 20480 + i * 4096, lds0 + L_W + slot * WS1 + i * 4096 + lw * 1024);
    };
    unsigned pk[3][2][2];
    if (!loader) {
        {
            f32x4 a0[5][2];
#pragma unroll
            for (int t = 0; t < 5; ++t) { a0[t][0] = f32x4{0, 0, 0, 0}; a0[t][1] = f32x4{0, 0, 0, 0}; }
            const int xr = L_X + (32 * lw + r16) * 128 + ((g ^ (r16 >> 1)) << 4);
            const int q4 = r16 >> 2, p4 = r16 & 3;
            const int wr = L_W + (4 * g + q4) * 144 + p4 * 8, wr4 = L_W + (4 * g + q4) * 144 + 128 + (p4 & 1) * 8;
            bf16x8 wa[5], xa[2], wb[5], xb[2];
            unsigned fw = 0;
            xa[0] = ld128(lds + xr); xa[1] = ld128(lds + xr + 2048);
#pragma unroll
            for (int t = 0; t < 5; ++t) { const lptr p = lds + (t < 4 ? wr + t * 32 : wr4); wa[t] = cat8(trrd(p), trrd(p + 16 * 144)); }
            int slot = 0;
            for (int kb = 0; kb < 8; ++kb) {
                const lptr sx = lds + slot * XS, sw = lds + slot * WS1 + 32 * 144;
                LGKM0();
                SB();
                xb[0] = ld128(sx + (xr ^ 64)); xb[1] = ld128(sx + (xr ^ 64) + 2048);
#pragma unroll
                for (int t = 0; t < 5; ++t) { const lptr p = sw + (t < 4 ? wr + t * 32 : wr4); wb[t] = cat8(trrd(p), trrd(p + 16 * 144)); }
                SB();
#pragma unroll
                for (int t = 0; t < 5; ++t) { a0[t][0] = MFMA16(wa[t], xa[0], a0[t][0]); a0[t][1] = MFMA16(wa[t], xa[1], a0[t][1]); }
                SB();
                if (kb == 6 && lw == 0) { WAITV(0); flag_confirm(fw, F_W + (hp * 16 + r16) * 32, tag); }
                BAR();
                if (kb == 5 && lw == 0) fw = flag_request(F_W + (hp * 16 + r16) * 32);
                slot = slot == 2 ? 0 : slot + 1;
                const lptr nx = lds + slot * XS, nw = lds + slot * WS1;
                SB();
                bf16x8 xn[2], wn[5];
                xn[0] = ld128(nx + xr); xn[1] = ld128(nx + xr + 2048);
#pragma unroll
                for (int t = 0; t < 5; ++t) { const lptr p = nw + (t < 4 ? wr + t * 32 : wr4); wn[t] = cat8(trrd(p), trrd(p + 16 * 144)); }
                SB();
#pragma unroll
                for (int t = 0; t < 5; ++t) { a0[t][0] = MFMA16(wb[t], xb[0], a0[t][0]); a0[t][1] = MFMA16(wb[t], xb[1], a0[t][1]); }
                SB();
                xa[0] = xn[0]; xa[1] = xn[1];
#pragma unroll
                for (int t = 0; t < 5; ++t) wa[t] = wn[t];
            }
            {
                const lptr sx = lds + 2 * XS, sw = lds + 2 * WS1 + 32 * 144;
                LGKM0();
                SB();
                xb[0] = ld128(sx + (xr ^ 64)); xb[1] = ld128(sx + (xr ^ 64) + 2048);
#pragma unroll
                for (int t = 0; t < 5; ++t) { const lptr p = sw + (t < 4 ? wr + t * 32 : wr4); wb[t] = cat8(trrd(p), trrd(p + 16 * 144)); }
                SB();
#pragma unroll
                for (int t = 0; t < 5; ++t) { a0[t][0] = MFMA16(wa[t], xa[0], a0[t][0]); a0[t][1] = MFMA16(wa[t], xa[1], a0[t][1]); }
                SB();
                BAR();
#pragma unroll
                for (int t = 0; t < 5; ++t) { a0[t][0] = MFMA16(wb[t], xb[0], a0[t][0]); a0[t][1] = MFMA16(wb[t], xb[1], a0[t][1]); }
            }
            STAMP(2);
            const unsigned padv = hp >= 8 ? 0x00003f80u : 0u;
#pragma unroll
            for (int tt = 0; tt < 2; ++tt) {
                const int tok = 32 * lw + 16 * tt + r16;
#pragma unroll
                for (int t = 0; t < 5; ++t) {
                    const int col = 16 * t + 4 * g;
                    if (t < 4 || g < 2) {
                        const int i0 = col / 18, i1 = (col + 2) / 18;
                        *(LAS unsigned*)(lds + L_STG + i0 * 6144 + tok * 48 + (col - 18 * i0) * 2) = cvtpk(a0[t][tt][0], a0[t][tt][1]);
                        *(LAS unsigned*)(lds + L_STG + i1 * 6144 + tok * 48 + (col + 2 - 18 * i1) * 2) = cvtpk(a0[t][tt][2], a0[t][tt][3]);
                    }
                }
                *(LAS unsigned*)(lds + L_STG + g * 6144 + tok * 48 + 36) = padv;
                *(LAS u32x2*)(lds + L_STG + g * 6144 + tok * 48 + 40) = u32x2{0u, 0u};
            }
            LGKM0();
            u32x4 kvv[6];
#pragma unroll
            for (int i = 0; i < 6; ++i) { const int e = i * 64 + lane, hd = e / 96, o = e % 96; kvv[i] = *(const LAS u32x4*)(lds + L_STG + hd * 6144 + 32 * lw * 48 + o * 16); }
#pragma unroll
            for (int i = 0; i < 6; ++i) { const int e = i * 64 + lane, hd = e / 96, o = e % 96;
                st_dev(wsr, (unsigned)(WS_KV + (size_t)((rt * 16 + 2 * (hp & 7) + (hd >> 1)) * 4 + (hd & 1) * 2 + (hp >> 3)) * 6144) + 32 * lw * 48 + o * 16, kvv[i]); }
        }
        BAR();
        STAMP(3);
        f32x4 acc[5][4];
#pragma unroll
        for (int t = 0; t < 5; ++t)
#pragma unroll
            for (int u = 0; u < 4; ++u) acc[t][u] = f32x4{0, 0, 0, 0};
        const int th = lw & 1;
        const int xr = L_X + (64 * th + r16) * 128 + ((g ^ (r16 >> 1)) << 4), wr = L_W + h * 10240 + lane * 16;
        bf16x8 wa[5], xa[4], wb[5], xb[4];
        unsigned fk = 0;
        gu32* fkp = F_KV + (((lane & 1) * 8 + (hp >> 1)) * 16 + (rt - (lane >> 1) % 3 >= 0 ? rt - (lane >> 1) % 3 : rt)) * 32;
#pragma unroll
        for (int u = 0; u < 4; ++u) xa[u] = ld128(lds + xr + u * 2048);
#pragma unroll
        for (int t = 0; t < 5; ++t) wa[t] = ld128(lds + wr + t * 2048);
        int slot = 0;
        for (int i = 0; i < 8; ++i) {
            const lptr sx = lds + slot * XS, sw = lds + slot * WS1;
            SB();
#pragma unroll
            for (int u = 0; u < 4; ++u) xb[u] = ld128(sx + (xr ^ 64) + u * 2048);
#pragma unroll
            for (int t = 0; t < 5; ++t) { wb[t] = ld128(sw + wr + t * 2048 + 1024); SB();
#pragma unroll
                for (int u = 0; u < 4; ++u) acc[t][u] = MFMA16(wa[t], xa[u], acc[t][u]);
                SB(); }
            if (i == 2) WAITV(0);
            if (i == 5 && lw == 0) { WAITV(0); flag_confirm(fk, fkp, tag); }
            BAR();
            if (i == 2 && tid == 0) set_flag(F_KV + (hp * 16 + rt) * 32, tag);
            if (i == 4 && lw == 0) fk = flag_request(fkp);
            slot = slot == 2 ? 0 : slot + 1;
            const lptr nx = lds + slot * XS, nw = lds + slot * WS1;
            SB();
#pragma unroll
            for (int u = 0; u < 4; ++u) xa[u] = ld128(nx + xr + u * 2048);
#pragma unroll
            for (int t = 0; t < 5; ++t) { const bf16x8 n = ld128(nw + wr + t * 2048); SB();
#pragma unroll
                for (int u = 0; u < 4; ++u) acc[t][u] = MFMA16(wb[t], xb[u], acc[t][u]);
                SB(); wa[t] = n; }
        }
        {
            const lptr sx = lds + 2 * XS, sw = lds + 2 * WS1;
            SB();
#pragma unroll
            for (int u = 0; u < 4; ++u) xb[u] = ld128(sx + (xr ^ 64) + u * 2048);
#pragma unroll
            for (int t = 0; t < 5; ++t) { wb[t] = ld128(sw + wr + t * 2048 + 1024); SB();
#pragma unroll
                for (int u = 0; u < 4; ++u) acc[t][u] = MFMA16(wa[t], xa[u], acc[t][u]);
                SB(); }
            BAR();
#pragma unroll
            for (int t = 0; t < 5; ++t)
#pragma unroll
                for (int u = 0; u < 4; ++u) acc[t][u] = MFMA16(wb[t], xb[u], acc[t][u]);
        }
        STAMP(4);
        const lptr Qb = lds + L_Q + h * 6144, VAb = lds + L_VA + h * 6144;
#pragma unroll
        for (int u = 0; u < 4; ++u) {
            const int tok = 64 * th + 16 * u + r16;
            unsigned p2[3][2];
#pragma unroll
            for (int t = 0; t < 3; ++t) { p2[t][0] = cvtpk(acc[2 + t][u][0], acc[2 + t][u][1]); p2[t][1] = cvtpk(acc[2 + t][u][2], acc[2 + t][u][3]); }
            if (u < 2) {
#pragma unroll
                for (int t = 0; t < 3; ++t) { pk[t][u][0] = p2[t][0]; pk[t][u][1] = p2[t][1]; }
            } else {
#pragma unroll
                for (int t = 0; t < 3; ++t) *(LAS u32x2*)(lds + L_PKX + (lw * 3 + t) * 1024 + lane * 16 + (u - 2) * 8) = u32x2{p2[t][0], p2[t][1]};
            }
            *(LAS u32x2*)(VAb + tok * 48 + 8 * g) = u32x2{cvtpk(acc[0][u][0], acc[0][u][1]), cvtpk(acc[0][u][2], acc[0][u][3])};
            *(LAS u32x2*)(Qb + tok * 48 + 8 * g) = u32x2{cvtpk(acc[1][u][0] * QSCALE, acc[1][u][1] * QSCALE), cvtpk(acc[1][u][2] * QSCALE, acc[1][u][3] * QSCALE)};
            if (g == 2) *(LAS u32x2*)(VAb + tok * 48 + 32) = u32x2{p2[2][1], 0x00003f80u};
            else if (g == 3) {
                *(LAS unsigned*)(Qb + tok * 48 + 32) = cvtpk(acc[4][u][2] * QSCALE, acc[4][u][3] * QSCALE);
                if (h == 0) *(LAS float*)(lds + L_GATE + tok * 4) = __builtin_amdgcn_rcpf(1.f + __builtin_amdgcn_exp2f(-acc[4][u][0] * LOG2E));
            }
        }
    } else {
        for (int kb = 0; kb < 9; ++kb) {
            if (kb == 1) WAITV(8); else if (kb == 2) WAITV(10); else if (kb == 3) WAITV(8); else if (kb < 7) WAITV(6); else if (kb == 7) WAITV(5);
            BAR();
            if (kb == 4 && tid == 256) set_flag(F_W + (hp * 16 + rt) * 32, tag);
            if (kb < 6) { issue_x(kb + 3, kb % 3); issue_w0(kb + 3, kb % 3); }
            else issue_w1(kb, kb - 6);
            if (kb < 2) { w1_units(2 * kb); w1_units(2 * kb + 1); }
            if (kb == 7) {
#pragma unroll
                for (int i = 0; i < 2; ++i) { const int pc = lw + 4 * i;
                    if (pc < 6) { int c = pc * 64 + lane; if (c >= nC * 72) c = 0;
                        const int row = c / 9, cc = c % 9, C = C0 + (row >> 3), hq = C / 9, p = 8 * (C % 9) + (row & 7);
                        int br, head, d;
                        if (p < 32) { br = 1; head = p >> 4; d = p & 15; } else if (p < 36) { br = 1; head = (p >> 1) & 1; d = 16 + (p & 1); } else { br = 0; head = p >= 54; d = p - 36 - 18 * head; }
                        glds16_ptr((br ? woB : woA) + ((2 * hq + head) * 18 + d) * 1152 + cgp * 144 + cc * 16, lds0 + L_WOP + pc * 1024); }
                    else { int c = (pc - 6) * 64 + lane; if (c >= 108) c = 0; const int row = c / 3, cc = c % 3;
                        glds16((unsigned)(hp * 1296 + row * 36 + (cc == 2 ? 20 : cc * 16)), wphi, lds0 + L_PHI + (pc - 6) * 1024); } }
            }
        }
        WAITV(12);
        BAR();
        for (int i = 0; i < 5; ++i) {
            if (i == 0) WAITV(5); else WAITV(9);
            if (i == 1 && lw == 0 && lane < 4) *(LAS u32x4*)(lds + L_ZERO + lane * 16) = u32x4{0, 0, 0, 0};
            BAR();
            issue_x(i, i % 3); issue_w1(i, i % 3);
        }
        auto kv_copy = [&](int u) { const int ar = u / 18, tl = (u % 18) / 6, o = u % 6, srt = rt - 2 + tl;
            glds16_dev((unsigned)(WS_KV + (size_t)(((srt >= 0 ? srt : rt) * 16 + hp) * 4 + ar) * 6144) + o * 1024 + lane * 16, ws, lds0 + L_K + u * 1024); };
        WAITV(9); BAR();
        issue_x(5, 2); issue_w1(5, 2);
        WAITV(9); BAR();
#pragma unroll
        for (int n = 0; n < 9; ++n) { const int j = 4 * n + lw; kv_copy(j < 16 ? j : j + 32); }
        WAITV(9); BAR();
#pragma unroll
        for (int n = 0; n < 5; ++n) { const int j = 4 * n + lw; kv_copy(j < 16 ? 16 + j : 52 + j); }
        BAR();
#pragma unroll
        for (int n = 0; n < 4; ++n) kv_copy(32 + 4 * n + lw);
    }
    BAR();
    STAMP(5);
    const int cg = (b >> 3) & 7, r64 = 4 * (b & 7) + (b >> 6);
    unsigned fwo = 0;
    gu32* fwop = F_WO + (cg + 8 * (lane & 31)) * 32;
    {
        const int q4 = r16 >> 2, p4 = r16 & 3;
        const int U0 = wid * 4 + g, U = U0 < nC * 5 ? U0 : 0, Ci = (U * 52) >> 8, t = U - 5 * Ci, C = C0 + Ci, kq = C / 36, s = (C % 36) >> 2, gg = C & 3;
        const lptr p = lds + L_WOP + (8 * Ci + q4) * 144 + (t < 4 ? t * 32 + p4 * 8 : 128 + (p4 & 1) * 8);
        const u32x4 v = __builtin_bit_cast(u32x4, cat8(trrd(p), trrd(p + 4 * 144)));
        const unsigned off = (unsigned)(WS_WOT + (size_t)cgp * 165888) + (s * 4 + kq) * 4608 + (t < 4 ? t * 1024 + (gg * 16 + r16) * 16 : 4096 + (gg * 8 + r16) * 16);
        st_dev(wsr, U0 < nC * 5 && (t < 4 || r16 < 8) ? off : 0xfffffff0u, v);
    }
    if (loader) {
#pragma unroll
        for (int t = 0; t < 3; ++t) { const u32x4 v = *(const LAS u32x4*)(lds + L_PKX + (lw * 3 + t) * 1024 + lane * 16); pk[t][0][0] = v[0]; pk[t][0][1] = v[1]; pk[t][1][0] = v[2]; pk[t][1][1] = v[3]; }
    }
    unsigned phq[9];
    {
        unsigned phk[9];
        float p[2][9];
        const int q4 = r16 >> 2, p4 = r16 & 3;
        const lptr pw0 = lds + L_PHI + h * 864, zp = lds + L_ZERO;
        s16x4 R1[2][3];
#pragma unroll
        for (int hf = 0; hf < 2; ++hf)
#pragma unroll
            for (int i = 0; i < 3; ++i) R1[hf][i] = trrd((p4 >> 1) == hf ? pw0 + (4 * g + q4) * 48 + (i < 2 ? 16 * (p4 & 1) + 8 * i : 40) : zp);
#pragma unroll
        for (int X = 0; X < 2; ++X) {
            const bf16x8 bA = mk8(pk[X][0][0], pk[X][0][1], pk[2][0][0], pk[2][0][1]);
            const bf16x8 bB = mk8(pk[X][1][0], pk[X][1][1], pk[2][1][0], pk[2][1][1]);
            s16x4 R2[2][3];
#pragma unroll
            for (int hf = 0; hf < 2; ++hf)
#pragma unroll
                for (int i = 0; i < 3; ++i) R2[hf][i] = trrd((p4 >> 1) == hf && g == X && q4 >= 2 ? pw0 + (14 + q4) * 48 + (i < 2 ? 16 * (p4 & 1) + 8 * i : 40) : zp);
            f32x4 d[3];
#pragma unroll
            for (int i = 0; i < 3; ++i) { d[i] = MFMA16(cat8(R1[0][i], R2[0][i]), bA, (f32x4{0, 0, 0, 0})); d[i] = MFMA16(cat8(R1[1][i], R2[1][i]), bB, d[i]); }
#pragma unroll
            for (int j = 0; j < 4; ++j) { p[X][j] = d[0][j]; p[X][4 + j] = d[1][j]; }
            p[X][8] = (g & 1) ? d[2][3] : d[2][2];
        }
        feature_map(p[1], phk);
        const lptr pkp = lds + L_PK + h * 10240 + (32 * tg + 16 * (g >> 1) + r16) * 80 + (g & 1) * 40;
        *(LAS u32x2*)(pkp) = u32x2{phk[0], phk[1]}; *(LAS u32x2*)(pkp + 8) = u32x2{phk[2], phk[3]}; *(LAS u32x2*)(pkp + 16) = u32x2{phk[4], phk[5]};
        *(LAS u32x2*)(pkp + 24) = u32x2{phk[6], phk[7]}; *(LAS u32x2*)(pkp + 32) = u32x2{phk[8], 0u};
        feature_map(p[0], phq);
    }
    DRAIN_BAR();
    if (tid == 0) set_flag(F_WO + b * 32, tag);
    STAMP(6);
    if (wid >= 2) {
        const int w6 = wid - 2, hh = w6 / 3, mt = w6 % 3, q4 = r16 >> 2, p4 = r16 & 3;
        int u = 4 * mt + p4; if (u > 9) u = 9;
        const lptr pa = lds + L_PK + hh * 10240 + (8 * g + q4) * 80 + u * 8;
        const lptr pb0 = lds + L_VA + hh * 6144 + (8 * g + q4) * 48 + p4 * 8;
        const lptr pb1 = lds + L_VA + hh * 6144 + (8 * g + q4) * 48 + 32;
        f32x4 d0 = {0, 0, 0, 0}, d1 = {0, 0, 0, 0};
        bf16x8 a[4], b0[4], b1[4];
#pragma unroll
        for (int ks = 0; ks < 4; ++ks) {
            a[ks] = cat8(trrd(pa + ks * 32 * 80), trrd(pa + ks * 32 * 80 + 4 * 80));
            b0[ks] = cat8(trrd(pb0 + ks * 32 * 48), trrd(pb0 + ks * 32 * 48 + 4 * 48));
            b1[ks] = cat8(trrd(pb1 + ks * 32 * 48), trrd(pb1 + ks * 32 * 48 + 4 * 48));
        }
        SB();
#pragma unroll
        for (int ks = 0; ks < 4; ++ks) { d0 = MFMA16(a[ks], b0[ks], d0); d1 = MFMA16(a[ks], b1[ks], d1); }
        const int uu = 4 * mt + g;
        const unsigned po = (unsigned)(WS_PART + (size_t)(hp * 16 + rt) * 3072 + hh * 1520);
        if (uu < 10) {
            __builtin_amdgcn_raw_buffer_store_b64(u32x2{cvtpk(d0[0], d0[1]), cvtpk(d0[2], d0[3])}, wsr, po + (r16 * 10 + uu) * 8, 0, 16);
            if (r16 < 3) __builtin_amdgcn_raw_buffer_store_b64(u32x2{cvtpk(d1[0], d1[1]), cvtpk(d1[2], d1[3])}, wsr, po + ((16 + r16) * 10 + uu) * 8, 0, 16);
        }
    }
    const int q32 = lane & 31, hh = lane >> 5;
    const char* wot = ws + WS_WOT + (size_t)cg * 165888;
    auto g3_step = [&](int p) { const int s = p + cg; return s >= 9 ? s - 9 : s; };
    f32x16 oacc;
    u32x4 pv[3][4];
    auto pv_load = [&]() {
#pragma unroll
        for (int n = 0; n < 3; ++n) { const int gr = (3 * lw + n) * 16 + r16;
#pragma unroll
            for (int m = 0; m < 4; ++m) pv[n][m] = ld_dev(wsr, (unsigned)(WS_PART + (size_t)(hp * 16 + 4 * m + g) * 3072) + (gr < 190 ? gr : 0) * 16); }
    };
    {
        const lptr Kb = lds + L_K + h * 36864, Vb = Kb + 18432, Qb = lds + L_Q + h * 6144;
        const bf16x8 qf0 = ld128(Qb + (32 * tg + q32) * 48 + 16 * hh);
        const unsigned q16 = *(const LAS unsigned*)(Qb + (32 * tg + q32) * 48 + 32);
        const bf16x8 qf1 = mk8(hh ? 0u : q16, 0u, 0u, 0u);
        const lptr kp = Kb + (32 * tg + q32) * 48;
        const int i16 = lane & 15, dhalf = (lane >> 4) & 1;
        const lptr vp = Vb + (32 * tg + 4 * hh + (i16 >> 2)) * 48 + (dhalf ? 32 + ((i16 & 3) ? 8 : 0) : (i16 & 3) * 8);
        f32x16 negm;
#pragma unroll
        for (int r = 0; r < 16; ++r) { oacc[r] = 0.f; negm[r] = 0.f; }
        bf16x8 k0, k1;
        auto tile = [&](int KT, int KN, bool first, int mask) {
            SB();
            f32x16 s = MFMA32(k0, qf0, negm); s = MFMA32(k1, qf1, s);
            SB();
            const s16x4 va = trrd(vp + (KT * 32) * 48), vb = trrd(vp + (KT * 32 + 8) * 48), vc = trrd(vp + (KT * 32 + 16) * 48), vd = trrd(vp + (KT * 32 + 24) * 48);
            k0 = ld128(kp + KN * 32 * 48 + 16 * hh); k1 = ld128(kp + KN * 32 * 48 + 32);
            SB();
            if (mask) {
#pragma unroll
                for (int r = 0; r < 16; ++r) { const int jj = (r & 3) + 8 * (r >> 2) + 4 * hh; const bool ok = mask == 1 ? jj <= q32 : jj > q32; s[r] = ok ? s[r] : NEGBIG; }
            }
            float m = fmaxf(fmaxf(s[0], s[1]), s[2]);
#pragma unroll
            for (int r = 3; r < 15; r += 2) m = fmaxf(fmaxf(m, s[r]), s[r + 1]);
            m = x32max(fmaxf(m, s[15]));
            if (first || __any(m > THR)) {
                const float dl = first ? m : fmaxf(m, 0.f);
                const float f = first ? 1.f : __builtin_amdgcn_exp2f(-dl);
#pragma unroll
                for (int r = 0; r < 16; ++r) { s[r] -= dl; negm[r] -= dl; if (!first) oacc[r] *= f; }
            }
            unsigned pw[8];
#pragma unroll
            for (int r = 0; r < 8; ++r) pw[r] = cvtpk(__builtin_amdgcn_exp2f(s[2 * r]), __builtin_amdgcn_exp2f(s[2 * r + 1]));
            oacc = MFMA32(cat8(va, vb), mk8(pw[0], pw[1], pw[2], pw[3]), oacc);
            oacc = MFMA32(cat8(vc, vd), mk8(pw[4], pw[5], pw[6], pw[7]), oacc);
        };
        auto wo_piece = [&](int n) { const int pc = n * 4 + lw; if (pc < 54) { const int p = pc / 18, o = pc % 18;
            glds16_dev((unsigned)(o * 1024 + lane * 16), wot + g3_step(p) * G3_SLOT, lds0 + g3_slot(0) + p * G3_SLOT + o * 1024); } };
        auto wo_pieces = [&](int n0, int n1) { if (!loader) for (int n = n0; n < n1; ++n) wo_piece(n); };
        const int lo = rt >= 2 ? 0 : (rt == 1 ? 4 - tg : 8 - tg);
        const int last = lo > 1 ? lo : 1;
        k0 = ld128(kp + 8 * 32 * 48 + 16 * hh); k1 = ld128(kp + 8 * 32 * 48 + 32);
        tile(8, 7, true, 1);
        if (wid == 0) fwo = flag_request(fwop);
        int KT = 7;
        for (; KT >= last && KT >= 6; --KT) tile(KT, KT - 1, false, 0);
        WAITV(0);
        if (wid == 0) flag_confirm(fwo, fwop, tag);
        BAR();
        if (tid == 0) set_flag(F_P + (hp * 16 + rt) * 32, tag);
        STAMP(7);
        gu32* fpp = F_P + (hp * 16 + r16) * 32;
        if (rt >= 2) {
            tile(5, 4, false, 0); wo_pieces(0, 5);
            tile(4, 3, false, 0); wo_pieces(5, 10);
            unsigned fp = 0;
            if (loader) fp = flag_request(fpp);
            tile(3, 2, false, 0); wo_pieces(10, 14);
            if (loader) { WAITV(0); flag_confirm(fp, fpp, tag); pv_load(); }
            tile(2, 1, false, 0);
            tile(1, 0, false, 0);
            tile(0, 0, false, 2);
        } else {
            for (; KT >= last; --KT) tile(KT, KT - 1, false, 0);
            wo_pieces(0, 14);
            if (loader) { wait_flag(fpp, tag); pv_load(); }
        }
    }
    STAMP(8);
    {
        if (loader) {
            const unsigned one = (r16 < 8 && (r16 & 1) == 0) ? 0x00003f80u : (r16 < 8 ? 0x3f800000u : 0u);
            const bf16x8 idn = mk8((r16 >> 1) == 0 ? one : 0u, (r16 >> 1) == 1 ? one : 0u, (r16 >> 1) == 2 ? one : 0u, (r16 >> 1) == 3 ? one : 0u);
#pragma unroll
            for (int n = 0; n < 3; ++n) {
                f32x4 sm = {0, 0, 0, 0};
#pragma unroll
                for (int m = 0; m < 4; ++m) sm = MFMA16(idn, __builtin_bit_cast(bf16x8, pv[n][m]), sm);
                const int gr = (3 * lw + n) * 16 + r16, ph = gr >= 95, e0 = gr - 95 * ph, d = e0 / 5, u = 2 * (e0 - 5 * d) + g;
                if (gr < 190 && g < 2) *(LAS u32x2*)(lds + L_KVT + ph * 2016 + d * 96 + (u / 5) * 48 + (u % 5) * 8) = u32x2{cvtpk(sm[0], sm[1]), cvtpk(sm[2], sm[3])};
            }
        } else {
            const int e0 = lw * 64 + lane;
            if (e0 < 76) *(LAS u32x2*)(lds + L_KVT + (e0 / 38) * 2016 + ((e0 % 38) >> 1) * 96 + (e0 & 1) * 48 + 40) = u32x2{0u, 0u};
            else if (e0 < 88) *(LAS u32x4*)(lds + L_KVT + ((e0 - 76) / 6) * 2016 + 20 * 96 + ((e0 - 76) % 6) * 16) = u32x4{0, 0, 0, 0};
        }
        const int tokb = 32 * tg + q32;
        const float gb = *(const LAS float*)(lds + L_GATE + tokb * 4);
        auto sw = __builtin_amdgcn_permlane32_swap(__float_as_uint(oacc[10]), __float_as_uint(oacc[10]), false, false);
        const float sb = (1.f - gb) * __builtin_amdgcn_rcpf(__uint_as_float(sw[0]));
        const lptr ob = lds + L_OST + tokb * 144;
        *(LAS u32x2*)(ob + h * 32 + 8 * hh) = u32x2{cvtpk(oacc[0] * sb, oacc[1] * sb), cvtpk(oacc[2] * sb, oacc[3] * sb)};
        *(LAS u32x2*)(ob + h * 32 + 16 + 8 * hh) = u32x2{cvtpk(oacc[4] * sb, oacc[5] * sb), cvtpk(oacc[6] * sb, oacc[7] * sb)};
        if (hh == 0) *(LAS unsigned*)(ob + 64 + h * 4) = cvtpk(oacc[8] * sb, oacc[9] * sb);
    }
    BAR();
    STAMP(9);
    {
        f32x4 dd[3];
        const int eo = (r16 >> 2) & 1;
        const bool mine = (r16 >> 3) == (g >> 1);
        const bf16x8 b0 = mk8(phq[0], phq[1], phq[2], phq[3]), b1 = mk8(phq[4], phq[5], phq[6], phq[7]), b2 = mk8(phq[8], 0u, 0u, 0u);
        bf16x8 af[3][3];
#pragma unroll
        for (int i = 0; i < 3; ++i) {
            const int sg = 4 * i + (r16 & 3);
            int d = eo ? (sg < 8 ? 10 + sg : 20) : (sg < 10 ? sg : 20); if (sg == 10) d = 18; if (!mine) d = 20;
            const lptr ap = lds + L_KVT + h * 2016 + d * 96 + (g & 1) * 48;
            af[i][0] = ld128(ap); af[i][1] = ld128(ap + 16); af[i][2] = ld128(ap + 32);
        }
        const int tok = 32 * tg + 16 * (g >> 1) + r16;
        const float gate = *(const LAS float*)(lds + L_GATE + tok * 4);
        SB();
#pragma unroll
        for (int i = 0; i < 3; ++i) { f32x4 a = MFMA16(af[i][0], b0, (f32x4{0, 0, 0, 0})); a = MFMA16(af[i][1], b1, a); dd[i] = MFMA16(af[i][2], b2, a); }
        const float sc = gate * __builtin_amdgcn_rcpf(dd[2][2] + 1e-6f);
        const lptr op = lds + L_OST + tok * 144 + 72 + h * 36 + (g & 1) * 20;
        *(LAS unsigned*)(op) = cvtpk(dd[0][0] * sc, dd[0][1] * sc); *(LAS unsigned*)(op + 4) = cvtpk(dd[0][2] * sc, dd[0][3] * sc);
        *(LAS unsigned*)(op + 8) = cvtpk(dd[1][0] * sc, dd[1][1] * sc); *(LAS unsigned*)(op + 12) = cvtpk(dd[1][2] * sc, dd[1][3] * sc);
        if ((g & 1) == 0) *(LAS unsigned*)(op + 16) = cvtpk(dd[2][0] * sc, dd[2][1] * sc);
    }
    BAR();
    {
        u32x4 ov[3];
#pragma unroll
        for (int i = 0; i < 3; ++i) { const int e0 = i * 512 + tid, e = e0 < 1152 ? e0 : 0, R = e / 144, e1 = e % 144;
            ov[i] = *(const LAS u32x4*)(lds + L_OST + (16 * R + (e1 & 15)) * 144 + (e1 >> 4) * 16); }
#pragma unroll
        for (int i = 0; i < 3; ++i) { const int e0 = i * 512 + tid, R = e0 / 144, e1 = e0 % 144;
            if (e0 < 1152) st_dev(wsr, (unsigned)(WS_O + ((size_t)(rt * 8 + R) * 144 + 9 * hp) * 256 + e1 * 16), ov[i]); }
    }
    STAMP(10);
    {
        const int rt3 = r64 >> 1, rh = r64 & 1, kq = wid >> 1, rp = wid & 1;
        auto issue3 = [&](int p) {
            if (wid > 0) {
#pragma unroll
                for (int i = 0; i < 3; ++i) { const int pc = (wid - 1) + 7 * i;
                    if (pc < 18) glds16_dev((unsigned)(pc * 1024 + lane * 16), wot + g3_step(p) * G3_SLOT, lds0 + g3_slot(p) + pc * 1024); }
            }
        };
        DRAIN_BAR();
        if (tid == 0) set_flag(F_O + (rt * 16 + hp) * 32, tag);
        issue3(3); issue3(4); issue3(5); issue3(6); issue3(7);
        STAMP(11);
        if (wid == 0) wait_flag(F_O + (rt3 * 16 + r16) * 32, tag);
        DRAIN_BAR();
        STAMP(12);
        u32x4 of[9][2];
        const unsigned ob = (unsigned)(WS_O + ((size_t)(rt3 * 8 + 4 * rh + 2 * rp) * 144 + 36 * kq) * 256 + lane * 16);
        auto o_load = [&](int p) { const unsigned o = ob + g3_step(p) * 1024; of[p][0] = ld_dev(wsr, o); of[p][1] = ld_dev(wsr, o + 36864); };
        o_load(0); o_load(1); o_load(2);
        f32x4 a3[5][2];
#pragma unroll
        for (int t = 0; t < 5; ++t) { a3[t][0] = f32x4{0, 0, 0, 0}; a3[t][1] = f32x4{0, 0, 0, 0}; }
        const int wro = kq * 4608 + lane * 16, wr4 = kq * 4608 + 4096 + g * 128 + (r16 & 7) * 16;
        bf16x8 wf[5];
#pragma unroll
        for (int t = 0; t < 4; ++t) wf[t] = ld128(lds + g3_slot(0) + wro + t * 1024);
        wf[4] = ld128(lds + g3_slot(0) + wr4);
#pragma unroll
        for (int p = 0; p < 9; ++p) {
            if (p == 1) { BAR(); issue3(8); }
            if (p == 7) { WAITV(2); BAR(); }
            if (p + 3 < 9) o_load(p + 3);
            const lptr sp = lds + g3_slot(p + 1 < 9 ? p + 1 : 8);
            const bf16x8 bo0 = __builtin_bit_cast(bf16x8, of[p][0]), bo1 = __builtin_bit_cast(bf16x8, of[p][1]);
            SB();
#pragma unroll
            for (int t = 0; t < 5; ++t) {
                bf16x8 nw = wf[t];
                if (p + 1 < 9) nw = ld128(sp + (t < 4 ? wro + t * 1024 : wr4));
                SB();
                a3[t][0] = MFMA16(wf[t], bo0, a3[t][0]); a3[t][1] = MFMA16(wf[t], bo1, a3[t][1]);
                SB();
                wf[t] = nw;
            }
        }
        BAR();
        STAMP(13);
        const lptr rp0 = lds + rp * 40960 + lane * 16;
#pragma unroll
        for (int n = 0; n < 10; ++n) *(LAS f32x4*)(rp0 + (n * 4 + kq) * 1024) = a3[n % 5][n / 5];
        BAR();
        f32x4 pr[3][4];
#pragma unroll
        for (int j = 0; j < 3; ++j) { const int n = kq + 4 * j < 10 ? kq + 4 * j : kq;
#pragma unroll
            for (int o = 0; o < 4; ++o) pr[j][o] = *(const LAS f32x4*)(rp0 + (n * 4 + o) * 1024); }
#pragma unroll
        for (int j = 0; j < 3; ++j) { const int n = kq + 4 * j, tt = n >= 5, t = n - 5 * tt;
            const f32x4 u = (pr[j][0] + pr[j][1]) + (pr[j][2] + pr[j][3]);
            char* orow = (char*)out + (size_t)(rt3 * 128 + 64 * rh + 32 * rp + 16 * tt + r16) * 1152 + cg * 144 + g * 8 + t * 32;
            if (n < 10 && (t < 4 || g < 2)) *(u32x2*)orow = u32x2{cvtpk(u[0], u[1]), cvtpk(u[2], u[3])};
        }
        STAMP(14);
        asm volatile("fused_code_end:");
        if (b == 0 && tid == 0) set_flag((gu32*)(ws + WS_EPOCH), tag);
    }
}

extern "C" void kernel_launch(void* const* d_in, const int* in_sizes, int n_in, void* d_out, int out_size, void* d_ws, size_t ws_size, hipStream_t stream) {
    (void)hipFuncSetAttribute((const void*)fused_kernel, hipFuncAttributeMaxDynamicSharedMemorySize, LDS_BYTES);
    fused_kernel<<<256, 512, LDS_BYTES, stream>>>((const char*)d_in[0], (const char*)d_in[1], (const char*)d_in[5], (const char*)d_in[2], (const char*)d_in[3],
                                                  (const char*)d_in[6], (const char*)d_in[7], (char*)d_ws, (u16*)d_out);
}
```

```cpp
#include <hip/hip_runtime.h>
#include <hip/hip_bf16.h>
#include <stdint.h>

typedef unsigned short u16;
typedef short bf16x8 __attribute__((ext_vector_type(8)));
typedef short s16x4 __attribute__((ext_vector_type(4)));
typedef float f32x4 __attribute__((ext_vector_type(4)));
typedef float f32x16 __attribute__((ext_vector_type(16)));
typedef unsigned u32x4 __attribute__((ext_vector_type(4)));
typedef unsigned u32x2 __attribute__((ext_vector_type(2)));
typedef __bf16 bf16x2_t __attribute__((ext_vector_type(2)));
typedef float f32x2 __attribute__((ext_vector_type(2)));
#define LAS __attribute__((address_space(3)))
#define GAS __attribute__((address_space(1)))
typedef LAS char* lptr;
typedef GAS unsigned gu32;

constexpr size_t WS_EPOCH = 0;
constexpr size_t WS_FLAG = 4096;
constexpr size_t WS_WT   = 4096 + 5 * 256 * 128;
constexpr size_t WS_WOT  = WS_WT + 16 * 9 * 20480;
constexpr size_t WS_KV   = WS_WOT + 8 * 165888;
constexpr size_t WS_PART = WS_KV + 256 * 24576;
constexpr size_t WS_O    = WS_PART + 256 * 6144;
constexpr size_t WS_DBG  = WS_O + 16 * 8 * 36864;

constexpr int XS = 16384, WS1 = 20480;
constexpr int L_X = 0, L_W = 3 * XS;
constexpr int L_STG = L_W + 3 * WS1;
constexpr int L_WP = L_STG;
constexpr int L_GW = L_WP + 15 * 1024;
constexpr int L_PHI = L_STG + 24576;
constexpr int L_ZERO = L_PHI + 1792;
constexpr int L_WOP = L_PHI + 2048;
constexpr int L_PKX = 118784;
constexpr int L_K = 0;
constexpr int L_Q = 73728;
constexpr int L_VA = 86016;
constexpr int L_PK = 98304;
constexpr int L_GATE = 147456;
constexpr int L_KVT = 147968;
constexpr int L_OST = 73728;
constexpr int LDS_BYTES = 160 * 1024;
constexpr int G3_SLOT = 18432;
__device__ __forceinline__ constexpr int g3_slot(int p) { return (p < 3 ? 5 + p : p < 8 ? p - 3 : 5) * G3_SLOT; }
constexpr unsigned SPIN_CAP = 1u << 18;
constexpr float LOG2E = 1.4426950408889634f;
constexpr float QSCALE = 0.23570226039551584f * LOG2E;
constexpr float THR = 8.f;
constexpr float NEGBIG = -1e30f;

__device__ __forceinline__ unsigned cvtpk(float lo, float hi) { const f32x2 v = {lo, hi}; return __builtin_bit_cast(unsigned, __builtin_convertvector(v, bf16x2_t)); }
__device__ __forceinline__ void glds16(unsigned voff, const void* sbase, unsigned lds_addr) {
    unsigned sv;
    asm volatile("s_mov_b32 %0, m0\n\ts_mov_b32 m0, %3\n\ts_nop 0\n\tglobal_load_lds_dwordx4 %1, %2\n\ts_mov_b32 m0, %0" : "=&s"(sv) : "v"(voff), "s"(sbase), "s"(lds_addr) : "memory");
}
__device__ __forceinline__ void glds16_dev(unsigned voff, const void* sbase, unsigned lds_addr) {
    unsigned sv;
    asm volatile("s_mov_b32 %0, m0\n\ts_mov_b32 m0, %3\n\ts_nop 0\n\tglobal_load_lds_dwordx4 %1, %2 sc1\n\ts_mov_b32 m0, %0" : "=&s"(sv) : "v"(voff), "s"(sbase), "s"(lds_addr) : "memory");
}
__device__ __forceinline__ void glds16_ptr(const void* g, unsigned lds_addr) {
    unsigned sv;
    asm volatile("s_mov_b32 %0, m0\n\ts_mov_b32 m0, %2\n\ts_nop 0\n\tglobal_load_lds_dwordx4 %1, off\n\ts_mov_b32 m0, %0" : "=&s"(sv) : "v"(g), "s"(lds_addr) : "memory");
}
#define WAITV(n) asm volatile("s_waitcnt vmcnt(" #n ")" ::: "memory")
#define WAITL() asm volatile("s_waitcnt lgkmcnt(0)" ::: "memory")
#define LGKM0() do { __builtin_amdgcn_s_waitcnt(0xC07F); asm volatile("" ::: "memory"); } while (0)
#define BAR() do { asm volatile("" ::: "memory"); __builtin_amdgcn_s_waitcnt(0xC07F); __builtin_amdgcn_s_barrier(); asm volatile("" ::: "memory"); } while (0)
#define DRAIN_BAR() do { asm volatile("s_waitcnt vmcnt(0)" ::: "memory"); BAR(); } while (0)
#define SB() __builtin_amdgcn_sched_barrier(0)
#define MFMA16(a, b, c) __builtin_amdgcn_mfma_f32_16x16x32_bf16(a, b, c, 0, 0, 0)
#define MFMA32(a, b, c) __builtin_amdgcn_mfma_f32_32x32x16_bf16(a, b, c, 0, 0, 0)
__device__ __forceinline__ bf16x8 ld128(lptr p) { return *(const LAS bf16x8*)p; }
__device__ __forceinline__ s16x4 trrd(lptr p) { typedef short v4i16_t __attribute__((ext_vector_type(4))); return __builtin_bit_cast(s16x4, __builtin_amdgcn_ds_read_tr16_b64_v4i16((LAS v4i16_t*)p)); }
__device__ __forceinline__ bf16x8 cat8(s16x4 a, s16x4 b) { return __builtin_shufflevector(a, b, 0, 1, 2, 3, 4, 5, 6, 7); }
__device__ __forceinline__ bf16x8 mk8(unsigned a, unsigned b, unsigned c, unsigned d) { u32x4 w = {a, b, c, d}; return __builtin_bit_cast(bf16x8, w); }
__device__ __forceinline__ float x16max(float x) { auto s = __builtin_amdgcn_permlane16_swap(__float_as_uint(x), __float_as_uint(x), false, false); return fmaxf(__uint_as_float(s[0]), __uint_as_float(s[1])); }
__device__ __forceinline__ float x16sum(float x) { auto s = __builtin_amdgcn_permlane16_swap(__float_as_uint(x), __float_as_uint(x), false, false); return __uint_as_float(s[0]) + __uint_as_float(s[1]); }
__device__ __forceinline__ float x32max(float x) { auto s = __builtin_amdgcn_permlane32_swap(__float_as_uint(x), __float_as_uint(x), false, false); return fmaxf(__uint_as_float(s[0]), __uint_as_float(s[1])); }
__device__ __forceinline__ void st_dev(const __amdgpu_buffer_rsrc_t rs, unsigned off, u32x4 v) { __builtin_amdgcn_raw_buffer_store_b128(v, rs, off, 0, 16); }
__device__ __forceinline__ u32x4 ld_dev(const __amdgpu_buffer_rsrc_t rs, unsigned off) { return __builtin_amdgcn_raw_buffer_load_b128(rs, off, 0, 16); }
__device__ __forceinline__ void wait_flag(gu32* p, unsigned tag) {
    for (unsigned spins = 0; spins < SPIN_CAP; ++spins) {
        const unsigned v = __hip_atomic_load(p, __ATOMIC_RELAXED, __HIP_MEMORY_SCOPE_AGENT);
        if (__all(v == tag)) break;
        __builtin_amdgcn_s_sleep(1);
    }
}
__device__ __forceinline__ void set_flag(gu32* p, unsigned tag) { __hip_atomic_store(p, tag, __ATOMIC_RELAXED, __HIP_MEMORY_SCOPE_AGENT); }
__device__ __forceinline__ u32x4 gather8(lptr p, int stride) {
    unsigned w[4];
#pragma unroll
    for (int j = 0; j < 4; ++j) w[j] = (unsigned)*(const LAS u16*)(p + 2 * j * stride) | ((unsigned)*(const LAS u16*)(p + (2 * j + 1) * stride) << 16);
    return u32x4{w[0], w[1], w[2], w[3]};
}
__device__ __forceinline__ int piece_start(int j) { return (9 * j + 1) >> 1; }

__device__ __forceinline__ unsigned flag_request(gu32* p) { unsigned v; asm volatile("global_load_dword %0, %1, off sc1" : "=v"(v) : "v"(p) : "memory"); return v; }
__device__ __forceinline__ void flag_confirm(unsigned v, gu32* p, unsigned tag) {
    asm volatile("" : "+v"(v));
    if (!__all(v == tag)) wait_flag(p, tag);
}

#define STAMP(i) do {} while (0)
#define WSTAMP(i) do {} while (0)

__device__ __forceinline__ void feature_map(const float (&p)[9], unsigned (&o)[9]) {
    float mx = fmaxf(fmaxf(p[0], p[1]), p[2]), mn = fminf(fminf(p[0], p[1]), p[2]);
#pragma unroll
    for (int i = 3; i < 9; i += 2) { mx = fmaxf(fmaxf(mx, p[i]), p[i + 1]); mn = fminf(fminf(mn, p[i]), p[i + 1]); }
    mx = x16max(mx); mn = -x16max(-mn);
    const float mxl = -mx * LOG2E, mnl = mn * LOG2E;
    float a[9], b[9], sa = 0.f, sb = 0.f;
#pragma unroll
    for (int i = 0; i < 9; ++i) { a[i] = __builtin_amdgcn_exp2f(__builtin_fmaf(p[i], LOG2E, mxl)); b[i] = __builtin_amdgcn_exp2f(__builtin_fmaf(p[i], -LOG2E, mnl)); sa += a[i]; sb += b[i]; }
    sa = x16sum(sa); sb = x16sum(sb);
    const float ra = __builtin_amdgcn_rcpf(sa), rb = __builtin_amdgcn_rcpf(sb);
#pragma unroll
    for (int i = 0; i < 9; ++i) { a[i] *= ra; b[i] *= rb; }
    o[0] = cvtpk(a[0], a[1]); o[1] = cvtpk(a[2], a[3]); o[2] = cvtpk(a[4], a[5]); o[3] = cvtpk(a[6], a[7]); o[4] = cvtpk(a[8], b[0]);
    o[5] = cvtpk(b[1], b[2]); o[6] = cvtpk(b[3], b[4]); o[7] = cvtpk(b[5], b[6]); o[8] = cvtpk(b[7], b[8]);
}

__global__ __launch_bounds__(512, 2) void fused_kernel(const char* __restrict__ x, const char* __restrict__ wA, const char* __restrict__ wB, const char* __restrict__ wphi,
                                                       const char* __restrict__ woA, const char* __restrict__ woB, const char* __restrict__ gw,
                                                       char* __restrict__ ws, u16* __restrict__ out) {
    extern __shared__ __attribute__((aligned(16))) char lds_raw[];
    const lptr lds = (lptr)lds_raw;
    const unsigned lds0 = (unsigned)(uintptr_t)lds_raw;
    const int tid = threadIdx.x, lane = tid & 63, wid = __builtin_amdgcn_readfirstlane(tid >> 6);
    const int b = blockIdx.x, hp = (b & 3) * 4 + ((b >> 3) & 3), rt = ((b >> 2) & 1) * 8 + (b >> 5);
    const bool loader = wid >= 4;
    const int lw = wid & 3;
    const int h = lw >> 1, tg = 2 * (lw & 1) + (wid >> 2);
    const int r16 = lane & 15, g = lane >> 4;
    gu32* flags = (gu32*)(ws + WS_FLAG);
    gu32* F_W = flags;
    gu32* F_WO = flags + 256 * 32;
    gu32* F_KV = flags + 512 * 32;
    gu32* F_P = flags + 768 * 32;
    gu32* F_O = flags + 1024 * 32;
    {
        unsigned long long pc0; unsigned len;
        asm volatile("s_getpc_b64 %0\nfused_code_begin:" : "=s"(pc0));
        asm volatile("s_mov_b32 %0, fused_code_end-fused_code_begin" : "=s"(len));
        if (wid == 3) {
            const unsigned j = blockIdx.x >> 3;
            if ((j + 1) * 1024 <= len) glds16(j * 1024 + lane * 16, (const void*)pc0, lds0 + 160256);
            if ((j + 33) * 1024 <= len) glds16((j + 32) * 1024 + lane * 16, (const void*)pc0, lds0 + 160256);
        }
    }
    STAMP(0);
    unsigned tagv = flag_request((gu32*)(ws + WS_EPOCH));
    const __amdgpu_buffer_rsrc_t wsr = __builtin_amdgcn_make_buffer_rsrc(ws, 0, 0x2000000, 0x00020000);

    const int G0 = piece_start(rt), nG = piece_start(rt + 1) - G0;
    const int j2 = b >> 3, cgp = b & 7, C0 = piece_start(j2), nC = piece_start(j2 + 1) - C0;
#pragma unroll
    for (int i = 0; i < 2; ++i) {
        const int pc = wid + 8 * i;
        if (pc < 15) { int c = pc * 64 + lane; if (c >= nG * 192) c = 0;
            const int row = c / 24, ch = c % 24, w = ch / 3, cc = ch % 3, sg = w >> 1;
            const char* src = (sg < 3 ? wA + sg * 1152 : wB) + (size_t)(8 * G0 + row) * 3456 + (2 * hp + (w & 1)) * 36 + (cc == 2 ? 20 : cc * 16);
            glds16_ptr(src, lds0 + L_WP + pc * 1024); }
        else glds16(G0 * 16 + (lane < nG ? lane : 0) * 16, gw, lds0 + L_GW);
    }
    unsigned xo[4], w0o[3];
    const char* w0base = wB + 1152 + (hp >> 3) * 1152 + (hp & 7) * 144;
    const int krot = (2 * (hp & 3) + (rt & 7)) % 9;
    auto kact = [&](int kb) { const int k = kb + krot; return k >= 9 ? k - 9 : k; };
    auto issue_x = [&](int kb, int slot) {
        const char* src = x + kact(kb) * 128;
#pragma unroll
        for (int i = 0; i < 4; ++i) glds16(xo[i], src, lds0 + L_X + slot * XS + lw * 4096 + i * 1024);
    };
    auto issue_w0 = [&](int kb, int slot) {
        const char* src = w0base + (size_t)kact(kb) * 64 * 3456;
        glds16(w0o[0], src, lds0 + L_W + slot * WS1 + lw * 1024); glds16(w0o[1], src, lds0 + L_W + slot * WS1 + (lw + 4) * 1024);
        if (lw == 0) glds16(w0o[2], src, lds0 + L_W + slot * WS1 + 8192);
    };
    if (loader) {
#pragma unroll
        for (int i = 0; i < 4; ++i) { const int row = 32 * lw + 8 * i + (lane >> 3); xo[i] = (unsigned)(rt * 128 + row) * 1152u + (unsigned)(((lane & 7) ^ ((row >> 1) & 7)) << 4); }
#pragma unroll
        for (int i = 0; i < 3; ++i) { int c = (lw + 4 * i) * 64 + lane; if (c >= 576) c = 0;
            const int rho = c / 9, cc = c % 9, k = (rho & 32) + 8 * ((rho & 15) >> 2) + (rho & 3) + 4 * ((rho >> 4) & 1); w0o[i] = (unsigned)k * 3456u + cc * 16; }
        issue_x(0, 0); issue_w0(0, 0);
        issue_x(1, 1); issue_w0(1, 1);
        WAITV(6);
    } else WAITV(0);
    BAR();
    asm volatile("" : "+v"(tagv));
    const unsigned tag = __builtin_amdgcn_readfirstlane(tagv) + 1u;
    STAMP(1);
    if (loader) { issue_x(2, 2); issue_w0(2, 2); }
    auto w1_units = [&](int i) {
        const int q4 = r16 >> 2, p4 = r16 & 3;
        const int U0 = i * 16 + lw * 4 + g, U = U0 < nG * 10 ? U0 : 0, Gi = (U * 26) >> 8, ht = U - 10 * Gi, hh2 = ht >= 5, t = ht - 5 * hh2;
        const int sg = t < 4 ? (0x1032 >> (4 * t)) & 3 : p4;
        const lptr p = lds + L_WP + (8 * Gi + q4) * 384 + (sg * 2 + hh2) * 48 + (t < 4 ? p4 * 8 : 40);
        const s16x4 ra = trrd(p), rb = trrd(p + 4 * 384);
        const u32x4 gv = *(const LAS u32x4*)(lds + L_GW + Gi * 16);
        u32x4 v = __builtin_bit_cast(u32x4, cat8(ra, rb));
        const bool isg = t == 4 && r16 == 12;
        v = u32x4{isg ? gv[0] : v[0], isg ? gv[1] : v[1], isg ? gv[2] : v[2], isg ? gv[3] : v[3]};
        const int G = G0 + Gi;
        const unsigned off = (unsigned)(WS_WT + (size_t)hp * 9 * 20480) + (G >> 3) * 20480 + (ht * 2 + ((G >> 2) & 1)) * 1024 + ((G & 3) * 16 + r16) * 16;
        st_dev(wsr, U0 < nG * 10 ? off : 0xfffffff0u, v);
    };
    const char* wt = ws + WS_WT + (size_t)hp * 9 * 20480;
    auto issue_w1 = [&](int kb, int slot) {
#pragma unroll
        for (int i = 0; i < 5; ++i) glds16_dev((unsigned)(lw * 1024 + lane * 16), wt + kact(kb) * 20480 + i * 4096, lds0 + L_W + slot * WS1 + i * 4096 + lw * 1024);
    };
    unsigned pk[3][2][2];
    if (!loader) {
        {
            f32x4 a0[5][2];
#pragma unroll
            for (int t = 0; t < 5; ++t) { a0[t][0] = f32x4{0, 0, 0, 0}; a0[t][1] = f32x4{0, 0, 0, 0}; }
            const int xr = L_X + (32 * lw + r16) * 128 + ((g ^ (r16 >> 1)) << 4);
            const int q4 = r16 >> 2, p4 = r16 & 3;
            const int wr = L_W + (4 * g + q4) * 144 + p4 * 8, wr4 = L_W + (4 * g + q4) * 144 + 128 + (p4 & 1) * 8;
            bf16x8 wa[5], xa[2], wb[5], xb[2];
            unsigned fw = 0;
            xa[0] = ld128(lds + xr); xa[1] = ld128(lds + xr + 2048);
#pragma unroll
            for (int t = 0; t < 5; ++t) { const lptr p = lds + (t < 4 ? wr + t * 32 : wr4); wa[t] = cat8(trrd(p), trrd(p + 16 * 144)); }
            int slot = 0;
            for (int kb = 0; kb < 8; ++kb) {
                const lptr sx = lds + slot * XS, sw = lds + slot * WS1 + 32 * 144;
                LGKM0();
                SB();
                xb[0] = ld128(sx + (xr ^ 64)); xb[1] = ld128(sx + (xr ^ 64) + 2048);
#pragma unroll
                for (int t = 0; t < 5; ++t) { const lptr p = sw + (t < 4 ? wr + t * 32 : wr4); wb[t] = cat8(trrd(p), trrd(p + 16 * 144)); }
                SB();
#pragma unroll
                for (int t = 0; t < 5; ++t) { a0[t][0] = MFMA16(wa[t], xa[0], a0[t][0]); a0[t][1] = MFMA16(wa[t], xa[1], a0[t][1]); }
                SB();
                if (kb == 6 && lw == 0) { WAITV(0); flag_confirm(fw, F_W + (hp * 16 + r16) * 32, tag); }
                BAR();
                if (kb == 5 && lw == 0) fw = flag_request(F_W + (hp * 16 + r16) * 32);
                slot = slot == 2 ? 0 : slot + 1;
                const lptr nx = lds + slot * XS, nw = lds + slot * WS1;
                SB();
                bf16x8 xn[2], wn[5];
                xn[0] = ld128(nx + xr); xn[1] = ld128(nx + xr + 2048);
#pragma unroll
                for (int t = 0; t < 5; ++t) { const lptr p = nw + (t < 4 ? wr + t * 32 : wr4); wn[t] = cat8(trrd(p), trrd(p + 16 * 144)); }
                SB();
#pragma unroll
                for (int t = 0; t < 5; ++t) { a0[t][0] = MFMA16(wb[t], xb[0], a0[t][0]); a0[t][1] = MFMA16(wb[t], xb[1], a0[t][1]); }
                SB();
                xa[0] = xn[0]; xa[1] = xn[1];
#pragma unroll
                for (int t = 0; t < 5; ++t) wa[t] = wn[t];
            }
            {
                const lptr sx = lds + 2 * XS, sw = lds + 2 * WS1 + 32 * 144;
                LGKM0();
                SB();
                xb[0] = ld128(sx + (xr ^ 64)); xb[1] = ld128(sx + (xr ^ 64) + 2048);
#pragma unroll
                for (int t = 0; t < 5; ++t) { const lptr p = sw + (t < 4 ? wr + t * 32 : wr4); wb[t] = cat8(trrd(p), trrd(p + 16 * 144)); }
                SB();
#pragma unroll
                for (int t = 0; t < 5; ++t) { a0[t][0] = MFMA16(wa[t], xa[0], a0[t][0]); a0[t][1] = MFMA16(wa[t], xa[1], a0[t][1]); }
                SB();
                BAR();
#pragma unroll
                for (int t = 0; t < 5; ++t) { a0[t][0] = MFMA16(wb[t], xb[0], a0[t][0]); a0[t][1] = MFMA16(wb[t], xb[1], a0[t][1]); }
            }
            STAMP(2);
            const unsigned padv = hp >= 8 ? 0x00003f80u : 0u;
#pragma unroll
            for (int tt = 0; tt < 2; ++tt) {
                const int tok = 32 * lw + 16 * tt + r16;
#pragma unroll
                for (int t = 0; t < 5; ++t) {
                    const int col = 16 * t + 4 * g;
                    if (t < 4 || g < 2) {
                        const int i0 = col / 18, i1 = (col + 2) / 18;
                        *(LAS unsigned*)(lds + L_STG + i0 * 6144 + tok * 48 + (col - 18 * i0) * 2) = cvtpk(a0[t][tt][0], a0[t][tt][1]);
                        *(LAS unsigned*)(lds + L_STG + i1 * 6144 + tok * 48 + (col + 2 - 18 * i1) * 2) = cvtpk(a0[t][tt][2], a0[t][tt][3]);
                    }
                }
                *(LAS unsigned*)(lds + L_STG + g * 6144 + tok * 48 + 36) = padv;
                *(LAS u32x2*)(lds + L_STG + g * 6144 + tok * 48 + 40) = u32x2{0u, 0u};
            }
            LGKM0();
            u32x4 kvv[6];
#pragma unroll
            for (int i = 0; i < 6; ++i) { const int e = i * 64 + lane, hd = e / 96, o = e % 96; kvv[i] = *(const LAS u32x4*)(lds + L_STG + hd * 6144 + 32 * lw * 48 + o * 16); }
#pragma unroll
            for (int i = 0; i < 6; ++i) { const int e = i * 64 + lane, hd = e / 96, o = e % 96;
                st_dev(wsr, (unsigned)(WS_KV + (size_t)((rt * 16 + 2 * (hp & 7) + (hd >> 1)) * 4 + (hd & 1) * 2 + (hp >> 3)) * 6144) + 32 * lw * 48 + o * 16, kvv[i]); }
        }
        BAR();
        STAMP(3);
        f32x4 acc[5][4];
#pragma unroll
        for (int t = 0; t < 5; ++t)
#pragma unroll
            for (int u = 0; u < 4; ++u) acc[t][u] = f32x4{0, 0, 0, 0};
        const int th = lw & 1;
        const int xr = L_X + (64 * th + r16) * 128 + ((g ^ (r16 >> 1)) << 4), wr = L_W + h * 10240 + lane * 16;
        bf16x8 wa[5], xa[4], wb[5], xb[4];
        unsigned fk = 0;
        gu32* fkp = F_KV + (((lane & 1) * 8 + (hp >> 1)) * 16 + (rt - (lane >> 1) % 3 >= 0 ? rt - (lane >> 1) % 3 : rt)) * 32;
#pragma unroll
        for (int u = 0; u < 4; ++u) xa[u] = ld128(lds + xr + u * 2048);
#pragma unroll
        for (int t = 0; t < 5; ++t) wa[t] = ld128(lds + wr + t * 2048);
        int slot = 0;
        for (int i = 0; i < 8; ++i) {
            const lptr sx = lds + slot * XS, sw = lds + slot * WS1;
            SB();
#pragma unroll
            for (int u = 0; u < 4; ++u) xb[u] = ld128(sx + (xr ^ 64) + u * 2048);
#pragma unroll
            for (int t = 0; t < 5; ++t) { wb[t] = ld128(sw + wr + t * 2048 + 1024); SB();
#pragma unroll
                for (int u = 0; u < 4; ++u) acc[t][u] = MFMA16(wa[t], xa[u], acc[t][u]);
                SB(); }
            if (i == 2) WAITV(0);
            if (i == 5 && lw == 0) { WAITV(0); flag_confirm(fk, fkp, tag); }
            BAR();
            if (i == 2 && tid == 0) set_flag(F_KV + (hp * 16 + rt) * 32, tag);
            if (i == 4 && lw == 0) fk = flag_request(fkp);
            slot = slot == 2 ? 0 : slot + 1;
            const lptr nx = lds + slot * XS, nw = lds + slot * WS1;
            SB();
#pragma unroll
            for (int u = 0; u < 4; ++u) xa[u] = ld128(nx + xr + u * 2048);
#pragma unroll
            for (int t = 0; t < 5; ++t) { const bf16x8 n = ld128(nw + wr + t * 2048); SB();
#pragma unroll
                for (int u = 0; u < 4; ++u) acc[t][u] = MFMA16(wb[t], xb[u], acc[t][u]);
                SB(); wa[t] = n; }
        }
        {
            const lptr sx = lds + 2 * XS, sw = lds + 2 * WS1;
            SB();
#pragma unroll
            for (int u = 0; u < 4; ++u) xb[u] = ld128(sx + (xr ^ 64) + u * 2048);
#pragma unroll
            for (int t = 0; t < 5; ++t) { wb[t] = ld128(sw + wr + t * 2048 + 1024); SB();
#pragma unroll
                for (int u = 0; u < 4; ++u) acc[t][u] = MFMA16(wa[t], xa[u], acc[t][u]);
                SB(); }
            BAR();
#pragma unroll
            for (int t = 0; t < 5; ++t)
#pragma unroll
                for (int u = 0; u < 4; ++u) acc[t][u] = MFMA16(wb[t], xb[u], acc[t][u]);
        }
        STAMP(4);
        const lptr Qb = lds + L_Q + h * 6144, VAb = lds + L_VA + h * 6144;
#pragma unroll
        for (int u = 0; u < 4; ++u) {
            const int tok = 64 * th + 16 * u + r16;
            unsigned p2[3][2];
#pragma unroll
            for (int t = 0; t < 3; ++t) { p2[t][0] = cvtpk(acc[2 + t][u][0], acc[2 + t][u][1]); p2[t][1] = cvtpk(acc[2 + t][u][2], acc[2 + t][u][3]); }
            if (u < 2) {
#pragma unroll
                for (int t = 0; t < 3; ++t) { pk[t][u][0] = p2[t][0]; pk[t][u][1] = p2[t][1]; }
            } else {
#pragma unroll
                for (int t = 0; t < 3; ++t) *(LAS u32x2*)(lds + L_PKX + (lw * 3 + t) * 1024 + lane * 16 + (u - 2) * 8) = u32x2{p2[t][0], p2[t][1]};
            }
            *(LAS u32x2*)(VAb + tok * 48 + 8 * g) = u32x2{cvtpk(acc[0][u][0], acc[0][u][1]), cvtpk(acc[0][u][2], acc[0][u][3])};
            *(LAS u32x2*)(Qb + tok * 48 + 8 * g) = u32x2{cvtpk(acc[1][u][0] * QSCALE, acc[1][u][1] * QSCALE), cvtpk(acc[1][u][2] * QSCALE, acc[1][u][3] * QSCALE)};
            if (g == 2) *(LAS u32x2*)(VAb + tok * 48 + 32) = u32x2{p2[2][1], 0x00003f80u};
            else if (g == 3) {
                *(LAS unsigned*)(Qb + tok * 48 + 32) = cvtpk(acc[4][u][2] * QSCALE, acc[4][u][3] * QSCALE);
                if (h == 0) *(LAS float*)(lds + L_GATE + tok * 4) = __builtin_amdgcn_rcpf(1.f + __builtin_amdgcn_exp2f(-acc[4][u][0] * LOG2E));
            }
        }
    } else {
        for (int kb = 0; kb < 9; ++kb) {
            if (kb == 1) WAITV(8); else if (kb == 2) WAITV(10); else if (kb == 3) WAITV(8); else if (kb < 7) WAITV(6); else if (kb == 7) WAITV(5);
            BAR();
            if (kb == 4 && tid == 256) set_flag(F_W + (hp * 16 + rt) * 32, tag);
            if (kb < 6) { issue_x(kb + 3, kb % 3); issue_w0(kb + 3, kb % 3); }
            else issue_w1(kb, kb - 6);
            if (kb < 2) { w1_units(2 * kb); w1_units(2 * kb + 1); }
            if (kb == 7) {
#pragma unroll
                for (int i = 0; i < 2; ++i) { const int pc = lw + 4 * i;
                    if (pc < 6) { int c = pc * 64 + lane; if (c >= nC * 72) c = 0;
                        const int row = c / 9, cc = c % 9, C = C0 + (row >> 3), hq = C / 9, p = 8 * (C % 9) + (row & 7);
                        int br, head, d;
                        if (p < 32) { br = 1; head = p >> 4; d = p & 15; } else if (p < 36) { br = 1; head = (p >> 1) & 1; d = 16 + (p & 1); } else { br = 0; head = p >= 54; d = p - 36 - 18 * head; }
                        glds16_ptr((br ? woB : woA) + ((2 * hq + head) * 18 + d) * 1152 + cgp * 144 + cc * 16, lds0 + L_WOP + pc * 1024); }
                    else { int c = (pc - 6) * 64 + lane; if (c >= 108) c = 0; const int row = c / 3, cc = c % 3;
                        glds16((unsigned)(hp * 1296 + row * 36 + (cc == 2 ? 20 : cc * 16)), wphi, lds0 + L_PHI + (pc - 6) * 1024); } }
            }
        }
        WAITV(12);
        BAR();
        for (int i = 0; i < 5; ++i) {
            if (i == 0) WAITV(5); else WAITV(9);
            if (i == 1 && lw == 0 && lane < 4) *(LAS u32x4*)(lds + L_ZERO + lane * 16) = u32x4{0, 0, 0, 0};
            BAR();
            issue_x(i, i % 3); issue_w1(i, i % 3);
        }
        auto kv_copy = [&](int u) { const int ar = u / 18, tl = (u % 18) / 6, o = u % 6, srt = rt - 2 + tl;
            glds16_dev((unsigned)(WS_KV + (size_t)(((srt >= 0 ? srt : rt) * 16 + hp) * 4 + ar) * 6144) + o * 1024 + lane * 16, ws, lds0 + L_K + u * 1024); };
        WAITV(9); BAR();
        issue_x(5, 2); issue_w1(5, 2);
        WAITV(9); BAR();
#pragma unroll
        for (int n = 0; n < 9; ++n) { const int j = 4 * n + lw; kv_copy(j < 16 ? j : j + 32); }
        WAITV(9); BAR();
#pragma unroll
        for (int n = 0; n < 5; ++n) { const int j = 4 * n + lw; kv_copy(j < 16 ? 16 + j : 52 + j); }
        BAR();
#pragma unroll
        for (int n = 0; n < 4; ++n) kv_copy(32 + 4 * n + lw);
    }
    BAR();
    STAMP(5);
    const int cg = (b >> 3) & 7, r64 = 4 * (b & 7) + (b >> 6);
    unsigned fwo = 0;
    gu32* fwop = F_WO + (cg + 8 * (lane & 31)) * 32;
    {
        const int q4 = r16 >> 2, p4 = r16 & 3;
        const int U0 = wid * 4 + g, U = U0 < nC * 5 ? U0 : 0, Ci = (U * 52) >> 8, t = U - 5 * Ci, C = C0 + Ci, kq = C / 36, s = (C % 36) >> 2, gg = C & 3;
        const lptr p = lds + L_WOP + (8 * Ci + q4) * 144 + (t < 4 ? t * 32 + p4 * 8 : 128 + (p4 & 1) * 8);
        const u32x4 v = __builtin_bit_cast(u32x4, cat8(trrd(p), trrd(p + 4 * 144)));
        const unsigned off = (unsigned)(WS_WOT + (size_t)cgp * 165888) + (s * 4 + kq) * 4608 + (t < 4 ? t * 1024 + (gg * 16 + r16) * 16 : 4096 + (gg * 8 + r16) * 16);
        st_dev(wsr, U0 < nC * 5 && (t < 4 || r16 < 8) ? off : 0xfffffff0u, v);
    }
    if (loader) {
#pragma unroll
        for (int t = 0; t < 3; ++t) { const u32x4 v = *(const LAS u32x4*)(lds + L_PKX + (lw * 3 + t) * 1024 + lane * 16); pk[t][0][0] = v[0]; pk[t][0][1] = v[1]; pk[t][1][0] = v[2]; pk[t][1][1] = v[3]; }
    }
    unsigned phq[9];
    {
        unsigned phk[9];
        float p[2][9];
        const int q4 = r16 >> 2, p4 = r16 & 3;
        const lptr pw0 = lds + L_PHI + h * 864, zp = lds + L_ZERO;
        s16x4 R1[2][3];
#pragma unroll
        for (int hf = 0; hf < 2; ++hf)
#pragma unroll
            for (int i = 0; i < 3; ++i) R1[hf][i] = trrd((p4 >> 1) == hf ? pw0 + (4 * g + q4) * 48 + (i < 2 ? 16 * (p4 & 1) + 8 * i : 40) : zp);
#pragma unroll
        for (int X = 0; X < 2; ++X) {
            const bf16x8 bA = mk8(pk[X][0][0], pk[X][0][1], pk[2][0][0], pk[2][0][1]);
            const bf16x8 bB = mk8(pk[X][1][0], pk[X][1][1], pk[2][1][0], pk[2][1][1]);
            s16x4 R2[2][3];
#pragma unroll
            for (int hf = 0; hf < 2; ++hf)
#pragma unroll
                for (int i = 0; i < 3; ++i) R2[hf][i] = trrd((p4 >> 1) == hf && g == X && q4 >= 2 ? pw0 + (14 + q4) * 48 + (i < 2 ? 16 * (p4 & 1) + 8 * i : 40) : zp);
            f32x4 d[3];
#pragma unroll
            for (int i = 0; i < 3; ++i) { d[i] = MFMA16(cat8(R1[0][i], R2[0][i]), bA, (f32x4{0, 0, 0, 0})); d[i] = MFMA16(cat8(R1[1][i], R2[1][i]), bB, d[i]); }
#pragma unroll
            for (int j = 0; j < 4; ++j) { p[X][j] = d[0][j]; p[X][4 + j] = d[1][j]; }
            p[X][8] = (g & 1) ? d[2][3] : d[2][2];
        }
        feature_map(p[1], phk);
        const lptr pkp = lds + L_PK + h * 10240 + (32 * tg + 16 * (g >> 1) + r16) * 80 + (g & 1) * 40;
        *(LAS u32x2*)(pkp) = u32x2{phk[0], phk[1]}; *(LAS u32x2*)(pkp + 8) = u32x2{phk[2], phk[3]}; *(LAS u32x2*)(pkp + 16) = u32x2{phk[4], phk[5]};
        *(LAS u32x2*)(pkp + 24) = u32x2{phk[6], phk[7]}; *(LAS u32x2*)(pkp + 32) = u32x2{phk[8], 0u};
        feature_map(p[0], phq);
    }
    DRAIN_BAR();
    if (tid == 0) set_flag(F_WO + b * 32, tag);
    STAMP(6);
    if (wid >= 2) {
        const int w6 = wid - 2, hh = w6 / 3, mt = w6 % 3, q4 = r16 >> 2, p4 = r16 & 3;
        int u = 4 * mt + p4; if (u > 9) u = 9;
        const lptr pa = lds + L_PK + hh * 10240 + (8 * g + q4) * 80 + u * 8;
        const lptr pb0 = lds + L_VA + hh * 6144 + (8 * g + q4) * 48 + p4 * 8;
        const lptr pb1 = lds + L_VA + hh * 6144 + (8 * g + q4) * 48 + 32;
        f32x4 d0 = {0, 0, 0, 0}, d1 = {0, 0, 0, 0};
        bf16x8 a[4], b0[4], b1[4];
#pragma unroll
        for (int ks = 0; ks < 4; ++ks) {
            a[ks] = cat8(trrd(pa + ks * 32 * 80), trrd(pa + ks * 32 * 80 + 4 * 80));
            b0[ks] = cat8(trrd(pb0 + ks * 32 * 48), trrd(pb0 + ks * 32 * 48 + 4 * 48));
            b1[ks] = cat8(trrd(pb1 + ks * 32 * 48), trrd(pb1 + ks * 32 * 48 + 4 * 48));
        }
        SB();
#pragma unroll
        for (int ks = 0; ks < 4; ++ks) { d0 = MFMA16(a[ks], b0[ks], d0); d1 = MFMA16(a[ks], b1[ks], d1); }
        const int uu = 4 * mt + g;
        const unsigned po = (unsigned)(WS_PART + (size_t)(hp * 16 + rt) * 3072 + hh * 1520);
        if (uu < 10) {
            __builtin_amdgcn_raw_buffer_store_b64(u32x2{cvtpk(d0[0], d0[1]), cvtpk(d0[2], d0[3])}, wsr, po + (r16 * 10 + uu) * 8, 0, 16);
            if (r16 < 3) __builtin_amdgcn_raw_buffer_store_b64(u32x2{cvtpk(d1[0], d1[1]), cvtpk(d1[2], d1[3])}, wsr, po + ((16 + r16) * 10 + uu) * 8, 0, 16);
        }
    }
    const int q32 = lane & 31, hh = lane >> 5;
    const char* wot = ws + WS_WOT + (size_t)cg * 165888;
    auto g3_step = [&](int p) { const int s = p + cg; return s >= 9 ? s - 9 : s; };
    f32x16 oacc;
    u32x4 pv[3][4];
    auto pv_load = [&]() {
#pragma unroll
        for (int n = 0; n < 3; ++n) { const int gr = (3 * lw + n) * 16 + r16;
#pragma unroll
            for (int m = 0; m < 4; ++m) pv[n][m] = ld_dev(wsr, (unsigned)(WS_PART + (size_t)(hp * 16 + 4 * m + g) * 3072) + (gr < 190 ? gr : 0) * 16); }
    };
    {
        const lptr Kb = lds + L_K + h * 36864, Vb = Kb + 18432, Qb = lds + L_Q + h * 6144;
        const bf16x8 qf0 = ld128(Qb + (32 * tg + q32) * 48 + 16 * hh);
        const unsigned q16 = *(const LAS unsigned*)(Qb + (32 * tg + q32) * 48 + 32);
        const bf16x8 qf1 = mk8(hh ? 0u : q16, 0u, 0u, 0u);
        const lptr kp = Kb + (32 * tg + q32) * 48;
        const int i16 = lane & 15, dhalf = (lane >> 4) & 1;
        const lptr vp = Vb + (32 * tg + 4 * hh + (i16 >> 2)) * 48 + (dhalf ? 32 + ((i16 & 3) ? 8 : 0) : (i16 & 3) * 8);
        f32x16 negm;
#pragma unroll
        for (int r = 0; r < 16; ++r) { oacc[r] = 0.f; negm[r] = 0.f; }
        bf16x8 k0, k1;
        auto tile = [&](int KT, int KN, bool first, int mask) {
            SB();
            f32x16 s = MFMA32(k0, qf0, negm); s = MFMA32(k1, qf1, s);
            SB();
            const s16x4 va = trrd(vp + (KT * 32) * 48), vb = trrd(vp + (KT * 32 + 8) * 48), vc = trrd(vp + (KT * 32 + 16) * 48), vd = trrd(vp + (KT * 32 + 24) * 48);
            k0 = ld128(kp + KN * 32 * 48 + 16 * hh); k1 = ld128(kp + KN * 32 * 48 + 32);
            SB();
            if (mask) {
#pragma unroll
                for (int r = 0; r < 16; ++r) { const int jj = (r & 3) + 8 * (r >> 2) + 4 * hh; const bool ok = mask == 1 ? jj <= q32 : jj > q32; s[r] = ok ? s[r] : NEGBIG; }
            }
            float m = fmaxf(fmaxf(s[0], s[1]), s[2]);
#pragma unroll
            for (int r = 3; r < 15; r += 2) m = fmaxf(fmaxf(m, s[r]), s[r + 1]);
            m = x32max(fmaxf(m, s[15]));
            if (first || __any(m > THR)) {
                const float dl = first ? m : fmaxf(m, 0.f);
                const float f = first ? 1.f : __builtin_amdgcn_exp2f(-dl);
#pragma unroll
                for (int r = 0; r < 16; ++r) { s[r] -= dl; negm[r] -= dl; if (!first) oacc[r] *= f; }
            }
            unsigned pw[8];
#pragma unroll
            for (int r = 0; r < 8; ++r) pw[r] = cvtpk(__builtin_amdgcn_exp2f(s[2 * r]), __builtin_amdgcn_exp2f(s[2 * r + 1]));
            oacc = MFMA32(cat8(va, vb), mk8(pw[0], pw[1], pw[2], pw[3]), oacc);
            oacc = MFMA32(cat8(vc, vd), mk8(pw[4], pw[5], pw[6], pw[7]), oacc);
        };
        auto wo_piece = [&](int n) { const int pc = n * 4 + lw; if (pc < 54) { const int p = pc / 18, o = pc % 18;
            glds16_dev((unsigned)(o * 1024 + lane * 16), wot + g3_step(p) * G3_SLOT, lds0 + g3_slot(0) + p * G3_SLOT + o * 1024); } };
        auto wo_pieces = [&](int n0, int n1) { if (!loader) for (int n = n0; n < n1; ++n) wo_piece(n); };
        const int lo = rt >= 2 ? 0 : (rt == 1 ? 4 - tg : 8 - tg);
        const int last = lo > 1 ? lo : 1;
        k0 = ld128(kp + 8 * 32 * 48 + 16 * hh); k1 = ld128(kp + 8 * 32 * 48 + 32);
        tile(8, 7, true, 1);
        if (wid == 0) fwo = flag_request(fwop);
        int KT = 7;
        for (; KT >= last && KT >= 6; --KT) tile(KT, KT - 1, false, 0);
        WAITV(0);
        if (wid == 0) flag_confirm(fwo, fwop, tag);
        BAR();
        if (tid == 0) set_flag(F_P + (hp * 16 + rt) * 32, tag);
        STAMP(7);
        gu32* fpp = F_P + (hp * 16 + r16) * 32;
        if (rt >= 2) {
            tile(5, 4, false, 0); wo_pieces(0, 5);
            tile(4, 3, false, 0); wo_pieces(5, 10);
            unsigned fp = 0;
            if (loader) fp = flag_request(fpp);
            tile(3, 2, false, 0); wo_pieces(10, 14);
            if (loader) { WAITV(0); flag_confirm(fp, fpp, tag); pv_load(); }
            tile(2, 1, false, 0);
            tile(1, 0, false, 0);
            tile(0, 0, false, 2);
        } else {
            for (; KT >= last; --KT) tile(KT, KT - 1, false, 0);
            wo_pieces(0, 14);
            if (loader) { wait_flag(fpp, tag); pv_load(); }
        }
    }
    STAMP(8);
    {
        if (loader) {
            const unsigned one = (r16 < 8 && (r16 & 1) == 0) ? 0x00003f80u : (r16 < 8 ? 0x3f800000u : 0u);
            const bf16x8 idn = mk8((r16 >> 1) == 0 ? one : 0u, (r16 >> 1) == 1 ? one : 0u, (r16 >> 1) == 2 ? one : 0u, (r16 >> 1) == 3 ? one : 0u);
#pragma unroll
            for (int n = 0; n < 3; ++n) {
                f32x4 sm = {0, 0, 0, 0};
#pragma unroll
                for (int m = 0; m < 4; ++m) sm = MFMA16(idn, __builtin_bit_cast(bf16x8, pv[n][m]), sm);
                const int gr = (3 * lw + n) * 16 + r16, ph = gr >= 95, e0 = gr - 95 * ph, d = e0 / 5, u = 2 * (e0 - 5 * d) + g;
                if (gr < 190 && g < 2) *(LAS u32x2*)(lds + L_KVT + ph * 2016 + d * 96 + (u / 5) * 48 + (u % 5) * 8) = u32x2{cvtpk(sm[0], sm[1]), cvtpk(sm[2], sm[3])};
            }
        } else {
            const int e0 = lw * 64 + lane;
            if (e0 < 76) *(LAS u32x2*)(lds + L_KVT + (e0 / 38) * 2016 + ((e0 % 38) >> 1) * 96 + (e0 & 1) * 48 + 40) = u32x2{0u, 0u};
            else if (e0 < 88) *(LAS u32x4*)(lds + L_KVT + ((e0 - 76) / 6) * 2016 + 20 * 96 + ((e0 - 76) % 6) * 16) = u32x4{0, 0, 0, 0};
        }
        const int tokb = 32 * tg + q32;
        const float gb = *(const LAS float*)(lds + L_GATE + tokb * 4);
        auto sw = __builtin_amdgcn_permlane32_swap(__float_as_uint(oacc[10]), __float_as_uint(oacc[10]), false, false);
        const float sb = (1.f - gb) * __builtin_amdgcn_rcpf(__uint_as_float(sw[0]));
        const lptr ob = lds + L_OST + tokb * 144;
        *(LAS u32x2*)(ob + h * 32 + 8 * hh) = u32x2{cvtpk(oacc[0] * sb, oacc[1] * sb), cvtpk(oacc[2] * sb, oacc[3] * sb)};
        *(LAS u32x2*)(ob + h * 32 + 16 + 8 * hh) = u32x2{cvtpk(oacc[4] * sb, oacc[5] * sb), cvtpk(oacc[6] * sb, oacc[7] * sb)};
        if (hh == 0) *(LAS unsigned*)(ob + 64 + h * 4) = cvtpk(oacc[8] * sb, oacc[9] * sb);
    }
    BAR();
    STAMP(9);
    {
        f32x4 dd[3];
        const int eo = (r16 >> 2) & 1;
        const bool mine = (r16 >> 3) == (g >> 1);
        const bf16x8 b0 = mk8(phq[0], phq[1], phq[2], phq[3]), b1 = mk8(phq[4], phq[5], phq[6], phq[7]), b2 = mk8(phq[8], 0u, 0u, 0u);
        bf16x8 af[3][3];
#pragma unroll
        for (int i = 0; i < 3; ++i) {
            const int sg = 4 * i + (r16 & 3);
            int d = eo ? (sg < 8 ? 10 + sg : 20) : (sg < 10 ? sg : 20); if (sg == 10) d = 18; if (!mine) d = 20;
            const lptr ap = lds + L_KVT + h * 2016 + d * 96 + (g & 1) * 48;
            af[i][0] = ld128(ap); af[i][1] = ld128(ap + 16); af[i][2] = ld128(ap + 32);
        }
        const int tok = 32 * tg + 16 * (g >> 1) + r16;
        const float gate = *(const LAS float*)(lds + L_GATE + tok * 4);
        SB();
#pragma unroll
        for (int i = 0; i < 3; ++i) { f32x4 a = MFMA16(af[i][0], b0, (f32x4{0, 0, 0, 0})); a = MFMA16(af[i][1], b1, a); dd[i] = MFMA16(af[i][2], b2, a); }
        const float sc = gate * __builtin_amdgcn_rcpf(dd[2][2] + 1e-6f);
        const lptr op = lds + L_OST + tok * 144 + 72 + h * 36 + (g & 1) * 20;
        *(LAS unsigned*)(op) = cvtpk(dd[0][0] * sc, dd[0][1] * sc); *(LAS unsigned*)(op + 4) = cvtpk(dd[0][2] * sc, dd[0][3] * sc);
        *(LAS unsigned*)(op + 8) = cvtpk(dd[1][0] * sc, dd[1][1] * sc); *(LAS unsigned*)(op + 12) = cvtpk(dd[1][2] * sc, dd[1][3] * sc);
        if ((g & 1) == 0) *(LAS unsigned*)(op + 16) = cvtpk(dd[2][0] * sc, dd[2][1] * sc);
    }
    BAR();
    {
        u32x4 ov[3];
#pragma unroll
        for (int i = 0; i < 3; ++i) { const int e0 = i * 512 + tid, e = e0 < 1152 ? e0 : 0, R = e / 144, e1 = e % 144;
            ov[i] = *(const LAS u32x4*)(lds + L_OST + (16 * R + (e1 & 15)) * 144 + (e1 >> 4) * 16); }
#pragma unroll
        for (int i = 0; i < 3; ++i) { const int e0 = i * 512 + tid, R = e0 / 144, e1 = e0 % 144;
            if (e0 < 1152) st_dev(wsr, (unsigned)(WS_O + ((size_t)(rt * 8 + R) * 144 + 9 * hp) * 256 + e1 * 16), ov[i]); }
    }
    STAMP(10);
    {
        const int rt3 = r64 >> 1, rh = r64 & 1, kq = wid >> 1, rp = wid & 1;
        auto issue3 = [&](int p) {
            if (wid > 0) {
#pragma unroll
                for (int i = 0; i < 3; ++i) { const int pc = (wid - 1) + 7 * i;
                    if (pc < 18) glds16_dev((unsigned)(pc * 1024 + lane * 16), wot + g3_step(p) * G3_SLOT, lds0 + g3_slot(p) + pc * 1024); }
            }
        };
        DRAIN_BAR();
        if (tid == 0) set_flag(F_O + (rt * 16 + hp) * 32, tag);
        issue3(3); issue3(4); issue3(5); issue3(6); issue3(7);
        STAMP(11);
        if (wid == 0) wait_flag(F_O + (rt3 * 16 + r16) * 32, tag);
        DRAIN_BAR();
        STAMP(12);
        u32x4 of[9][2];
        const unsigned ob = (unsigned)(WS_O + ((size_t)(rt3 * 8 + 4 * rh + 2 * rp) * 144 + 36 * kq) * 256 + lane * 16);
        auto o_load = [&](int p) { const unsigned o = ob + g3_step(p) * 1024; of[p][0] = ld_dev(wsr, o); of[p][1] = ld_dev(wsr, o + 36864); };
        o_load(0); o_load(1); o_load(2);
        f32x4 a3[5][2];
#pragma unroll
        for (int t = 0; t < 5; ++t) { a3[t][0] = f32x4{0, 0, 0, 0}; a3[t][1] = f32x4{0, 0, 0, 0}; }
        const int wro = kq * 4608 + lane * 16, wr4 = kq * 4608 + 4096 + g * 128 + (r16 & 7) * 16;
        bf16x8 wf[5];
#pragma unroll
        for (int t = 0; t < 4; ++t) wf[t] = ld128(lds + g3_slot(0) + wro + t * 1024);
        wf[4] = ld128(lds + g3_slot(0) + wr4);
#pragma unroll
        for (int p = 0; p < 9; ++p) {
            if (p == 1) { BAR(); issue3(8); }
            if (p == 7) { WAITV(2); BAR(); }
            if (p + 3 < 9) o_load(p + 3);
            const lptr sp = lds + g3_slot(p + 1 < 9 ? p + 1 : 8);
            const bf16x8 bo0 = __builtin_bit_cast(bf16x8, of[p][0]), bo1 = __builtin_bit_cast(bf16x8, of[p][1]);
            SB();
#pragma unroll
            for (int t = 0; t < 5; ++t) {
                bf16x8 nw = wf[t];
                if (p + 1 < 9) nw = ld128(sp + (t < 4 ? wro + t * 1024 : wr4));
                SB();
                a3[t][0] = MFMA16(wf[t], bo0, a3[t][0]); a3[t][1] = MFMA16(wf[t], bo1, a3[t][1]);
                SB();
                wf[t] = nw;
            }
        }
        BAR();
        STAMP(13);
        const lptr rp0 = lds + rp * 40960 + lane * 16;
#pragma unroll
        for (int n = 0; n < 10; ++n) *(LAS f32x4*)(rp0 + (n * 4 + kq) * 1024) = a3[n % 5][n / 5];
        BAR();
        f32x4 pr[3][4];
#pragma unroll
        for (int j = 0; j < 3; ++j) { const int n = kq + 4 * j < 10 ? kq + 4 * j : kq;
#pragma unroll
            for (int o = 0; o < 4; ++o) pr[j][o] = *(const LAS f32x4*)(rp0 + (n * 4 + o) * 1024); }
#pragma unroll
        for (int j = 0; j < 3; ++j) { const int n = kq + 4 * j, tt = n >= 5, t = n - 5 * tt;
            const f32x4 u = (pr[j][0] + pr[j][1]) + (pr[j][2] + pr[j][3]);
            char* orow = (char*)out + (size_t)(rt3 * 128 + 64 * rh + 32 * rp + 16 * tt + r16) * 1152 + cg * 144 + g * 8 + t * 32;
            if (n < 10 && (t < 4 || g < 2)) *(u32x2*)orow = u32x2{cvtpk(u[0], u[1]), cvtpk(u[2], u[3])};
        }
        STAMP(14);
        asm volatile("fused_code_end:");
        if (b == 0 && tid == 0) set_flag((gu32*)(ws + WS_EPOCH), tag);
    }
}

extern "C" void kernel_launch(void* const* d_in, const int* in_sizes, int n_in, void* d_out, int out_size, void* d_ws, size_t ws_size, hipStream_t stream) {
    (void)hipFuncSetAttribute((const void*)fused_kernel, hipFuncAttributeMaxDynamicSharedMemorySize, LDS_BYTES);
    fused_kernel<<<256, 512, LDS_BYTES, stream>>>((const char*)d_in[0], (const char*)d_in[1], (const char*)d_in[5], (const char*)d_in[2], (const char*)d_in[3],
                                                  (const char*)d_in[6], (const char*)d_in[7], (char*)d_ws, (u16*)d_out);
}
```

```cpp
#include <hip/hip_runtime.h>
#include <hip/hip_bf16.h>
#include <stdint.h>

typedef unsigned short u16;
typedef short bf16x8 __attribute__((ext_vector_type(8)));
typedef short s16x4 __attribute__((ext_vector_type(4)));
typedef float f32x4 __attribute__((ext_vector_type(4)));
typedef float f32x16 __attribute__((ext_vector_type(16)));
typedef unsigned u32x4 __attribute__((ext_vector_type(4)));
typedef unsigned u32x2 __attribute__((ext_vector_type(2)));
typedef __bf16 bf16x2_t __attribute__((ext_vector_type(2)));
typedef float f32x2 __attribute__((ext_vector_type(2)));
#define LAS __attribute__((address_space(3)))
#define GAS __attribute__((address_space(1)))
typedef LAS char* lptr;
typedef GAS unsigned gu32;

constexpr size_t WS_EPOCH = 0;
constexpr size_t WS_FLAG = 4096;
constexpr size_t WS_WT   = 4096 + 5 * 256 * 128;
constexpr size_t WS_WOT  = WS_WT + 16 * 9 * 20480;
constexpr size_t WS_KV   = WS_WOT + 8 * 165888;
constexpr size_t WS_PART = WS_KV + 256 * 24576;
constexpr size_t WS_O    = WS_PART + 256 * 6144;
constexpr size_t WS_DBG  = WS_O + 16 * 8 * 36864;

constexpr int XS = 16384, WS1 = 20480;
constexpr int L_X = 0, L_W = 3 * XS;
constexpr int L_STG = L_W + 3 * WS1;
constexpr int L_WP = L_STG;
constexpr int L_GW = L_WP + 15 * 1024;
constexpr int L_PHI = L_STG + 24576;
constexpr int L_ZERO = L_PHI + 1792;
constexpr int L_WOP = L_PHI + 2048;
constexpr int L_PKX = 118784;
constexpr int L_K = 0;
constexpr int L_Q = 73728;
constexpr int L_VA = 86016;
constexpr int L_PK = 98304;
constexpr int L_GATE = 147456;
constexpr int L_KVT = 147968;
constexpr int L_OST = 73728;
constexpr int LDS_BYTES = 160 * 1024;
constexpr int G3_SLOT = 18432;
__device__ __forceinline__ constexpr int g3_slot(int p) { return (p < 3 ? 5 + p : p < 8 ? p - 3 : 5) * G3_SLOT; }
constexpr unsigned SPIN_CAP = 1u << 18;
constexpr float LOG2E = 1.4426950408889634f;
constexpr float QSCALE = 0.23570226039551584f * LOG2E;
constexpr float THR = 8.f;
constexpr float NEGBIG = -1e30f;

__device__ __forceinline__ unsigned cvtpk(float lo, float hi) { const f32x2 v = {lo, hi}; return __builtin_bit_cast(unsigned, __builtin_convertvector(v, bf16x2_t)); }
__device__ __forceinline__ void glds16(unsigned voff, const void* sbase, unsigned lds_addr) {
    unsigned sv;
    asm volatile("s_mov_b32 %0, m0\n\ts_mov_b32 m0, %3\n\ts_nop 0\n\tglobal_load_lds_dwordx4 %1, %2\n\ts_mov_b32 m0, %0" : "=&s"(sv) : "v"(voff), "s"(sbase), "s"(lds_addr) : "memory");
}
__device__ __forceinline__ void glds16_dev(unsigned voff, const void* sbase, unsigned lds_addr) {
    unsigned sv;
    asm volatile("s_mov_b32 %0, m0\n\ts_mov_b32 m0, %3\n\ts_nop 0\n\tglobal_load_lds_dwordx4 %1, %2 sc1\n\ts_mov_b32 m0, %0" : "=&s"(sv) : "v"(voff), "s"(sbase), "s"(lds_addr) : "memory");
}
__device__ __forceinline__ void glds16_ptr(const void* g, unsigned lds_addr) {
    unsigned sv;
    asm volatile("s_mov_b32 %0, m0\n\ts_mov_b32 m0, %2\n\ts_nop 0\n\tglobal_load_lds_dwordx4 %1, off\n\ts_mov_b32 m0, %0" : "=&s"(sv) : "v"(g), "s"(lds_addr) : "memory");
}
#define WAITV(n) asm volatile("s_waitcnt vmcnt(" #n ")" ::: "memory")
#define WAITL() asm volatile("s_waitcnt lgkmcnt(0)" ::: "memory")
#define LGKM0() do { __builtin_amdgcn_s_waitcnt(0xC07F); asm volatile("" ::: "memory"); } while (0)
#define BAR() do { asm volatile("" ::: "memory"); __builtin_amdgcn_s_waitcnt(0xC07F); __builtin_amdgcn_s_barrier(); asm volatile("" ::: "memory"); } while (0)
#define DRAIN_BAR() do { asm volatile("s_waitcnt vmcnt(0)" ::: "memory"); BAR(); } while (0)
#define SB() __builtin_amdgcn_sched_barrier(0)
#define MFMA16(a, b, c) __builtin_amdgcn_mfma_f32_16x16x32_bf16(a, b, c, 0, 0, 0)
#define MFMA32(a, b, c) __builtin_amdgcn_mfma_f32_32x32x16_bf16(a, b, c, 0, 0, 0)
__device__ __forceinline__ bf16x8 ld128(lptr p) { return *(const LAS bf16x8*)p; }
__device__ __forceinline__ s16x4 trrd(lptr p) { typedef short v4i16_t __attribute__((ext_vector_type(4))); return __builtin_bit_cast(s16x4, __builtin_amdgcn_ds_read_tr16_b64_v4i16((LAS v4i16_t*)p)); }
__device__ __forceinline__ bf16x8 cat8(s16x4 a, s16x4 b) { return __builtin_shufflevector(a, b, 0, 1, 2, 3, 4, 5, 6, 7); }
__device__ __forceinline__ bf16x8 mk8(unsigned a, unsigned b, unsigned c, unsigned d) { u32x4 w = {a, b, c, d}; return __builtin_bit_cast(bf16x8, w); }
__device__ __forceinline__ float vmax2(unsigned a, unsigned b) { float r; asm("v_max_f32 %0, %1, %2" : "=v"(r) : "v"(a), "v"(b)); return r; }
__device__ __forceinline__ float x16max(float x) { auto s = __builtin_amdgcn_permlane16_swap(__float_as_uint(x), __float_as_uint(x), false, false); return vmax2(s[0], s[1]); }
__device__ __forceinline__ float x16sum(float x) { auto s = __builtin_amdgcn_permlane16_swap(__float_as_uint(x), __float_as_uint(x), false, false); return __uint_as_float(s[0]) + __uint_as_float(s[1]); }
__device__ __forceinline__ float x32max(float x) { auto s = __builtin_amdgcn_permlane32_swap(__float_as_uint(x), __float_as_uint(x), false, false); return vmax2(s[0], s[1]); }
__device__ __forceinline__ void st_dev(const __amdgpu_buffer_rsrc_t rs, unsigned off, u32x4 v) { __builtin_amdgcn_raw_buffer_store_b128(v, rs, off, 0, 16); }
__device__ __forceinline__ u32x4 ld_dev(const __amdgpu_buffer_rsrc_t rs, unsigned off) { return __builtin_amdgcn_raw_buffer_load_b128(rs, off, 0, 16); }
__device__ __forceinline__ void wait_flag(gu32* p, unsigned tag) {
    for (unsigned spins = 0; spins < SPIN_CAP; ++spins) {
        const unsigned v = __hip_atomic_load(p, __ATOMIC_RELAXED, __HIP_MEMORY_SCOPE_AGENT);
        if (__all(v == tag)) break;
        __builtin_amdgcn_s_sleep(1);
    }
}
__device__ __forceinline__ void set_flag(gu32* p, unsigned tag) { __hip_atomic_store(p, tag, __ATOMIC_RELAXED, __HIP_MEMORY_SCOPE_AGENT); }
__device__ __forceinline__ u32x4 gather8(lptr p, int stride) {
    unsigned w[4];
#pragma unroll
    for (int j = 0; j < 4; ++j) w[j] = (unsigned)*(const LAS u16*)(p + 2 * j * stride) | ((unsigned)*(const LAS u16*)(p + (2 * j + 1) * stride) << 16);
    return u32x4{w[0], w[1], w[2], w[3]};
}
template <int V> struct IC { static constexpr int value = V; };
__device__ __forceinline__ int piece_start(int j) { return (9 * j + 1) >> 1; }

__device__ __forceinline__ unsigned flag_request(gu32* p) { unsigned v; asm volatile("global_load_dword %0, %1, off sc1" : "=v"(v) : "v"(p) : "memory"); return v; }
__device__ __forceinline__ void flag_confirm(unsigned v, gu32* p, unsigned tag) {
    asm volatile("" : "+v"(v));
    if (!__all(v == tag)) wait_flag(p, tag);
}

#define STAMP(i) do {} while (0)
#define WSTAMP(i) do {} while (0)

__device__ __forceinline__ void feature_map(const float (&p)[9], unsigned (&o)[9]) {
    float mx = fmaxf(fmaxf(p[0], p[1]), p[2]), mn = fminf(fminf(p[0], p[1]), p[2]);
#pragma unroll
    for (int i = 3; i < 9; i += 2) { mx = fmaxf(fmaxf(mx, p[i]), p[i + 1]); mn = fminf(fminf(mn, p[i]), p[i + 1]); }
    mx = x16max(mx); mn = -x16max(-mn);
    const float mxl = -mx * LOG2E, mnl = mn * LOG2E;
    float a[9], b[9], sa = 0.f, sb = 0.f;
#pragma unroll
    for (int i = 0; i < 9; ++i) { a[i] = __builtin_amdgcn_exp2f(__builtin_fmaf(p[i], LOG2E, mxl)); b[i] = __builtin_amdgcn_exp2f(__builtin_fmaf(p[i], -LOG2E, mnl)); sa += a[i]; sb += b[i]; }
    sa = x16sum(sa); sb = x16sum(sb);
    const float ra = __builtin_amdgcn_rcpf(sa), rb = __builtin_amdgcn_rcpf(sb);
#pragma unroll
    for (int i = 0; i < 9; ++i) { a[i] *= ra; b[i] *= rb; }
    o[0] = cvtpk(a[0], a[1]); o[1] = cvtpk(a[2], a[3]); o[2] = cvtpk(a[4], a[5]); o[3] = cvtpk(a[6], a[7]); o[4] = cvtpk(a[8], b[0]);
    o[5] = cvtpk(b[1], b[2]); o[6] = cvtpk(b[3], b[4]); o[7] = cvtpk(b[5], b[6]); o[8] = cvtpk(b[7], b[8]);
}

__global__ __launch_bounds__(512, 2) void fused_kernel(const char* __restrict__ x, const char* __restrict__ wA, const char* __restrict__ wB, const char* __restrict__ wphi,
                                                       const char* __restrict__ woA, const char* __restrict__ woB, const char* __restrict__ gw,
                                                       char* __restrict__ ws, u16* __restrict__ out) {
    asm volatile("" :: "s"(x), "s"(wA), "s"(wB), "s"(wphi), "s"(woA), "s"(woB), "s"(gw), "s"(ws), "s"(out));
    extern __shared__ __attribute__((aligned(16))) char lds_raw[];
    const lptr lds = (lptr)lds_raw;
    const unsigned lds0 = (unsigned)(uintptr_t)lds_raw;
    const int tid = threadIdx.x, lane = tid & 63, wid = __builtin_amdgcn_readfirstlane(tid >> 6);
    const int b = blockIdx.x, hp = (b & 3) * 4 + ((b >> 3) & 3), rt = ((b >> 2) & 1) * 8 + (b >> 5);
    const bool loader = wid >= 4;
    const int lw = wid & 3;
    const int h = lw >> 1, tg = 2 * (lw & 1) + (wid >> 2);
    const int r16 = lane & 15, g = lane >> 4;
    gu32* flags = (gu32*)(ws + WS_FLAG);
    gu32* F_W = flags;
    gu32* F_WO = flags + 256 * 32;
    gu32* F_KV = flags + 512 * 32;
    gu32* F_P = flags + 768 * 32;
    gu32* F_O = flags + 1024 * 32;
    {
        unsigned long long pc0; unsigned len;
        asm volatile("s_getpc_b64 %0\nfused_code_begin:" : "=s"(pc0));
        asm volatile("s_mov_b32 %0, fused_code_end-fused_code_begin" : "=s"(len));
        if (wid == 3) {
            const unsigned j = blockIdx.x >> 3;
            if ((j + 1) * 1024 <= len) glds16(j * 1024 + lane * 16, (const void*)pc0, lds0 + 160256);
            if ((j + 33) * 1024 <= len) glds16((j + 32) * 1024 + lane * 16, (const void*)pc0, lds0 + 160256);
        }
    }
    STAMP(0);
    unsigned tagv = flag_request((gu32*)(ws + WS_EPOCH));
    const __amdgpu_buffer_rsrc_t wsr = __builtin_amdgcn_make_buffer_rsrc(ws, 0, 0x2000000, 0x00020000);

    const int G0 = piece_start(rt), nG = piece_start(rt + 1) - G0;
    const int j2 = b >> 3, cgp = b & 7, C0 = piece_start(j2), nC = piece_start(j2 + 1) - C0;
#pragma unroll
    for (int i = 0; i < 2; ++i) {
        const int pc = wid + 8 * i;
        if (pc < 15) { int c = pc * 64 + lane; if (c >= nG * 192) c = 0;
            const int row = c / 24, ch = c % 24, w = ch / 3, cc = ch % 3, sg = w >> 1;
            const char* src = (sg < 3 ? wA + sg * 1152 : wB) + (size_t)(8 * G0 + row) * 3456 + (2 * hp + (w & 1)) * 36 + (cc == 2 ? 20 : cc * 16);
            glds16_ptr(src, lds0 + L_WP + pc * 1024); }
        else glds16(G0 * 16 + (lane < nG ? lane : 0) * 16, gw, lds0 + L_GW);
    }
    unsigned xo[4], w0o[3];
    const char* w0base = wB + 1152 + (hp >> 3) * 1152 + (hp & 7) * 144;
    const int krot = (2 * (hp & 3) + (rt & 7)) % 9;
    auto kact = [&](int kb) { const int k = kb + krot; return k >= 9 ? k - 9 : k; };
    auto issue_x = [&](int kb, int slot) {
        const char* src = x + kact(kb) * 128;
#pragma unroll
        for (int i = 0; i < 4; ++i) glds16(xo[i], src, lds0 + L_X + slot * XS + lw * 4096 + i * 1024);
    };
    auto issue_w0 = [&](int kb, int slot) {
        const char* src = w0base + (size_t)kact(kb) * 64 * 3456;
#pragma unroll
        for (int i = 0; i < 3; ++i) glds16(w0o[i], src, lds0 + L_W + slot * WS1 + (lw * 3 + i) * 1024);
    };
    if (loader) {
#pragma unroll
        for (int i = 0; i < 4; ++i) { const int row = 32 * lw + 8 * i + (lane >> 3); xo[i] = (unsigned)(rt * 128 + row) * 1152u + (unsigned)(((lane & 7) ^ ((row >> 1) & 7)) << 4); }
#pragma unroll
        for (int i = 0; i < 3; ++i) { const int c = (lw * 3 + i) * 64 + lane, rho = c / 12, ch = c % 12, hd = ch / 3, cc = ch % 3;
            const int k = (rho & 32) + 8 * ((rho & 15) >> 2) + (rho & 3) + 4 * ((rho >> 4) & 1); w0o[i] = (unsigned)k * 3456u + hd * 36 + (cc == 2 ? 20 : cc * 16); }
        issue_x(0, 0); issue_w0(0, 0);
        issue_x(1, 1); issue_w0(1, 1);
        WAITV(7);
    } else WAITV(0);
    BAR();
    asm volatile("" : "+v"(tagv));
    const unsigned tag = __builtin_amdgcn_readfirstlane(tagv) + 1u;
    STAMP(1);
    if (loader) { issue_x(2, 2); issue_w0(2, 2); }
    auto w1_units = [&](int i) {
        const int q4 = r16 >> 2, p4 = r16 & 3;
        const int U0 = i * 16 + lw * 4 + g, U = U0 < nG * 10 ? U0 : 0, Gi = (U * 26) >> 8, ht = U - 10 * Gi, hh2 = ht >= 5, t = ht - 5 * hh2;
        const int sg = t < 4 ? (0x1032 >> (4 * t)) & 3 : p4;
        const lptr p = lds + L_WP + (8 * Gi + q4) * 384 + (sg * 2 + hh2) * 48 + (t < 4 ? p4 * 8 : 40);
        const s16x4 ra = trrd(p), rb = trrd(p + 4 * 384);
        const u32x4 gv = *(const LAS u32x4*)(lds + L_GW + Gi * 16);
        u32x4 v = __builtin_bit_cast(u32x4, cat8(ra, rb));
        const bool isg = t == 4 && r16 == 12;
        v = u32x4{isg ? gv[0] : v[0], isg ? gv[1] : v[1], isg ? gv[2] : v[2], isg ? gv[3] : v[3]};
        const int G = G0 + Gi;
        const unsigned off = (unsigned)(WS_WT + (size_t)hp * 9 * 20480) + (G >> 3) * 20480 + (ht * 2 + ((G >> 2) & 1)) * 1024 + ((G & 3) * 16 + r16) * 16;
        st_dev(wsr, U0 < nG * 10 ? off : 0xfffffff0u, v);
    };
    const char* wt = ws + WS_WT + (size_t)hp * 9 * 20480;
    auto issue_w1 = [&](int kb, int slot) {
#pragma unroll
        for (int i = 0; i < 5; ++i) glds16_dev((unsigned)(lw * 1024 + lane * 16), wt + kact(kb) * 20480 + i * 4096, lds0 + L_W + slot * WS1 + i * 4096 + lw * 1024);
    };
    u32x4 pkt[2][2];
    if (!loader) {
        {
            f32x4 a0[5][2];
#pragma unroll
            for (int t = 0; t < 5; ++t) { a0[t][0] = f32x4{0, 0, 0, 0}; a0[t][1] = f32x4{0, 0, 0, 0}; }
            const int xr = L_X + (32 * lw + r16) * 128 + ((g ^ (r16 >> 1)) << 4);
            const int q4 = r16 >> 2, p4 = r16 & 3;
            const int wr = L_W + (4 * g + q4) * 192 + p4 * 8, wr4 = L_W + (4 * g + q4) * 192 + p4 * 48 + 40;
            bf16x8 wa[5], xa[2], wb[5], xb[2];
            unsigned fw = 0;
            xa[0] = ld128(lds + xr); xa[1] = ld128(lds + xr + 2048);
#pragma unroll
            for (int t = 0; t < 5; ++t) { const lptr p = lds + (t < 4 ? wr + t * 48 : wr4); wa[t] = cat8(trrd(p), trrd(p + 16 * 192)); }
            int slot = 0;
            for (int kb = 0; kb < 8; ++kb) {
                const lptr sx = lds + slot * XS, sw = lds + slot * WS1 + 32 * 192;
                LGKM0();
                SB();
                xb[0] = ld128(sx + (xr ^ 64)); xb[1] = ld128(sx + (xr ^ 64) + 2048);
#pragma unroll
                for (int t = 0; t < 5; ++t) { const lptr p = sw + (t < 4 ? wr + t * 48 : wr4); wb[t] = cat8(trrd(p), trrd(p + 16 * 192)); }
                SB();
#pragma unroll
                for (int t = 0; t < 5; ++t) { a0[t][0] = MFMA16(wa[t], xa[0], a0[t][0]); a0[t][1] = MFMA16(wa[t], xa[1], a0[t][1]); }
                SB();
                if (kb == 6 && lw == 0) { WAITV(0); flag_confirm(fw, F_W + (hp * 16 + r16) * 32, tag); }
                BAR();
                if (kb == 5 && lw == 0) fw = flag_request(F_W + (hp * 16 + r16) * 32);
                slot = slot == 2 ? 0 : slot + 1;
                const lptr nx = lds + slot * XS, nw = lds + slot * WS1;
                SB();
                bf16x8 xn[2], wn[5];
                xn[0] = ld128(nx + xr); xn[1] = ld128(nx + xr + 2048);
#pragma unroll
                for (int t = 0; t < 5; ++t) { const lptr p = nw + (t < 4 ? wr + t * 48 : wr4); wn[t] = cat8(trrd(p), trrd(p + 16 * 192)); }
                SB();
#pragma unroll
                for (int t = 0; t < 5; ++t) { a0[t][0] = MFMA16(wb[t], xb[0], a0[t][0]); a0[t][1] = MFMA16(wb[t], xb[1], a0[t][1]); }
                SB();
                xa[0] = xn[0]; xa[1] = xn[1];
#pragma unroll
                for (int t = 0; t < 5; ++t) wa[t] = wn[t];
            }
            {
                const lptr sx = lds + 2 * XS, sw = lds + 2 * WS1 + 32 * 192;
                LGKM0();
                SB();
                xb[0] = ld128(sx + (xr ^ 64)); xb[1] = ld128(sx + (xr ^ 64) + 2048);
#pragma unroll
                for (int t = 0; t < 5; ++t) { const lptr p = sw + (t < 4 ? wr + t * 48 : wr4); wb[t] = cat8(trrd(p), trrd(p + 16 * 192)); }
                SB();
#pragma unroll
                for (int t = 0; t < 5; ++t) { a0[t][0] = MFMA16(wa[t], xa[0], a0[t][0]); a0[t][1] = MFMA16(wa[t], xa[1], a0[t][1]); }
                SB();
                BAR();
#pragma unroll
                for (int t = 0; t < 5; ++t) { a0[t][0] = MFMA16(wb[t], xb[0], a0[t][0]); a0[t][1] = MFMA16(wb[t], xb[1], a0[t][1]); }
            }
            STAMP(2);
            const unsigned padv = hp >= 8 ? 0x00003f80u : 0u;
#pragma unroll
            for (int tt = 0; tt < 2; ++tt) {
                const lptr rp = lds + L_STG + (32 * lw + 16 * tt + r16) * 48;
#pragma unroll
                for (int t = 0; t < 4; ++t) *(LAS u32x2*)(rp + t * 6144 + 8 * g) = u32x2{cvtpk(a0[t][tt][0], a0[t][tt][1]), cvtpk(a0[t][tt][2], a0[t][tt][3])};
                *(LAS u32x4*)(rp + g * 6144 + 32) = u32x4{cvtpk(a0[4][tt][2], a0[4][tt][3]), padv, 0u, 0u};
            }
            LGKM0();
            u32x4 kvv[6];
#pragma unroll
            for (int i = 0; i < 6; ++i) { const int e = i * 64 + lane, hd = e / 96, o = e % 96; kvv[i] = *(const LAS u32x4*)(lds + L_STG + hd * 6144 + 32 * lw * 48 + o * 16); }
#pragma unroll
            for (int i = 0; i < 6; ++i) { const int e = i * 64 + lane, hd = e / 96, o = e % 96;
                st_dev(wsr, (unsigned)(WS_KV + (size_t)((rt * 16 + 2 * (hp & 7) + (hd >> 1)) * 4 + (hd & 1) * 2 + (hp >> 3)) * 6144) + 32 * lw * 48 + o * 16, kvv[i]); }
        }
        BAR();
        STAMP(3);
        f32x4 acc[5][4];
#pragma unroll
        for (int t = 0; t < 5; ++t)
#pragma unroll
            for (int u = 0; u < 4; ++u) acc[t][u] = f32x4{0, 0, 0, 0};
        const int th = lw & 1;
        const int xr = L_X + (64 * th + r16) * 128 + ((g ^ (r16 >> 1)) << 4), wr = L_W + h * 10240 + lane * 16;
        bf16x8 wa[5], xa[4], wb[5], xb[4];
        unsigned fk = 0;
        gu32* fkp = F_KV + (((lane & 1) * 8 + (hp >> 1)) * 16 + (rt - (lane >> 1) % 3 >= 0 ? rt - (lane >> 1) % 3 : rt)) * 32;
#pragma unroll
        for (int u = 0; u < 4; ++u) xa[u] = ld128(lds + xr + u * 2048);
#pragma unroll
        for (int t = 0; t < 5; ++t) wa[t] = ld128(lds + wr + t * 2048);
        int slot = 0;
        for (int i = 0; i < 8; ++i) {
            const lptr sx = lds + slot * XS, sw = lds + slot * WS1;
            SB();
#pragma unroll
            for (int u = 0; u < 4; ++u) xb[u] = ld128(sx + (xr ^ 64) + u * 2048);
#pragma unroll
            for (int t = 0; t < 5; ++t) { wb[t] = ld128(sw + wr + t * 2048 + 1024); SB();
#pragma unroll
                for (int u = 0; u < 4; ++u) acc[t][u] = MFMA16(wa[t], xa[u], acc[t][u]);
                SB(); }
            if (i == 2) WAITV(0);
            if (i == 5 && lw == 0) { WAITV(0); flag_confirm(fk, fkp, tag); }
            BAR();
            if (i == 2 && tid == 0) set_flag(F_KV + (hp * 16 + rt) * 32, tag);
            if (i == 4 && lw == 0) fk = flag_request(fkp);
            slot = slot == 2 ? 0 : slot + 1;
            const lptr nx = lds + slot * XS, nw = lds + slot * WS1;
            SB();
#pragma unroll
            for (int u = 0; u < 4; ++u) xa[u] = ld128(nx + xr + u * 2048);
#pragma unroll
            for (int t = 0; t < 5; ++t) { const bf16x8 n = ld128(nw + wr + t * 2048); SB();
#pragma unroll
                for (int u = 0; u < 4; ++u) acc[t][u] = MFMA16(wb[t], xb[u], acc[t][u]);
                SB(); wa[t] = n; }
        }
        {
            const lptr sx = lds + 2 * XS, sw = lds + 2 * WS1;
            SB();
#pragma unroll
            for (int u = 0; u < 4; ++u) xb[u] = ld128(sx + (xr ^ 64) + u * 2048);
#pragma unroll
            for (int t = 0; t < 5; ++t) { wb[t] = ld128(sw + wr + t * 2048 + 1024); SB();
#pragma unroll
                for (int u = 0; u < 4; ++u) acc[t][u] = MFMA16(wa[t], xa[u], acc[t][u]);
                SB(); }
            BAR();
#pragma unroll
            for (int t = 0; t < 5; ++t)
#pragma unroll
                for (int u = 0; u < 4; ++u) acc[t][u] = MFMA16(wb[t], xb[u], acc[t][u]);
        }
        STAMP(4);
        const lptr Qb = lds + L_Q + h * 6144 + (64 * th + r16) * 48, VAb = lds + L_VA + h * 6144 + (64 * th + r16) * 48;
        unsigned st0[4]; float gt[4];
#pragma unroll
        for (int u = 0; u < 4; ++u) {
            unsigned p2[3][2];
#pragma unroll
            for (int t = 0; t < 3; ++t) { p2[t][0] = cvtpk(acc[2 + t][u][0], acc[2 + t][u][1]); p2[t][1] = cvtpk(acc[2 + t][u][2], acc[2 + t][u][3]); }
#pragma unroll
            for (int X = 0; X < 2; ++X) {
                const u32x4 tp = {p2[X][0], p2[X][1], p2[2][0], p2[2][1]};
                if (u < 2) pkt[X][u] = tp; else *(LAS u32x4*)(lds + L_PKX + ((lw * 2 + X) * 2 + (u - 2)) * 1024 + lane * 16) = tp;
            }
            *(LAS u32x2*)(VAb + u * 768 + 8 * g) = u32x2{cvtpk(acc[0][u][0], acc[0][u][1]), cvtpk(acc[0][u][2], acc[0][u][3])};
            *(LAS u32x2*)(Qb + u * 768 + 8 * g) = u32x2{cvtpk(acc[1][u][0] * QSCALE, acc[1][u][1] * QSCALE), cvtpk(acc[1][u][2] * QSCALE, acc[1][u][3] * QSCALE)};
            st0[u] = g == 2 ? p2[2][1] : cvtpk(acc[4][u][2] * QSCALE, acc[4][u][3] * QSCALE);
            gt[u] = __builtin_amdgcn_rcpf(1.f + __builtin_amdgcn_exp2f(-acc[4][u][0] * LOG2E));
        }
        if (g >= 2) {
            const lptr sp = (g == 2 ? VAb : Qb) + 32; const unsigned hi = g == 2 ? 0x00003f80u : 0u;
#pragma unroll
            for (int u = 0; u < 4; ++u) *(LAS u32x2*)(sp + u * 768) = u32x2{st0[u], hi};
            if (g == 3 && h == 0) {
#pragma unroll
                for (int u = 0; u < 4; ++u) *(LAS float*)(lds + L_GATE + (64 * th + 16 * u + r16) * 4) = gt[u];
            }
        }
    } else {
        for (int kb = 0; kb < 9; ++kb) {
            if (kb == 1) WAITV(9); else if (kb == 2) WAITV(11); else if (kb == 3) WAITV(9); else if (kb < 7) WAITV(7); else if (kb == 7) WAITV(5);
            BAR();
            if (kb == 4 && tid == 256) set_flag(F_W + (hp * 16 + rt) * 32, tag);
            if (kb < 6) { issue_x(kb + 3, kb % 3); issue_w0(kb + 3, kb % 3); }
            else issue_w1(kb, kb - 6);
            if (kb < 2) { w1_units(2 * kb); w1_units(2 * kb + 1); }
            if (kb == 7) {
#pragma unroll
                for (int i = 0; i < 2; ++i) { const int pc = lw + 4 * i;
                    if (pc < 6) { int c = pc * 64 + lane; if (c >= nC * 72) c = 0;
                        const int row = c / 9, cc = c % 9, C = C0 + (row >> 3), hq = C / 9, p = 8 * (C % 9) + (row & 7);
                        int br, head, d;
                        if (p < 64) { br = p < 32; head = (p >> 4) & 1; d = p & 15; } else { br = p < 68; head = (p >> 1) & 1; d = 16 + (p & 1); }
                        glds16_ptr((br ? woB : woA) + ((2 * hq + head) * 18 + d) * 1152 + cgp * 144 + cc * 16, lds0 + L_WOP + pc * 1024); }
                    else { int c = (pc - 6) * 64 + lane; if (c >= 108) c = 0; const int row = c / 3, cc = c % 3;
                        glds16((unsigned)(hp * 1296 + row * 36 + (cc == 2 ? 20 : cc * 16)), wphi, lds0 + L_PHI + (pc - 6) * 1024); } }
            }
        }
        WAITV(12);
        BAR();
        for (int i = 0; i < 5; ++i) {
            if (i == 0) WAITV(5); else WAITV(9);
            if (i == 1 && lw == 0 && lane < 4) *(LAS u32x4*)(lds + L_ZERO + lane * 16) = u32x4{0, 0, 0, 0};
            BAR();
            issue_x(i, i % 3); issue_w1(i, i % 3);
        }
        auto kv_copy = [&](int u) { const int ar = u / 18, tl = (u % 18) / 6, o = u % 6, srt = rt - 2 + tl;
            glds16_dev((unsigned)(WS_KV + (size_t)(((srt >= 0 ? srt : rt) * 16 + hp) * 4 + ar) * 6144) + o * 1024 + lane * 16, ws, lds0 + L_K + u * 1024); };
        WAITV(9); BAR();
        issue_x(5, 2); issue_w1(5, 2);
        WAITV(9); BAR();
#pragma unroll
        for (int n = 0; n < 9; ++n) { const int j = 4 * n + lw; kv_copy(j < 16 ? j : j + 32); }
        WAITV(9); BAR();
#pragma unroll
        for (int n = 0; n < 5; ++n) { const int j = 4 * n + lw; kv_copy(j < 16 ? 16 + j : 52 + j); }
#pragma unroll
        for (int n = 0; n < 2; ++n) {
            const int q4 = r16 >> 2, p4 = r16 & 3;
            const int U0 = n * 16 + lw * 4 + g, U = U0 < nC * 5 ? U0 : 0, Ci = (U * 52) >> 8, t = U - 5 * Ci, C = C0 + Ci, kq = (C * 57) >> 11, s = (C - 36 * kq) >> 2, gg = C & 3;
            const lptr p = lds + L_WOP + (8 * Ci + q4) * 144 + (t < 4 ? t * 32 + p4 * 8 : 128 + (p4 & 1) * 8);
            const u32x4 v = __builtin_bit_cast(u32x4, cat8(trrd(p), trrd(p + 4 * 144)));
            const unsigned off = (unsigned)(WS_WOT + (size_t)cgp * 165888) + (s * 4 + kq) * 4608 + (t < 4 ? t * 1024 + (gg * 16 + r16) * 16 : 4096 + (gg * 8 + r16) * 16);
            st_dev(wsr, U0 < nC * 5 && (t < 4 || r16 < 8) ? off : 0xfffffff0u, v);
        }
        BAR();
#pragma unroll
        for (int n = 0; n < 4; ++n) kv_copy(32 + 4 * n + lw);
    }
    BAR();
    STAMP(5);
    const int cg = (b >> 3) & 7, r64 = 4 * (b & 7) + (b >> 6);
    if (loader) {
#pragma unroll
        for (int X = 0; X < 2; ++X)
#pragma unroll
            for (int u = 0; u < 2; ++u) pkt[X][u] = *(const LAS u32x4*)(lds + L_PKX + ((lw * 2 + X) * 2 + u) * 1024 + lane * 16);
    }
    if (loader) __builtin_amdgcn_s_setprio(1);
    unsigned phq[9];
    {
        unsigned phk[9];
        float p[2][9];
        const int q4 = r16 >> 2, p4 = r16 & 3;
        const unsigned pw0 = lds0 + L_PHI + h * 864, zp = lds0 + L_ZERO;
        s16x4 R1[2][3], R2[2][2][3];
#pragma unroll
        for (int hf = 0; hf < 2; ++hf) {
            const bool ok = (p4 >> 1) == hf;
            const lptr a = (lptr)(uintptr_t)(ok ? pw0 + (4 * g + q4) * 48 + 16 * (p4 & 1) : zp), a2 = (lptr)(uintptr_t)(ok ? pw0 + (4 * g + q4) * 48 + 40 : zp);
            R1[hf][0] = trrd(a); R1[hf][1] = trrd(a + 8); R1[hf][2] = trrd(a2);
#pragma unroll
            for (int X = 0; X < 2; ++X) {
                const bool ok2 = ok && g == X && q4 >= 2;
                const lptr c = (lptr)(uintptr_t)(ok2 ? pw0 + (14 + q4) * 48 + 16 * (p4 & 1) : zp), c2 = (lptr)(uintptr_t)(ok2 ? pw0 + (14 + q4) * 48 + 40 : zp);
                R2[X][hf][0] = trrd(c); R2[X][hf][1] = trrd(c + 8); R2[X][hf][2] = trrd(c2);
            }
        }
        SB();
#pragma unroll
        for (int X = 0; X < 2; ++X) {
            const bf16x8 bA = __builtin_bit_cast(bf16x8, pkt[X][0]), bB = __builtin_bit_cast(bf16x8, pkt[X][1]);
            f32x4 d[3];
#pragma unroll
            for (int i = 0; i < 3; ++i) d[i] = MFMA16(cat8(R1[0][i], R2[X][0][i]), bA, (f32x4{0, 0, 0, 0}));
#pragma unroll
            for (int i = 0; i < 3; ++i) d[i] = MFMA16(cat8(R1[1][i], R2[X][1][i]), bB, d[i]);
#pragma unroll
            for (int j = 0; j < 4; ++j) { p[X][j] = d[0][j]; p[X][4 + j] = d[1][j]; }
            p[X][8] = (g & 1) ? d[2][3] : d[2][2];
        }
        feature_map(p[1], phk);
        const lptr pkp = lds + L_PK + h * 10240 + (32 * tg + 16 * (g >> 1) + r16) * 80 + (g & 1) * 40;
        *(LAS u32x2*)(pkp) = u32x2{phk[0], phk[1]}; *(LAS u32x2*)(pkp + 8) = u32x2{phk[2], phk[3]}; *(LAS u32x2*)(pkp + 16) = u32x2{phk[4], phk[5]};
        *(LAS u32x2*)(pkp + 24) = u32x2{phk[6], phk[7]}; *(LAS u32x2*)(pkp + 32) = u32x2{phk[8], 0u};
        feature_map(p[0], phq);
    }
    if (loader) WAITV(0);
    BAR();
    STAMP(6);
    if (wid >= 2) {
        const int w6 = wid - 2, hh = w6 / 3, mt = w6 % 3, q4 = r16 >> 2, p4 = r16 & 3;
        int u = 4 * mt + p4; if (u > 9) u = 9;
        const lptr pa = lds + L_PK + hh * 10240 + (8 * g + q4) * 80 + u * 8;
        const lptr pb0 = lds + L_VA + hh * 6144 + (8 * g + q4) * 48 + p4 * 8;
        const lptr pb1 = lds + L_VA + hh * 6144 + (8 * g + q4) * 48 + 32;
        f32x4 d0 = {0, 0, 0, 0}, d1 = {0, 0, 0, 0};
        bf16x8 a[4], b0[4], b1[4];
#pragma unroll
        for (int ks = 0; ks < 4; ++ks) {
            a[ks] = cat8(trrd(pa + ks * 32 * 80), trrd(pa + ks * 32 * 80 + 4 * 80));
            b0[ks] = cat8(trrd(pb0 + ks * 32 * 48), trrd(pb0 + ks * 32 * 48 + 4 * 48));
            b1[ks] = cat8(trrd(pb1 + ks * 32 * 48), trrd(pb1 + ks * 32 * 48 + 4 * 48));
        }
        SB();
#pragma unroll
        for (int ks = 0; ks < 4; ++ks) { d0 = MFMA16(a[ks], b0[ks], d0); d1 = MFMA16(a[ks], b1[ks], d1); }
        const int uu = 4 * mt + g;
        const unsigned po = (unsigned)(WS_PART + (size_t)(hp * 16 + rt) * 3072 + hh * 1520);
        if (uu < 10) {
            __builtin_amdgcn_raw_buffer_store_b64(u32x2{cvtpk(d0[0], d0[1]), cvtpk(d0[2], d0[3])}, wsr, po + (r16 * 10 + uu) * 8, 0, 16);
            if (r16 < 3) __builtin_amdgcn_raw_buffer_store_b64(u32x2{cvtpk(d1[0], d1[1]), cvtpk(d1[2], d1[3])}, wsr, po + ((16 + r16) * 10 + uu) * 8, 0, 16);
        }
    }
    const int q32 = lane & 31, hh = lane >> 5;
    const char* wot = ws + WS_WOT + (size_t)cg * 165888;
    auto g3_step = [&](int p) { const int s = p + cg; return s >= 9 ? s - 9 : s; };
    f32x16 oacc;
    u32x4 pv[3][4];
    auto pv_load = [&]() {
#pragma unroll
        for (int n = 0; n < 3; ++n) { const int gr = (3 * lw + n) * 16 + r16;
#pragma unroll
            for (int m = 0; m < 4; ++m) pv[n][m] = ld_dev(wsr, (unsigned)(WS_PART + (size_t)(hp * 16 + 4 * m + g) * 3072) + (gr < 190 ? gr : 0) * 16); }
    };
    {
        const lptr Kb = lds + L_K + h * 36864, Vb = Kb + 18432, Qb = lds + L_Q + h * 6144;
        const bf16x8 qf0 = ld128(Qb + (32 * tg + q32) * 48 + 16 * hh);
        const unsigned q16 = *(const LAS unsigned*)(Qb + (32 * tg + q32) * 48 + 32);
        const bf16x8 qf1 = mk8(hh ? 0u : q16, 0u, 0u, 0u);
        const lptr kp = Kb + (32 * tg + q32) * 48;
        const int i16 = lane & 15, dhalf = (lane >> 4) & 1;
        const lptr vp = Vb + (32 * tg + 4 * hh + (i16 >> 2)) * 48 + (dhalf ? 32 + ((i16 & 3) ? 8 : 0) : (i16 & 3) * 8);
        f32x16 negm;
#pragma unroll
        for (int r = 0; r < 16; ++r) { oacc[r] = 0.f; negm[r] = 0.f; }
        bf16x8 k0, k1;
        auto tile = [&](int KT, int KN, bool first, int mask) {
            SB();
            f32x16 s = MFMA32(k0, qf0, negm); s = MFMA32(k1, qf1, s);
            SB();
            const s16x4 va = trrd(vp + (KT * 32) * 48), vb = trrd(vp + (KT * 32 + 8) * 48), vc = trrd(vp + (KT * 32 + 16) * 48), vd = trrd(vp + (KT * 32 + 24) * 48);
            k0 = ld128(kp + KN * 32 * 48 + 16 * hh); k1 = ld128(kp + KN * 32 * 48 + 32);
            SB();
            if (mask) {
#pragma unroll
                for (int r = 0; r < 16; ++r) { const int jj = (r & 3) + 8 * (r >> 2) + 4 * hh; const bool ok = mask == 1 ? jj <= q32 : jj > q32; s[r] = ok ? s[r] : NEGBIG; }
            }
            float m = fmaxf(fmaxf(s[0], s[1]), s[2]);
#pragma unroll
            for (int r = 3; r < 15; r += 2) m = fmaxf(fmaxf(m, s[r]), s[r + 1]);
            m = x32max(fmaxf(m, s[15]));
            if (first || __any(m > THR)) {
                const float dl = first ? m : fmaxf(m, 0.f);
                const float f = first ? 1.f : __builtin_amdgcn_exp2f(-dl);
#pragma unroll
                for (int r = 0; r < 16; ++r) { s[r] -= dl; negm[r] -= dl; if (!first) oacc[r] *= f; }
            }
            unsigned pw[8];
#pragma unroll
            for (int r = 0; r < 8; ++r) pw[r] = cvtpk(__builtin_amdgcn_exp2f(s[2 * r]), __builtin_amdgcn_exp2f(s[2 * r + 1]));
            oacc = MFMA32(cat8(va, vb), mk8(pw[0], pw[1], pw[2], pw[3]), oacc);
            oacc = MFMA32(cat8(vc, vd), mk8(pw[4], pw[5], pw[6], pw[7]), oacc);
        };
        auto wo_piece = [&](int n) { const int pc = n * 4 + lw; if (pc < 54) { const int p = pc / 18, o = pc % 18;
            glds16_dev((unsigned)(o * 1024 + lane * 16), wot + g3_step(p) * G3_SLOT, lds0 + g3_slot(0) + p * G3_SLOT + o * 1024); } };
        auto wo_pieces = [&](int n0, int n1) { if (!loader) for (int n = n0; n < n1; ++n) wo_piece(n); };
        const int lo = rt >= 2 ? 0 : (rt == 1 ? 4 - tg : 8 - tg);
        const int last = lo > 1 ? lo : 1;
        k0 = ld128(kp + 8 * 32 * 48 + 16 * hh); k1 = ld128(kp + 8 * 32 * 48 + 32);
        tile(8, 7, true, 1);
        int KT = 7;
        for (; KT >= last && KT >= 6; --KT) tile(KT, KT - 1, false, 0);
        WAITV(0);
        BAR();
        if (tid == 0) { set_flag(F_P + (hp * 16 + rt) * 32, tag); set_flag(F_WO + b * 32, tag); }
        STAMP(7);
        gu32* fpp = loader ? F_P + (hp * 16 + r16) * 32 : F_WO + (cg + 8 * (lane & 31)) * 32;
        if (rt >= 2) {
            tile(5, 4, false, 0);
            tile(4, 3, false, 0);
            const unsigned fp = flag_request(fpp);
            tile(3, 2, false, 0);
            WAITV(0);
            flag_confirm(fp, fpp, tag);
            if (loader) pv_load(); else wo_pieces(0, 5);
            tile(2, 1, false, 0); wo_pieces(5, 10);
            tile(1, 0, false, 0); wo_pieces(10, 14);
            tile(0, 0, false, 2);
        } else {
            for (; KT >= last; --KT) tile(KT, KT - 1, false, 0);
            wait_flag(fpp, tag);
            if (loader) pv_load(); else wo_pieces(0, 14);
        }
    }
    STAMP(8);
    const unsigned obase = (unsigned)(WS_O + ((size_t)(rt * 8 + 2 * tg) * 144 + 9 * hp) * 256);
    const lptr stg = lds + L_OST;
    {
        const int tokb = 32 * tg + q32;
        const float gb = *(const LAS float*)(lds + L_GATE + tokb * 4);
        if (loader) {
            const unsigned one = (r16 < 8 && (r16 & 1) == 0) ? 0x00003f80u : (r16 < 8 ? 0x3f800000u : 0u);
            const bf16x8 idn = mk8((r16 >> 1) == 0 ? one : 0u, (r16 >> 1) == 1 ? one : 0u, (r16 >> 1) == 2 ? one : 0u, (r16 >> 1) == 3 ? one : 0u);
#pragma unroll
            for (int n = 0; n < 3; ++n) {
                f32x4 sm = {0, 0, 0, 0};
#pragma unroll
                for (int m = 0; m < 4; ++m) sm = MFMA16(idn, __builtin_bit_cast(bf16x8, pv[n][m]), sm);
                const int gr = (3 * lw + n) * 16 + r16, ph = gr >= 95, e0 = gr - 95 * ph, d = (e0 * 205) >> 10, u = 2 * (e0 - 5 * d) + g;
                if (gr < 190 && g < 2) *(LAS u32x2*)(lds + L_KVT + ph * 2016 + d * 96 + (u >= 5 ? 48 + (u - 5) * 8 : u * 8)) = u32x2{cvtpk(sm[0], sm[1]), cvtpk(sm[2], sm[3])};
            }
        } else {
            const int e0 = lw * 64 + lane;
            if (e0 < 76) *(LAS u32x2*)(lds + L_KVT + (e0 / 38) * 2016 + ((e0 % 38) >> 1) * 96 + (e0 & 1) * 48 + 40) = u32x2{0u, 0u};
            else if (e0 < 88) *(LAS u32x4*)(lds + L_KVT + ((e0 - 76) / 6) * 2016 + 20 * 96 + ((e0 - 76) % 6) * 16) = u32x4{0, 0, 0, 0};
        }
        auto sw = __builtin_amdgcn_permlane32_swap(__float_as_uint(oacc[10]), __float_as_uint(oacc[10]), false, false);
        const float sb = (1.f - gb) * __builtin_amdgcn_rcpf(__uint_as_float(sw[0]));
        const auto s0 = __builtin_amdgcn_permlane32_swap(cvtpk(oacc[0] * sb, oacc[1] * sb), cvtpk(oacc[4] * sb, oacc[5] * sb), false, false);
        const auto s1 = __builtin_amdgcn_permlane32_swap(cvtpk(oacc[2] * sb, oacc[3] * sb), cvtpk(oacc[6] * sb, oacc[7] * sb), false, false);
        st_dev(wsr, obase + (q32 >> 4) * 36864 + (2 * h + hh) * 256 + (q32 & 15) * 16, u32x4{s0[0], s1[0], s0[1], s1[1]});
        if (hh == 0) *(LAS unsigned*)(stg + tokb * 16 + h * 4) = cvtpk(oacc[8] * sb, oacc[9] * sb);
    }
    BAR();
    STAMP(9);
    {
        f32x4 dd[3];
        const int eo = (r16 >> 2) & 1;
        const bool mine = (r16 >> 3) == (g >> 1);
        const bf16x8 b0 = mk8(phq[0], phq[1], phq[2], phq[3]), b1 = mk8(phq[4], phq[5], phq[6], phq[7]), b2 = mk8(phq[8], 0u, 0u, 0u);
        bf16x8 af[3][3];
#pragma unroll
        for (int i = 0; i < 3; ++i) {
            const int sg = 4 * i + (r16 & 3);
            const int d = !mine ? 20 : i < 2 ? 8 * eo + sg : sg == 8 ? 16 + eo : sg == 10 ? 18 : 20;
            const lptr ap = lds + L_KVT + h * 2016 + d * 96 + (g & 1) * 48;
            af[i][0] = ld128(ap); af[i][1] = ld128(ap + 16); af[i][2] = ld128(ap + 32);
        }
        const int tok = 32 * tg + 16 * (g >> 1) + r16;
        const float gate = *(const LAS float*)(lds + L_GATE + tok * 4);
        SB();
#pragma unroll
        for (int i = 0; i < 3; ++i) { f32x4 a = MFMA16(af[i][0], b0, (f32x4{0, 0, 0, 0})); a = MFMA16(af[i][1], b1, a); dd[i] = MFMA16(af[i][2], b2, a); }
        const float sc = gate * __builtin_amdgcn_rcpf(dd[2][2] + 1e-6f);
        st_dev(wsr, obase + (g >> 1) * 36864 + (4 + 2 * h + (g & 1)) * 256 + r16 * 16,
               u32x4{cvtpk(dd[0][0] * sc, dd[0][1] * sc), cvtpk(dd[0][2] * sc, dd[0][3] * sc), cvtpk(dd[1][0] * sc, dd[1][1] * sc), cvtpk(dd[1][2] * sc, dd[1][3] * sc)});
        *(LAS u16*)(stg + tok * 16 + 8 + h * 4 + (g & 1) * 2) = (u16)cvtpk(dd[2][0] * sc, 0.f);
    }
    BAR();
    if (tid < 128) st_dev(wsr, (unsigned)(WS_O + ((size_t)(rt * 8 + (tid >> 4)) * 144 + 9 * hp + 8) * 256) + (tid & 15) * 16, *(const LAS u32x4*)(stg + tid * 16));
    STAMP(10);
    {
        const int rt3 = r64 >> 1, rh = r64 & 1, kq = wid >> 1, rp = wid & 1;
        auto issue3 = [&](int p) {
            if (wid > 0) {
#pragma unroll
                for (int i = 0; i < 3; ++i) { const int pc = (wid - 1) + 7 * i;
                    if (pc < 18) glds16_dev((unsigned)(pc * 1024 + lane * 16), wot + g3_step(p) * G3_SLOT, lds0 + g3_slot(p) + pc * 1024); }
            }
        };
        issue3(3); issue3(4);
        if (wid == 0) WAITV(0); else if (wid < 5) WAITV(6); else WAITV(4);
        BAR();
        if (tid == 0) set_flag(F_O + (rt * 16 + hp) * 32, tag);
        issue3(5); issue3(6); issue3(7);
        STAMP(11);
        if (wid == 0) wait_flag(F_O + (rt3 * 16 + r16) * 32, tag);
        DRAIN_BAR();
        STAMP(12);
        u32x4 of[9][2];
        const unsigned ob = (unsigned)(WS_O + ((size_t)(rt3 * 8 + 4 * rh + 2 * rp) * 144 + 36 * kq) * 256 + lane * 16);
        auto o_load = [&](int p) { const unsigned o = ob + g3_step(p) * 1024; of[p][0] = ld_dev(wsr, o); of[p][1] = ld_dev(wsr, o + 36864); };
        o_load(0); o_load(1); o_load(2);
        f32x4 a3[5][2];
#pragma unroll
        for (int t = 0; t < 5; ++t) { a3[t][0] = f32x4{0, 0, 0, 0}; a3[t][1] = f32x4{0, 0, 0, 0}; }
        const int wro = kq * 4608 + lane * 16, wr4 = kq * 4608 + 4096 + g * 128 + (r16 & 7) * 16;
        bf16x8 wf[5];
#pragma unroll
        for (int t = 0; t < 4; ++t) wf[t] = ld128(lds + g3_slot(0) + wro + t * 1024);
        wf[4] = ld128(lds + g3_slot(0) + wr4);
#pragma unroll
        for (int p = 0; p < 9; ++p) {
            if (p == 1) { BAR(); issue3(8); }
            if (p == 7) { WAITV(2); BAR(); }
            if (p + 3 < 9) o_load(p + 3);
            const lptr sp = lds + g3_slot(p + 1 < 9 ? p + 1 : 8);
            const bf16x8 bo0 = __builtin_bit_cast(bf16x8, of[p][0]), bo1 = __builtin_bit_cast(bf16x8, of[p][1]);
            SB();
#pragma unroll
            for (int t = 0; t < 5; ++t) {
                bf16x8 nw = wf[t];
                if (p + 1 < 9) nw = ld128(sp + (t < 4 ? wro + t * 1024 : wr4));
                SB();
                a3[t][0] = MFMA16(wf[t], bo0, a3[t][0]); a3[t][1] = MFMA16(wf[t], bo1, a3[t][1]);
                SB();
                wf[t] = nw;
            }
        }
        BAR();
        STAMP(13);
        const lptr rp0 = lds + rp * 40960 + lane * 16;
        char* orow = (char*)out + (size_t)(rt3 * 128 + 64 * rh + 32 * rp + r16) * 1152 + cg * 144 + g * 8;
        auto finish = [&](auto KQ) {
            constexpr int q = decltype(KQ)::value;
#pragma unroll
            for (int n = 0; n < 10; ++n) if ((n & 3) != q) *(LAS f32x4*)(rp0 + (n * 4 + q) * 1024) = a3[n % 5][n / 5];
            BAR();
            f32x4 pr[3][3];
#pragma unroll
            for (int j = 0; j < 3; ++j) if (q + 4 * j < 10) {
#pragma unroll
                for (int o = 0; o < 3; ++o) pr[j][o] = *(const LAS f32x4*)(rp0 + ((q + 4 * j) * 4 + ((q + 1 + o) & 3)) * 1024); }
#pragma unroll
            for (int j = 0; j < 3; ++j) if (q + 4 * j < 10) { const int n = q + 4 * j, t = n % 5, tt = n / 5;
                const f32x4 u = (a3[t][tt] + pr[j][0]) + (pr[j][1] + pr[j][2]);
                if (t < 4 || g < 2) *(u32x2*)(orow + tt * 16 * 1152 + t * 32) = u32x2{cvtpk(u[0], u[1]), cvtpk(u[2], u[3])};
            }
        };
        if (kq == 0) finish(IC<0>{}); else if (kq == 1) finish(IC<1>{}); else if (kq == 2) finish(IC<2>{}); else finish(IC<3>{});
        STAMP(14);
        asm volatile("fused_code_end:");
        if (b == 0 && tid == 0) set_flag((gu32*)(ws + WS_EPOCH), tag);
    }
}

extern "C" void kernel_launch(void* const* d_in, const int* in_sizes, int n_in, void* d_out, int out_size, void* d_ws, size_t ws_size, hipStream_t stream) {
    (void)hipFuncSetAttribute((const void*)fused_kernel, hipFuncAttributeMaxDynamicSharedMemorySize, LDS_BYTES);
    fused_kernel<<<256, 512, LDS_BYTES, stream>>>((const char*)d_in[0], (const char*)d_in[1], (const char*)d_in[5], (const char*)d_in[2], (const char*)d_in[3],
                                                  (const char*)d_in[6], (const char*)d_in[7], (char*)d_ws, (u16*)d_out);
}
```

```cpp
#include <hip/hip_runtime.h>
#include <hip/hip_bf16.h>
#include <stdint.h>

typedef unsigned short u16;
typedef short bf16x8 __attribute__((ext_vector_type(8)));
typedef short s16x4 __attribute__((ext_vector_type(4)));
typedef float f32x4 __attribute__((ext_vector_type(4)));
typedef float f32x16 __attribute__((ext_vector_type(16)));
typedef unsigned u32x4 __attribute__((ext_vector_type(4)));
typedef unsigned u32x2 __attribute__((ext_vector_type(2)));
typedef __bf16 bf16x2_t __attribute__((ext_vector_type(2)));
typedef float f32x2 __attribute__((ext_vector_type(2)));
#define LAS __attribute__((address_space(3)))
#define GAS __attribute__((address_space(1)))
typedef LAS char* lptr;
typedef GAS unsigned gu32;

constexpr size_t WS_EPOCH = 0;
constexpr size_t WS_FLAG = 4096;
constexpr size_t WS_WT   = 4096 + 5 * 256 * 128;
constexpr size_t WS_WOT  = WS_WT + 16 * 9 * 28672;
constexpr size_t WS_KV   = WS_WOT + 8 * 165888;
constexpr size_t WS_PART = WS_KV + 256 * 24576;
constexpr size_t WS_O    = WS_PART + 256 * 6144;
constexpr size_t WS_DBG  = WS_O + 16 * 8 * 36864;

constexpr int XS = 16384, WS1 = 28672;
constexpr int L_X = 0, L_W = 3 * XS;
constexpr int L_WP = L_W + 2 * WS1;
constexpr int L_GW = L_WP + 23 * 1024;
constexpr int L_PHI = L_W + 3 * WS1;
constexpr int L_ZERO = L_PHI + 1792;
constexpr int L_WOP = L_PHI + 2048;
constexpr int L_PKX = 118784;
constexpr int L_CNT = 152064;
constexpr int L_K = 0;
constexpr int L_Q = 73728;
constexpr int L_VA = 86016;
constexpr int L_PK = 98304;
constexpr int L_GATE = 147456;
constexpr int L_KVT = 147968;
constexpr int L_OST = 73728;
constexpr int LDS_BYTES = 160 * 1024;
constexpr int G3_SLOT = 18432;
__device__ __forceinline__ constexpr int g3_slot(int p) { return (p < 3 ? 5 + p : p < 8 ? p - 3 : 5) * G3_SLOT; }
constexpr unsigned SPIN_CAP = 1u << 18;
constexpr float LOG2E = 1.4426950408889634f;
constexpr float QSCALE = 0.23570226039551584f * LOG2E;
constexpr float THR = 8.f;
constexpr float NEGBIG = -1e30f;

__device__ __forceinline__ unsigned cvtpk(float lo, float hi) { const f32x2 v = {lo, hi}; return __builtin_bit_cast(unsigned, __builtin_convertvector(v, bf16x2_t)); }
__device__ __forceinline__ void glds16(unsigned voff, const void* sbase, unsigned lds_addr) {
    unsigned sv;
    asm volatile("s_mov_b32 %0, m0\n\ts_mov_b32 m0, %3\n\ts_nop 0\n\tglobal_load_lds_dwordx4 %1, %2\n\ts_mov_b32 m0, %0" : "=&s"(sv) : "v"(voff), "s"(sbase), "s"(lds_addr) : "memory");
}
__device__ __forceinline__ void glds16_dev(unsigned voff, const void* sbase, unsigned lds_addr) {
    unsigned sv;
    asm volatile("s_mov_b32 %0, m0\n\ts_mov_b32 m0, %3\n\ts_nop 0\n\tglobal_load_lds_dwordx4 %1, %2 sc1\n\ts_mov_b32 m0, %0" : "=&s"(sv) : "v"(voff), "s"(sbase), "s"(lds_addr) : "memory");
}
__device__ __forceinline__ void glds16_ptr(const void* g, unsigned lds_addr) {
    unsigned sv;
    asm volatile("s_mov_b32 %0, m0\n\ts_mov_b32 m0, %2\n\ts_nop 0\n\tglobal_load_lds_dwordx4 %1, off\n\ts_mov_b32 m0, %0" : "=&s"(sv) : "v"(g), "s"(lds_addr) : "memory");
}
#define WAITV(n) asm volatile("s_waitcnt vmcnt(" #n ")" ::: "memory")
#define WAITL() asm volatile("s_waitcnt lgkmcnt(0)" ::: "memory")
#define LGKM0() do { __builtin_amdgcn_s_waitcnt(0xC07F); asm volatile("" ::: "memory"); } while (0)
#define BAR() do { asm volatile("" ::: "memory"); __builtin_amdgcn_s_waitcnt(0xC07F); __builtin_amdgcn_s_barrier(); asm volatile("" ::: "memory"); } while (0)
#define DRAIN_BAR() do { asm volatile("s_waitcnt vmcnt(0)" ::: "memory"); BAR(); } while (0)
#define SB() __builtin_amdgcn_sched_barrier(0)
#define MFMA16(a, b, c) __builtin_amdgcn_mfma_f32_16x16x32_bf16(a, b, c, 0, 0, 0)
#define MFMA32(a, b, c) __builtin_amdgcn_mfma_f32_32x32x16_bf16(a, b, c, 0, 0, 0)
__device__ __forceinline__ bf16x8 ld128(lptr p) { return *(const LAS bf16x8*)p; }
__device__ __forceinline__ s16x4 trrd(lptr p) { typedef short v4i16_t __attribute__((ext_vector_type(4))); return __builtin_bit_cast(s16x4, __builtin_amdgcn_ds_read_tr16_b64_v4i16((LAS v4i16_t*)p)); }
__device__ __forceinline__ bf16x8 cat8(s16x4 a, s16x4 b) { return __builtin_shufflevector(a, b, 0, 1, 2, 3, 4, 5, 6, 7); }
__device__ __forceinline__ bf16x8 mk8(unsigned a, unsigned b, unsigned c, unsigned d) { u32x4 w = {a, b, c, d}; return __builtin_bit_cast(bf16x8, w); }
__device__ __forceinline__ float vmax2(unsigned a, unsigned b) { float r; asm("v_max_f32 %0, %1, %2" : "=v"(r) : "v"(a), "v"(b)); return r; }
__device__ __forceinline__ float x16max(float x) { auto s = __builtin_amdgcn_permlane16_swap(__float_as_uint(x), __float_as_uint(x), false, false); return vmax2(s[0], s[1]); }
__device__ __forceinline__ float x16sum(float x) { auto s = __builtin_amdgcn_permlane16_swap(__float_as_uint(x), __float_as_uint(x), false, false); return __uint_as_float(s[0]) + __uint_as_float(s[1]); }
__device__ __forceinline__ float x32max(float x) { auto s = __builtin_amdgcn_permlane32_swap(__float_as_uint(x), __float_as_uint(x), false, false); return vmax2(s[0], s[1]); }
__device__ __forceinline__ void st_dev(const __amdgpu_buffer_rsrc_t rs, unsigned off, u32x4 v) { __builtin_amdgcn_raw_buffer_store_b128(v, rs, off, 0, 16); }
__device__ __forceinline__ u32x4 ld_dev(const __amdgpu_buffer_rsrc_t rs, unsigned off) { return __builtin_amdgcn_raw_buffer_load_b128(rs, off, 0, 16); }
__device__ __forceinline__ void wait_flag(gu32* p, unsigned tag) {
    for (unsigned spins = 0; spins < SPIN_CAP; ++spins) {
        const unsigned v = __hip_atomic_load(p, __ATOMIC_RELAXED, __HIP_MEMORY_SCOPE_AGENT);
        if (__all(v == tag)) break;
        __builtin_amdgcn_s_sleep(1);
    }
}
__device__ __forceinline__ void set_flag(gu32* p, unsigned tag) { __hip_atomic_store(p, tag, __ATOMIC_RELAXED, __HIP_MEMORY_SCOPE_AGENT); }
__device__ __forceinline__ u32x4 gather8(lptr p, int stride) {
    unsigned w[4];
#pragma unroll
    for (int j = 0; j < 4; ++j) w[j] = (unsigned)*(const LAS u16*)(p + 2 * j * stride) | ((unsigned)*(const LAS u16*)(p + (2 * j + 1) * stride) << 16);
    return u32x4{w[0], w[1], w[2], w[3]};
}
template <int V> struct IC { static constexpr int value = V; };
__device__ __forceinline__ int piece_start(int j) { return (9 * j + 1) >> 1; }

__device__ __forceinline__ unsigned flag_request(gu32* p) { unsigned v; asm volatile("global_load_dword %0, %1, off sc1" : "=v"(v) : "v"(p) : "memory"); return v; }
__device__ __forceinline__ void flag_confirm(unsigned v, gu32* p, unsigned tag) {
    asm volatile("" : "+v"(v));
    if (!__all(v == tag)) wait_flag(p, tag);
}

#define STAMP(i) do {} while (0)
#define WSTAMP(i) do {} while (0)

__device__ __forceinline__ void feature_map(const float (&p)[9], unsigned (&o)[9]) {
    float mx = fmaxf(fmaxf(p[0], p[1]), p[2]), mn = fminf(fminf(p[0], p[1]), p[2]);
#pragma unroll
    for (int i = 3; i < 9; i += 2) { mx = fmaxf(fmaxf(mx, p[i]), p[i + 1]); mn = fminf(fminf(mn, p[i]), p[i + 1]); }
    mx = x16max(mx); mn = -x16max(-mn);
    const float mxl = -mx * LOG2E, mnl = mn * LOG2E;
    float a[9], b[9], sa = 0.f, sb = 0.f;
#pragma unroll
    for (int i = 0; i < 9; ++i) { a[i] = __builtin_amdgcn_exp2f(__builtin_fmaf(p[i], LOG2E, mxl)); b[i] = __builtin_amdgcn_exp2f(__builtin_fmaf(p[i], -LOG2E, mnl)); sa += a[i]; sb += b[i]; }
    sa = x16sum(sa); sb = x16sum(sb);
    const float ra = __builtin_amdgcn_rcpf(sa), rb = __builtin_amdgcn_rcpf(sb);
#pragma unroll
    for (int i = 0; i < 9; ++i) { a[i] *= ra; b[i] *= rb; }
    o[0] = cvtpk(a[0], a[1]); o[1] = cvtpk(a[2], a[3]); o[2] = cvtpk(a[4], a[5]); o[3] = cvtpk(a[6], a[7]); o[4] = cvtpk(a[8], b[0]);
    o[5] = cvtpk(b[1], b[2]); o[6] = cvtpk(b[3], b[4]); o[7] = cvtpk(b[5], b[6]); o[8] = cvtpk(b[7], b[8]);
}

__global__ __launch_bounds__(512, 2) void fused_kernel(const char* __restrict__ x, const char* __restrict__ wA, const char* __restrict__ wB, const char* __restrict__ wphi,
                                                       const char* __restrict__ woA, const char* __restrict__ woB, const char* __restrict__ gw,
                                                       char* __restrict__ ws, u16* __restrict__ out) {
    asm volatile("" :: "s"(x), "s"(wA), "s"(wB), "s"(wphi), "s"(woA), "s"(woB), "s"(gw), "s"(ws), "s"(out));
    extern __shared__ __attribute__((aligned(16))) char lds_raw[];
    const lptr lds = (lptr)lds_raw;
    const unsigned lds0 = (unsigned)(uintptr_t)lds_raw;
    const int tid = threadIdx.x, lane = tid & 63, wid = __builtin_amdgcn_readfirstlane(tid >> 6);
    const int b = blockIdx.x, hp = (b & 3) * 4 + ((b >> 3) & 3), rt = ((b >> 2) & 1) * 8 + (b >> 5);
    const bool loader = wid >= 4;
    const int lw = wid & 3;
    const int h = lw >> 1, tg = loader ? 3 - 2 * (lw & 1) : 2 * (lw & 1);
    const int r16 = lane & 15, g = lane >> 4;
    gu32* flags = (gu32*)(ws + WS_FLAG);
    gu32* F_W = flags;
    gu32* F_WO = flags + 256 * 32;
    gu32* F_KV = flags + 512 * 32;
    gu32* F_P = flags + 768 * 32;
    gu32* F_O = flags + 1024 * 32;
    {
        unsigned long long pc0; unsigned len;
        asm volatile("s_getpc_b64 %0\nfused_code_begin:" : "=s"(pc0));
        asm volatile("s_mov_b32 %0, fused_code_end-fused_code_begin" : "=s"(len));
        if (wid == 3) {
            const unsigned j = blockIdx.x >> 3;
            if ((j + 1) * 1024 <= len) glds16(j * 1024 + lane * 16, (const void*)pc0, lds0 + 160256);
            if ((j + 33) * 1024 <= len) glds16((j + 32) * 1024 + lane * 16, (const void*)pc0, lds0 + 160256);
        }
    }
    STAMP(0);
    unsigned tagv = flag_request((gu32*)(ws + WS_EPOCH));
    const __amdgpu_buffer_rsrc_t wsr = __builtin_amdgcn_make_buffer_rsrc(ws, 0, 0x2000000, 0x00020000);

    const int G0 = piece_start(rt), nG = piece_start(rt + 1) - G0;
    const int j2 = b >> 3, cgp = b & 7, C0 = piece_start(j2), nC = piece_start(j2 + 1) - C0;
#pragma unroll
    for (int i = 0; i < 3; ++i) {
        const int pc = wid + 8 * i;
        if (pc < 23) { int c = pc * 64 + lane; if (c >= nG * 288) c = 0;
            const int row = c / 36, ch = c % 36, w = ch / 3, cc = ch % 3, sg = w >> 1;
            const char* src = (sg < 3 ? wA + sg * 1152 : wB + (sg - 3) * 1152) + (size_t)(8 * G0 + row) * 3456 + (2 * hp + (w & 1)) * 36 + (cc == 2 ? 20 : cc * 16);
            glds16_ptr(src, lds0 + L_WP + pc * 1024); }
        else glds16(G0 * 16 + (lane < nG ? lane : 0) * 16, gw, lds0 + L_GW);
    }
    const int krot = (2 * (hp & 3) + (rt & 7)) % 9;
    auto kact = [&](int kb) { const int k = kb + krot; return k >= 9 ? k - 9 : k; };
    unsigned xo[4];
    auto issue_x = [&](int kb, int slot) {
        const char* src = x + kact(kb) * 128;
#pragma unroll
        for (int i = 0; i < 4; ++i) glds16(xo[i], src, lds0 + L_X + slot * XS + lw * 4096 + i * 1024);
    };
    if (loader) {
#pragma unroll
        for (int i = 0; i < 4; ++i) { const int row = 32 * lw + 8 * i + (lane >> 3); xo[i] = (unsigned)(rt * 128 + row) * 1152u + (unsigned)(((lane & 7) ^ ((row >> 1) & 7)) << 4); }
        issue_x(0, 0);
        WAITV(4);
    } else WAITV(0);
    BAR();
    asm volatile("" : "+v"(tagv));
    const unsigned tag = __builtin_amdgcn_readfirstlane(tagv) + 1u;
    STAMP(1);
    {
        const int q4 = r16 >> 2, p4 = r16 & 3;
        const unsigned wtb = (unsigned)(WS_WT + (size_t)hp * 9 * 28672);
        u32x4 v[3]; unsigned off[3];
#pragma unroll
        for (int i = 0; i < 2; ++i) {
            const int U0 = i * 32 + wid * 4 + g, U = U0 < nG * 12 ? U0 : 0, Gi = (U * 43) >> 9, ht = U - 12 * Gi, hh2 = ht >= 6, t = ht - 6 * hh2;
            const lptr p = lds + L_WP + (8 * Gi + q4) * 576 + (((0x541032 >> (4 * t)) & 7) * 2 + hh2) * 48 + p4 * 8;
            v[i] = __builtin_bit_cast(u32x4, cat8(trrd(p), trrd(p + 4 * 576)));
            const int G = G0 + Gi;
            off[i] = U0 < nG * 12 ? wtb + (G >> 3) * 28672 + ((hh2 * 7 + t) * 2 + ((G >> 2) & 1)) * 1024 + ((G & 3) * 16 + r16) * 16 : 0xfffffff0u;
        }
        {
            const int U0 = wid * 4 + g, U = U0 < nG * 2 ? U0 : 0, Gi = U >> 1, hh2 = U & 1;
            const u32x4 gt = gather8(lds + L_WP + 8 * Gi * 576 + ((r16 < 12 ? r16 >> 1 : 0) * 2 + hh2) * 48 + 44 + 2 * (r16 & 1), 576);
            const u32x4 gv = *(const LAS u32x4*)(lds + L_GW + Gi * 16);
            v[2] = r16 < 12 ? gt : r16 == 12 ? gv : u32x4{0, 0, 0, 0};
            const int G = G0 + Gi;
            off[2] = U0 < nG * 2 ? wtb + (G >> 3) * 28672 + ((hh2 * 7 + 6) * 2 + ((G >> 2) & 1)) * 1024 + ((G & 3) * 16 + r16) * 16 : 0xfffffff0u;
        }
#pragma unroll
        for (int i = 0; i < 3; ++i) st_dev(wsr, off[i], v[i]);
    }
    if (loader) { issue_x(1, 1); issue_x(2, 2); WAITV(8); } else WAITV(0);
    BAR();
    if (tid == 0) { set_flag(F_W + (hp * 16 + rt) * 32, tag); *(LAS u32x2*)(lds + L_CNT) = u32x2{0u, 0u}; }
    STAMP(2);
    const char* wt = ws + WS_WT + (size_t)hp * 9 * 28672;
    auto issue_w = [&](int kb, int slot) {
#pragma unroll
        for (int i = 0; i < 7; ++i) glds16_dev((unsigned)(lw * 1024 + lane * 16), wt + kact(kb) * 28672 + i * 4096, lds0 + L_W + slot * WS1 + i * 4096 + lw * 1024);
    };
    if (loader) {
#pragma unroll
        for (int i = 0; i < 2; ++i) { const int pc = lw + 4 * i;
            if (pc < 6) { int c = pc * 64 + lane; if (c >= nC * 72) c = 0;
                const int row = c / 9, cc = c % 9, C = C0 + (row >> 3), hq = C / 9, p = 8 * (C % 9) + (row & 7);
                int br, head, d;
                if (p < 64) { br = p < 32; head = (p >> 4) & 1; d = p & 15; } else { br = p < 68; head = (p >> 1) & 1; d = 16 + (p & 1); }
                glds16_ptr((br ? woB : woA) + ((2 * hq + head) * 18 + d) * 1152 + cgp * 144 + cc * 16, lds0 + L_WOP + pc * 1024); }
            else { int c = (pc - 6) * 64 + lane; if (c >= 108) c = 0; const int row = c / 3, cc = c % 3;
                glds16((unsigned)(hp * 1296 + row * 36 + (cc == 2 ? 20 : cc * 16)), wphi, lds0 + L_PHI + (pc - 6) * 1024); } }
    } else if (wid == 0) wait_flag(F_W + (hp * 16 + r16) * 32, tag);
    BAR();
    STAMP(3);
    u32x4 pkt[2][2];
    if (!loader) {
        f32x4 acc[7][4];
#pragma unroll
        for (int t = 0; t < 7; ++t)
#pragma unroll
            for (int u = 0; u < 4; ++u) acc[t][u] = f32x4{0, 0, 0, 0};
        const int th = lw & 1;
        const int xr = L_X + (64 * th + r16) * 128 + ((g ^ (r16 >> 1)) << 4), wr = L_W + h * 14336 + lane * 16;
        bf16x8 wa[7], xa[4], wb[7], xb[4];
        BAR();
#pragma unroll
        for (int u = 0; u < 4; ++u) xa[u] = ld128(lds + xr + u * 2048);
#pragma unroll
        for (int t = 0; t < 7; ++t) wa[t] = ld128(lds + wr + t * 2048);
        int slot = 0;
        for (int i = 0; i < 8; ++i) {
            const lptr sx = lds + slot * XS, sw = lds + slot * WS1;
            SB();
#pragma unroll
            for (int u = 0; u < 4; ++u) xb[u] = ld128(sx + (xr ^ 64) + u * 2048);
#pragma unroll
            for (int t = 0; t < 7; ++t) { wb[t] = ld128(sw + wr + t * 2048 + 1024); SB();
#pragma unroll
                for (int u = 0; u < 4; ++u) acc[t][u] = MFMA16(wa[t], xa[u], acc[t][u]);
                SB(); }
            BAR();
            slot = slot == 2 ? 0 : slot + 1;
            const lptr nx = lds + slot * XS, nw = lds + slot * WS1;
            SB();
#pragma unroll
            for (int u = 0; u < 4; ++u) xa[u] = ld128(nx + xr + u * 2048);
#pragma unroll
            for (int t = 0; t < 7; ++t) { const bf16x8 n = ld128(nw + wr + t * 2048); SB();
#pragma unroll
                for (int u = 0; u < 4; ++u) acc[t][u] = MFMA16(wb[t], xb[u], acc[t][u]);
                SB(); wa[t] = n; }
        }
        {
            const lptr sx = lds + 2 * XS, sw = lds + 2 * WS1;
            SB();
#pragma unroll
            for (int u = 0; u < 4; ++u) xb[u] = ld128(sx + (xr ^ 64) + u * 2048);
#pragma unroll
            for (int t = 0; t < 7; ++t) { wb[t] = ld128(sw + wr + t * 2048 + 1024); SB();
#pragma unroll
                for (int u = 0; u < 4; ++u) acc[t][u] = MFMA16(wa[t], xa[u], acc[t][u]);
                SB(); }
            BAR();
#pragma unroll
            for (int t = 0; t < 7; ++t)
#pragma unroll
                for (int u = 0; u < 4; ++u) acc[t][u] = MFMA16(wb[t], xb[u], acc[t][u]);
        }
        STAMP(4);
        const int row0 = (64 * th + r16) * 48;
        const lptr Kb = lds + L_K + h * 36864 + 256 * 48 + row0, Vb = Kb + 18432, Qb = lds + L_Q + h * 6144 + row0, VAb = lds + L_VA + h * 6144 + row0;
#pragma unroll
        for (int u = 0; u < 4; ++u) {
            *(LAS u32x2*)(Kb + u * 768 + 8 * g) = u32x2{cvtpk(acc[4][u][0], acc[4][u][1]), cvtpk(acc[4][u][2], acc[4][u][3])};
            *(LAS u32x2*)(Vb + u * 768 + 8 * g) = u32x2{cvtpk(acc[5][u][0], acc[5][u][1]), cvtpk(acc[5][u][2], acc[5][u][3])};
        }
        if (g == 2) {
#pragma unroll
            for (int u = 0; u < 4; ++u) {
                *(LAS u32x4*)(Kb + u * 768 + 32) = u32x4{cvtpk(acc[6][u][0], acc[6][u][1]), 0u, 0u, 0u};
                *(LAS u32x4*)(Vb + u * 768 + 32) = u32x4{cvtpk(acc[6][u][2], acc[6][u][3]), 0x00003f80u, 0u, 0u};
            }
        }
        unsigned st0[4], st1[4]; float gt[4];
#pragma unroll
        for (int u = 0; u < 4; ++u) {
            const unsigned s0 = cvtpk(acc[6][u][0], acc[6][u][1]), s1 = cvtpk(acc[6][u][2], acc[6][u][3]);
#pragma unroll
            for (int X = 0; X < 2; ++X) {
                const u32x4 tp = {cvtpk(acc[2 + X][u][0], acc[2 + X][u][1]), cvtpk(acc[2 + X][u][2], acc[2 + X][u][3]), s0, s1};
                if (u < 2) pkt[X][u] = tp; else *(LAS u32x4*)(lds + L_PKX + ((lw * 2 + X) * 2 + (u - 2)) * 1024 + lane * 16) = tp;
            }
            *(LAS u32x2*)(VAb + u * 768 + 8 * g) = u32x2{cvtpk(acc[0][u][0], acc[0][u][1]), cvtpk(acc[0][u][2], acc[0][u][3])};
            *(LAS u32x2*)(Qb + u * 768 + 8 * g) = u32x2{cvtpk(acc[1][u][0] * QSCALE, acc[1][u][1] * QSCALE), cvtpk(acc[1][u][2] * QSCALE, acc[1][u][3] * QSCALE)};
            st0[u] = s0; st1[u] = cvtpk(acc[6][u][2] * QSCALE, acc[6][u][3] * QSCALE);
            gt[u] = __builtin_amdgcn_rcpf(1.f + __builtin_amdgcn_exp2f(-acc[6][u][0] * LOG2E));
        }
        if (g == 1) {
#pragma unroll
            for (int u = 0; u < 4; ++u) { *(LAS u32x2*)(VAb + u * 768 + 32) = u32x2{st0[u], 0x00003f80u}; *(LAS unsigned*)(Qb + u * 768 + 32) = st1[u]; }
        } else if (g == 3 && h == 0) {
#pragma unroll
            for (int u = 0; u < 4; ++u) *(LAS float*)(lds + L_GATE + (64 * th + 16 * u + r16) * 4) = gt[u];
        }
    } else {
        issue_w(0, 0); issue_w(1, 1); issue_w(2, 2);
        WAITV(14);
        BAR();
        for (int i = 0; i < 8; ++i) {
            if (i == 0) WAITV(7); else if (i < 7) WAITV(11); else WAITV(1);
            if (i == 1 && lw == 0 && lane < 4) *(LAS u32x4*)(lds + L_ZERO + lane * 16) = u32x4{0, 0, 0, 0};
            BAR();
            if (i < 6) { issue_x(i + 3, i % 3); issue_w(i + 3, i % 3); }
            if (i == 6 || i == 7) {
                const int q4 = r16 >> 2, p4 = r16 & 3;
                const int U0 = (i - 6) * 16 + lw * 4 + g, U = U0 < nC * 5 ? U0 : 0, Ci = (U * 52) >> 8, t = U - 5 * Ci, C = C0 + Ci, kq = (C * 57) >> 11, s = (C - 36 * kq) >> 2, gg = C & 3;
                const lptr p = lds + L_WOP + (8 * Ci + q4) * 144 + (t < 4 ? t * 32 + p4 * 8 : 128 + (p4 & 1) * 8);
                const u32x4 v = __builtin_bit_cast(u32x4, cat8(trrd(p), trrd(p + 4 * 144)));
                const unsigned off = (unsigned)(WS_WOT + (size_t)cgp * 165888) + (s * 4 + kq) * 4608 + (t < 4 ? t * 1024 + (gg * 16 + r16) * 16 : 4096 + (gg * 8 + r16) * 16);
                st_dev(wsr, U0 < nC * 5 && (t < 4 || r16 < 8) ? off : 0xfffffff0u, v);
            }
        }
        BAR();
    }
    BAR();
    STAMP(5);
    const int cg = (b >> 3) & 7, r64 = 4 * (b & 7) + (b >> 6);
    if (loader) {
#pragma unroll
        for (int X = 0; X < 2; ++X)
#pragma unroll
            for (int u = 0; u < 2; ++u) pkt[X][u] = *(const LAS u32x4*)(lds + L_PKX + (((lw ^ 1) * 2 + X) * 2 + u) * 1024 + lane * 16);
    } else {
        u32x4 kvv[6];
        const lptr kr = lds + L_K + h * 36864 + (256 + 64 * (lw & 1)) * 48 + lane * 16;
#pragma unroll
        for (int i = 0; i < 3; ++i) { kvv[i] = *(const LAS u32x4*)(kr + i * 1024); kvv[3 + i] = *(const LAS u32x4*)(kr + 18432 + i * 1024); }
        const unsigned ko = (unsigned)(WS_KV + (size_t)((rt * 16 + hp) * 4 + h * 2) * 6144) + (lw & 1) * 3072 + lane * 16;
#pragma unroll
        for (int i = 0; i < 3; ++i) { st_dev(wsr, ko + i * 1024, kvv[i]); st_dev(wsr, ko + 6144 + i * 1024, kvv[3 + i]); }
    }
    unsigned phq[9];
    float p[2][9];
    {
        unsigned phk[9];
        const int q4 = r16 >> 2, p4 = r16 & 3;
        const unsigned pw0 = lds0 + L_PHI + h * 864, zp = lds0 + L_ZERO;
        s16x4 R1[2][3], R2[2][2][3];
#pragma unroll
        for (int hf = 0; hf < 2; ++hf) {
            const bool ok = (p4 >> 1) == hf;
            const lptr a = (lptr)(uintptr_t)(ok ? pw0 + (4 * g + q4) * 48 + 16 * (p4 & 1) : zp), a2 = (lptr)(uintptr_t)(ok ? pw0 + (4 * g + q4) * 48 + 40 : zp);
            R1[hf][0] = trrd(a); R1[hf][1] = trrd(a + 8); R1[hf][2] = trrd(a2);
#pragma unroll
            for (int X = 0; X < 2; ++X) {
                const bool ok2 = ok && g == 0 && (q4 >> 1) == X;
                const lptr c = (lptr)(uintptr_t)(ok2 ? pw0 + (16 + (q4 & 1)) * 48 + 16 * (p4 & 1) : zp), c2 = (lptr)(uintptr_t)(ok2 ? pw0 + (16 + (q4 & 1)) * 48 + 40 : zp);
                R2[X][hf][0] = trrd(c); R2[X][hf][1] = trrd(c + 8); R2[X][hf][2] = trrd(c2);
            }
        }
        SB();
#pragma unroll
        for (int X = 0; X < 2; ++X) {
            const bf16x8 bA = __builtin_bit_cast(bf16x8, pkt[X][0]), bB = __builtin_bit_cast(bf16x8, pkt[X][1]);
            f32x4 d[3];
#pragma unroll
            for (int i = 0; i < 3; ++i) d[i] = MFMA16(cat8(R1[0][i], R2[X][0][i]), bA, (f32x4{0, 0, 0, 0}));
#pragma unroll
            for (int i = 0; i < 3; ++i) d[i] = MFMA16(cat8(R1[1][i], R2[X][1][i]), bB, d[i]);
#pragma unroll
            for (int j = 0; j < 4; ++j) { p[X][j] = d[0][j]; p[X][4 + j] = d[1][j]; }
            p[X][8] = (g & 1) ? d[2][3] : d[2][2];
        }
        feature_map(p[1], phk);
        const lptr pkp = lds + L_PK + h * 10240 + (32 * tg + 16 * (g >> 1) + r16) * 80 + (g & 1) * 40;
        *(LAS u32x2*)(pkp) = u32x2{phk[0], phk[1]}; *(LAS u32x2*)(pkp + 8) = u32x2{phk[2], phk[3]}; *(LAS u32x2*)(pkp + 16) = u32x2{phk[4], phk[5]};
        *(LAS u32x2*)(pkp + 24) = u32x2{phk[6], phk[7]}; *(LAS u32x2*)(pkp + 32) = u32x2{phk[8], 0u};
        WAITV(0);
        if (!loader && lane == 0 && __hip_atomic_fetch_add((LAS unsigned*)(lds + L_CNT), 1u, __ATOMIC_RELAXED, __HIP_MEMORY_SCOPE_WORKGROUP) == 3u) set_flag(F_KV + (hp * 16 + rt) * 32, tag);
    }
    BAR();
    if (tid == 0) set_flag(F_WO + b * 32, tag);
    STAMP(6);
    const bool fetcher = !loader && (lw & 1) == 0 && rt > 0;
    gu32* fkp = F_KV + (hp * 16 + rt - 1 - (rt > 1 ? (lane & 1) : 0)) * 32;
    unsigned fk = 0;
    if (fetcher) fk = flag_request(fkp);
    if (wid != 0 && wid != 2) {
        const int w6 = wid == 1 ? 0 : wid - 2, hh = w6 / 3, mt = w6 % 3, q4 = r16 >> 2, p4 = r16 & 3;
        int u = 4 * mt + p4; if (u > 9) u = 9;
        const lptr pa = lds + L_PK + hh * 10240 + (8 * g + q4) * 80 + u * 8;
        const lptr pb0 = lds + L_VA + hh * 6144 + (8 * g + q4) * 48 + p4 * 8;
        const lptr pb1 = lds + L_VA + hh * 6144 + (8 * g + q4) * 48 + 32;
        f32x4 d0 = {0, 0, 0, 0}, d1 = {0, 0, 0, 0};
        bf16x8 a[4], b0[4], b1[4];
#pragma unroll
        for (int ks = 0; ks < 4; ++ks) {
            a[ks] = cat8(trrd(pa + ks * 32 * 80), trrd(pa + ks * 32 * 80 + 4 * 80));
            b0[ks] = cat8(trrd(pb0 + ks * 32 * 48), trrd(pb0 + ks * 32 * 48 + 4 * 48));
            b1[ks] = cat8(trrd(pb1 + ks * 32 * 48), trrd(pb1 + ks * 32 * 48 + 4 * 48));
        }
        SB();
#pragma unroll
        for (int ks = 0; ks < 4; ++ks) { d0 = MFMA16(a[ks], b0[ks], d0); d1 = MFMA16(a[ks], b1[ks], d1); }
        const int uu = 4 * mt + g;
        const unsigned po = (unsigned)(WS_PART + (size_t)(hp * 16 + rt) * 3072 + hh * 1520);
        if (uu < 10) {
            __builtin_amdgcn_raw_buffer_store_b64(u32x2{cvtpk(d0[0], d0[1]), cvtpk(d0[2], d0[3])}, wsr, po + (r16 * 10 + uu) * 8, 0, 16);
            if (r16 < 3) __builtin_amdgcn_raw_buffer_store_b64(u32x2{cvtpk(d1[0], d1[1]), cvtpk(d1[2], d1[3])}, wsr, po + ((16 + r16) * 10 + uu) * 8, 0, 16);
        }
    }
    const int q32 = lane & 31, hh = lane >> 5;
    const char* wot = ws + WS_WOT + (size_t)cg * 165888;
    auto g3_step = [&](int p) { const int s = p + cg; return s >= 9 ? s - 9 : s; };
    f32x16 oacc;
    u32x4 pv[3][4];
    auto pv_load = [&]() {
#pragma unroll
        for (int n = 0; n < 3; ++n) { const int gr = (3 * lw + n) * 16 + r16;
#pragma unroll
            for (int m = 0; m < 4; ++m) pv[n][m] = ld_dev(wsr, (unsigned)(WS_PART + (size_t)(hp * 16 + 4 * m + g) * 3072) + (gr < 190 ? gr : 0) * 16); }
    };
    {
        const lptr Kb = lds + L_K + h * 36864, Vb = Kb + 18432, Qb = lds + L_Q + h * 6144;
        const bf16x8 qf0 = ld128(Qb + (32 * tg + q32) * 48 + 16 * hh);
        const unsigned q16 = *(const LAS unsigned*)(Qb + (32 * tg + q32) * 48 + 32);
        const bf16x8 qf1 = mk8(hh ? 0u : q16, 0u, 0u, 0u);
        const lptr kp = Kb + (32 * tg + q32) * 48;
        const int i16 = lane & 15, dhalf = (lane >> 4) & 1;
        const lptr vp = Vb + (32 * tg + 4 * hh + (i16 >> 2)) * 48 + (dhalf ? 32 + ((i16 & 3) ? 8 : 0) : (i16 & 3) * 8);
        f32x16 negm;
#pragma unroll
        for (int r = 0; r < 16; ++r) { oacc[r] = 0.f; negm[r] = 0.f; }
        bf16x8 k0, k1;
        auto tile = [&](int KT, int KN, bool first, int mask) {
            SB();
            f32x16 s = MFMA32(k0, qf0, negm); s = MFMA32(k1, qf1, s);
            SB();
            const s16x4 va = trrd(vp + (KT * 32) * 48), vb = trrd(vp + (KT * 32 + 8) * 48), vc = trrd(vp + (KT * 32 + 16) * 48), vd = trrd(vp + (KT * 32 + 24) * 48);
            k0 = ld128(kp + KN * 32 * 48 + 16 * hh); k1 = ld128(kp + KN * 32 * 48 + 32);
            SB();
            if (mask) {
#pragma unroll
                for (int r = 0; r < 16; ++r) { const int jj = (r & 3) + 8 * (r >> 2) + 4 * hh; const bool ok = mask == 1 ? jj <= q32 : jj > q32; s[r] = ok ? s[r] : NEGBIG; }
            }
            float m = fmaxf(fmaxf(s[0], s[1]), s[2]);
#pragma unroll
            for (int r = 3; r < 15; r += 2) m = fmaxf(fmaxf(m, s[r]), s[r + 1]);
            m = x32max(fmaxf(m, s[15]));
            if (first || __any(m > THR)) {
                const float dl = first ? m : fmaxf(m, 0.f);
                const float f = first ? 1.f : __builtin_amdgcn_exp2f(-dl);
#pragma unroll
                for (int r = 0; r < 16; ++r) { s[r] -= dl; negm[r] -= dl; if (!first) oacc[r] *= f; }
            }
            unsigned pw[8];
#pragma unroll
            for (int r = 0; r < 8; ++r) pw[r] = cvtpk(__builtin_amdgcn_exp2f(s[2 * r]), __builtin_amdgcn_exp2f(s[2 * r + 1]));
            oacc = MFMA32(cat8(va, vb), mk8(pw[0], pw[1], pw[2], pw[3]), oacc);
            oacc = MFMA32(cat8(vc, vd), mk8(pw[4], pw[5], pw[6], pw[7]), oacc);
        };
        auto wo_piece = [&](int n) { const int pc = n * 4 + lw; if (pc < 54) { const int p = pc / 18, o = pc % 18;
            glds16_dev((unsigned)(o * 1024 + lane * 16), wot + g3_step(p) * G3_SLOT, lds0 + g3_slot(0) + p * G3_SLOT + o * 1024); } };
        auto wo_pieces = [&](int n0, int n1) { if (!loader) for (int n = n0; n < n1; ++n) wo_piece(n); };
        const int lo = rt >= 2 ? 0 : (rt == 1 ? 4 - tg : 8 - tg);
        const int last = lo > 1 ? lo : 1;
        k0 = ld128(kp + 8 * 32 * 48 + 16 * hh); k1 = ld128(kp + 8 * 32 * 48 + 32);
        tile(8, 7, true, 1);
        u32x4 hv[24];
        if (fetcher) {
            WAITV(0);
            flag_confirm(fk, fkp, tag);
#pragma unroll
            for (int n = 0; n < 24; ++n) { const int pc = 2 * n + (lw >> 1), tl = n >= 12, ar = (pc - 24 * tl) / 6, o = pc - 24 * tl - 6 * ar;
                hv[n] = ld_dev(wsr, (unsigned)(WS_KV + (size_t)(((rt - 2 + tl >= 0 ? rt - 2 + tl : 0) * 16 + hp) * 4 + ar) * 6144) + o * 1024 + lane * 16); }
        }
        for (int KT = 7; KT >= 8 - tg; --KT) tile(KT, KT - 1, false, 0);
        if (wid != 0 && wid != 2) {
            WAITV(0);
            if (lane == 0 && __hip_atomic_fetch_add((LAS unsigned*)(lds + L_CNT + 4), 1u, __ATOMIC_RELAXED, __HIP_MEMORY_SCOPE_WORKGROUP) == 5u) set_flag(F_P + (hp * 16 + rt) * 32, tag);
        }
        feature_map(p[0], phq);
        if (fetcher) {
#pragma unroll
            for (int n = 0; n < 24; ++n) { const int pc = 2 * n + (lw >> 1), tl = n >= 12, ar = (pc - 24 * tl) / 6, o = pc - 24 * tl - 6 * ar;
                *(LAS u32x4*)(lds + L_K + ar * 18432 + tl * 6144 + o * 1024 + lane * 16) = hv[n]; }
        }
        BAR();
        STAMP(7);
        gu32* fpp = loader ? F_P + (hp * 16 + r16) * 32 : F_WO + (cg + 8 * (lane & 31)) * 32;
        { const int KT = 7 - tg; k0 = ld128(kp + KT * 32 * 48 + 16 * hh); k1 = ld128(kp + KT * 32 * 48 + 32); }
        if (rt >= 2) {
            for (int KT = 7 - tg; KT >= 4; --KT) tile(KT, KT - 1, false, 0);
            const unsigned fp = flag_request(fpp);
            tile(3, 2, false, 0);
            WAITV(0);
            flag_confirm(fp, fpp, tag);
            if (loader) pv_load(); else wo_pieces(0, 5);
            tile(2, 1, false, 0); wo_pieces(5, 10);
            tile(1, 0, false, 0); wo_pieces(10, 14);
            tile(0, 0, false, 2);
        } else {
            if (rt == 1) for (int KT = 7 - tg; KT >= last; --KT) tile(KT, KT - 1, false, 0);
            wait_flag(fpp, tag);
            if (loader) pv_load(); else wo_pieces(0, 14);
        }
    }
    STAMP(8);
    const unsigned obase = (unsigned)(WS_O + ((size_t)(rt * 8 + 2 * tg) * 144 + 9 * hp) * 256);
    const lptr stg = lds + L_OST;
    {
        const int tokb = 32 * tg + q32;
        const float gb = *(const LAS float*)(lds + L_GATE + tokb * 4);
        if (loader) {
            const unsigned one = (r16 < 8 && (r16 & 1) == 0) ? 0x00003f80u : (r16 < 8 ? 0x3f800000u : 0u);
            const bf16x8 idn = mk8((r16 >> 1) == 0 ? one : 0u, (r16 >> 1) == 1 ? one : 0u, (r16 >> 1) == 2 ? one : 0u, (r16 >> 1) == 3 ? one : 0u);
#pragma unroll
            for (int n = 0; n < 3; ++n) {
                f32x4 sm = {0, 0, 0, 0};
#pragma unroll
                for (int m = 0; m < 4; ++m) sm = MFMA16(idn, __builtin_bit_cast(bf16x8, pv[n][m]), sm);
                const int gr = (3 * lw + n) * 16 + r16, ph = gr >= 95, e0 = gr - 95 * ph, d = (e0 * 205) >> 10, u = 2 * (e0 - 5 * d) + g;
                if (gr < 190 && g < 2) *(LAS u32x2*)(lds + L_KVT + ph * 2016 + d * 96 + (u >= 5 ? 48 + (u - 5) * 8 : u * 8)) = u32x2{cvtpk(sm[0], sm[1]), cvtpk(sm[2], sm[3])};
            }
        } else {
            const int e0 = lw * 64 + lane;
            if (e0 < 76) *(LAS u32x2*)(lds + L_KVT + (e0 / 38) * 2016 + ((e0 % 38) >> 1) * 96 + (e0 & 1) * 48 + 40) = u32x2{0u, 0u};
            else if (e0 < 88) *(LAS u32x4*)(lds + L_KVT + ((e0 - 76) / 6) * 2016 + 20 * 96 + ((e0 - 76) % 6) * 16) = u32x4{0, 0, 0, 0};
        }
        auto sw = __builtin_amdgcn_permlane32_swap(__float_as_uint(oacc[10]), __float_as_uint(oacc[10]), false, false);
        const float sb = (1.f - gb) * __builtin_amdgcn_rcpf(__uint_as_float(sw[0]));
        const auto s0 = __builtin_amdgcn_permlane32_swap(cvtpk(oacc[0] * sb, oacc[1] * sb), cvtpk(oacc[4] * sb, oacc[5] * sb), false, false);
        const auto s1 = __builtin_amdgcn_permlane32_swap(cvtpk(oacc[2] * sb, oacc[3] * sb), cvtpk(oacc[6] * sb, oacc[7] * sb), false, false);
        st_dev(wsr, obase + (q32 >> 4) * 36864 + (2 * h + hh) * 256 + (q32 & 15) * 16, u32x4{s0[0], s1[0], s0[1], s1[1]});
        if (hh == 0) *(LAS unsigned*)(stg + tokb * 16 + h * 4) = cvtpk(oacc[8] * sb, oacc[9] * sb);
    }
    BAR();
    STAMP(9);
    {
        f32x4 dd[3];
        const int eo = (r16 >> 2) & 1;
        const bool mine = (r16 >> 3) == (g >> 1);
        const bf16x8 b0 = mk8(phq[0], phq[1], phq[2], phq[3]), b1 = mk8(phq[4], phq[5], phq[6], phq[7]), b2 = mk8(phq[8], 0u, 0u, 0u);
        bf16x8 af[3][3];
#pragma unroll
        for (int i = 0; i < 3; ++i) {
            const int sg = 4 * i + (r16 & 3);
            const int d = !mine ? 20 : i < 2 ? 8 * eo + sg : sg == 8 ? 16 + eo : sg == 10 ? 18 : 20;
            const lptr ap = lds + L_KVT + h * 2016 + d * 96 + (g & 1) * 48;
            af[i][0] = ld128(ap); af[i][1] = ld128(ap + 16); af[i][2] = ld128(ap + 32);
        }
        const int tok = 32 * tg + 16 * (g >> 1) + r16;
        const float gate = *(const LAS float*)(lds + L_GATE + tok * 4);
        SB();
#pragma unroll
        for (int i = 0; i < 3; ++i) { f32x4 a = MFMA16(af[i][0], b0, (f32x4{0, 0, 0, 0})); a = MFMA16(af[i][1], b1, a); dd[i] = MFMA16(af[i][2], b2, a); }
        const float sc = gate * __builtin_amdgcn_rcpf(dd[2][2] + 1e-6f);
        st_dev(wsr, obase + (g >> 1) * 36864 + (4 + 2 * h + (g & 1)) * 256 + r16 * 16,
               u32x4{cvtpk(dd[0][0] * sc, dd[0][1] * sc), cvtpk(dd[0][2] * sc, dd[0][3] * sc), cvtpk(dd[1][0] * sc, dd[1][1] * sc), cvtpk(dd[1][2] * sc, dd[1][3] * sc)});
        *(LAS u16*)(stg + tok * 16 + 8 + h * 4 + (g & 1) * 2) = (u16)cvtpk(dd[2][0] * sc, 0.f);
    }
    BAR();
    if (tid < 128) st_dev(wsr, (unsigned)(WS_O + ((size_t)(rt * 8 + (tid >> 4)) * 144 + 9 * hp + 8) * 256) + (tid & 15) * 16, *(const LAS u32x4*)(stg + tid * 16));
    STAMP(10);
    {
        const int rt3 = r64 >> 1, rh = r64 & 1, kq = wid >> 1, rp = wid & 1;
        auto issue3 = [&](int p) {
            if (wid > 0) {
#pragma unroll
                for (int i = 0; i < 3; ++i) { const int pc = (wid - 1) + 7 * i;
                    if (pc < 18) glds16_dev((unsigned)(pc * 1024 + lane * 16), wot + g3_step(p) * G3_SLOT, lds0 + g3_slot(p) + pc * 1024); }
            }
        };
        issue3(3); issue3(4);
        if (wid == 0) WAITV(0); else if (wid < 5) WAITV(6); else WAITV(4);
        BAR();
        if (tid == 0) set_flag(F_O + (rt * 16 + hp) * 32, tag);
        issue3(5); issue3(6); issue3(7);
        STAMP(11);
        if (wid == 0) wait_flag(F_O + (rt3 * 16 + r16) * 32, tag);
        DRAIN_BAR();
        STAMP(12);
        u32x4 of[9][2];
        const unsigned ob = (unsigned)(WS_O + ((size_t)(rt3 * 8 + 4 * rh + 2 * rp) * 144 + 36 * kq) * 256 + lane * 16);
        auto o_load = [&](int p) { const unsigned o = ob + g3_step(p) * 1024; of[p][0] = ld_dev(wsr, o); of[p][1] = ld_dev(wsr, o + 36864); };
        o_load(0); o_load(1); o_load(2);
        f32x4 a3[5][2];
#pragma unroll
        for (int t = 0; t < 5; ++t) { a3[t][0] = f32x4{0, 0, 0, 0}; a3[t][1] = f32x4{0, 0, 0, 0}; }
        const int wro = kq * 4608 + lane * 16, wr4 = kq * 4608 + 4096 + g * 128 + (r16 & 7) * 16;
        bf16x8 wf[5];
#pragma unroll
        for (int t = 0; t < 4; ++t) wf[t] = ld128(lds + g3_slot(0) + wro + t * 1024);
        wf[4] = ld128(lds + g3_slot(0) + wr4);
#pragma unroll
        for (int p = 0; p < 9; ++p) {
            if (p == 1) { BAR(); issue3(8); }
            if (p == 7) { WAITV(2); BAR(); }
            if (p + 3 < 9) o_load(p + 3);
            const lptr sp = lds + g3_slot(p + 1 < 9 ? p + 1 : 8);
            const bf16x8 bo0 = __builtin_bit_cast(bf16x8, of[p][0]), bo1 = __builtin_bit_cast(bf16x8, of[p][1]);
            SB();
#pragma unroll
            for (int t = 0; t < 5; ++t) {
                bf16x8 nw = wf[t];
                if (p + 1 < 9) nw = ld128(sp + (t < 4 ? wro + t * 1024 : wr4));
                SB();
                a3[t][0] = MFMA16(wf[t], bo0, a3[t][0]); a3[t][1] = MFMA16(wf[t], bo1, a3[t][1]);
                SB();
                wf[t] = nw;
            }
        }
        BAR();
        STAMP(13);
        const lptr rp0 = lds + rp * 40960 + lane * 16;
        char* orow = (char*)out + (size_t)(rt3 * 128 + 64 * rh + 32 * rp + r16) * 1152 + cg * 144 + g * 8;
        auto finish = [&](auto KQ) {
            constexpr int q = decltype(KQ)::value;
#pragma unroll
            for (int n = 0; n < 10; ++n) if ((n & 3) != q) *(LAS f32x4*)(rp0 + (n * 4 + q) * 1024) = a3[n % 5][n / 5];
            BAR();
            f32x4 pr[3][3];
#pragma unroll
            for (int j = 0; j < 3; ++j) if (q + 4 * j < 10) {
#pragma unroll
                for (int o = 0; o < 3; ++o) pr[j][o] = *(const LAS f32x4*)(rp0 + ((q + 4 * j) * 4 + ((q + 1 + o) & 3)) * 1024); }
#pragma unroll
            for (int j = 0; j < 3; ++j) if (q + 4 * j < 10) { const int n = q + 4 * j, t = n % 5, tt = n / 5;
                const f32x4 u = (a3[t][tt] + pr[j][0]) + (pr[j][1] + pr[j][2]);
                if (t < 4 || g < 2) *(u32x2*)(orow + tt * 16 * 1152 + t * 32) = u32x2{cvtpk(u[0], u[1]), cvtpk(u[2], u[3])};
            }
        };
        if (kq == 0) finish(IC<0>{}); else if (kq == 1) finish(IC<1>{}); else if (kq == 2) finish(IC<2>{}); else finish(IC<3>{});
        STAMP(14);
        asm volatile("fused_code_end:");
        if (b == 0 && tid == 0) set_flag((gu32*)(ws + WS_EPOCH), tag);
    }
}

extern "C" void kernel_launch(void* const* d_in, const int* in_sizes, int n_in, void* d_out, int out_size, void* d_ws, size_t ws_size, hipStream_t stream) {
    (void)hipFuncSetAttribute((const void*)fused_kernel, hipFuncAttributeMaxDynamicSharedMemorySize, LDS_BYTES);
    fused_kernel<<<256, 512, LDS_BYTES, stream>>>((const char*)d_in[0], (const char*)d_in[1], (const char*)d_in[5], (const char*)d_in[2], (const char*)d_in[3],
                                                  (const char*)d_in[6], (const char*)d_in[7], (char*)d_ws, (u16*)d_out);
}
```

```cpp
#include <hip/hip_runtime.h>
#include <hip/hip_bf16.h>
#include <stdint.h>

typedef unsigned short u16;
typedef short bf16x8 __attribute__((ext_vector_type(8)));
typedef short s16x4 __attribute__((ext_vector_type(4)));
typedef float f32x4 __attribute__((ext_vector_type(4)));
typedef float f32x16 __attribute__((ext_vector_type(16)));
typedef unsigned u32x4 __attribute__((ext_vector_type(4)));
typedef unsigned u32x2 __attribute__((ext_vector_type(2)));
typedef __bf16 bf16x2_t __attribute__((ext_vector_type(2)));
typedef float f32x2 __attribute__((ext_vector_type(2)));
#define LAS __attribute__((address_space(3)))
#define GAS __attribute__((address_space(1)))
typedef LAS char* lptr;
typedef GAS unsigned gu32;

constexpr size_t WS_EPOCH = 0;
constexpr size_t WS_FLAG = 4096;
constexpr size_t WS_WT   = 4096 + 5 * 256 * 128;
constexpr size_t WS_WOT  = WS_WT + 16 * 9 * 28672;
constexpr size_t WS_KV   = WS_WOT + 8 * 165888;
constexpr size_t WS_PART = WS_KV + 256 * 24576;
constexpr size_t WS_O    = WS_PART + 256 * 6144;
constexpr size_t WS_DBG  = WS_O + 16 * 8 * 36864;

constexpr int XS = 16384, WS1 = 28672;
constexpr int L_X = 0, L_W = 3 * XS;
constexpr int L_WP = L_W + 2 * WS1;
constexpr int L_GW = L_WP + 23 * 1024;
constexpr int L_PHI = L_W + 3 * WS1;
constexpr int L_ZERO = L_PHI + 1792;
constexpr int L_WOP = L_PHI + 2048;
constexpr int L_PKX = 118784;
constexpr int L_CNT = 152064;
constexpr int L_K = 0;
constexpr int L_Q = 73728;
constexpr int L_VA = 86016;
constexpr int L_PK = 98304;
constexpr int L_GATE = 147456;
constexpr int L_KVT = 147968;
constexpr int L_OST = 73728;
constexpr int LDS_BYTES = 160 * 1024;
constexpr int G3_SLOT = 18432;
__device__ __forceinline__ constexpr int g3_slot(int p) { return (p < 3 ? 5 + p : p < 8 ? p - 3 : 5) * G3_SLOT; }
constexpr unsigned SPIN_CAP = 1u << 18;
constexpr float LOG2E = 1.4426950408889634f;
constexpr float QSCALE = 0.23570226039551584f * LOG2E;
constexpr float THR = 8.f;
constexpr float NEGBIG = -1e30f;

__device__ __forceinline__ unsigned cvtpk(float lo, float hi) { const f32x2 v = {lo, hi}; return __builtin_bit_cast(unsigned, __builtin_convertvector(v, bf16x2_t)); }
__device__ __forceinline__ void glds16(unsigned voff, const void* sbase, unsigned lds_addr) {
    unsigned sv;
    asm volatile("s_mov_b32 %0, m0\n\ts_mov_b32 m0, %3\n\ts_nop 0\n\tglobal_load_lds_dwordx4 %1, %2\n\ts_mov_b32 m0, %0" : "=&s"(sv) : "v"(voff), "s"(sbase), "s"(lds_addr) : "memory");
}
__device__ __forceinline__ void glds16_dev(unsigned voff, const void* sbase, unsigned lds_addr) {
    unsigned sv;
    asm volatile("s_mov_b32 %0, m0\n\ts_mov_b32 m0, %3\n\ts_nop 0\n\tglobal_load_lds_dwordx4 %1, %2 sc1\n\ts_mov_b32 m0, %0" : "=&s"(sv) : "v"(voff), "s"(sbase), "s"(lds_addr) : "memory");
}
__device__ __forceinline__ void glds16_ptr(const void* g, unsigned lds_addr) {
    unsigned sv;
    asm volatile("s_mov_b32 %0, m0\n\ts_mov_b32 m0, %2\n\ts_nop 0\n\tglobal_load_lds_dwordx4 %1, off\n\ts_mov_b32 m0, %0" : "=&s"(sv) : "v"(g), "s"(lds_addr) : "memory");
}
#define WAITV(n) asm volatile("s_waitcnt vmcnt(" #n ")" ::: "memory")
#define WAITL() asm volatile("s_waitcnt lgkmcnt(0)" ::: "memory")
#define LGKM0() do { __builtin_amdgcn_s_waitcnt(0xC07F); asm volatile("" ::: "memory"); } while (0)
#define BAR() do { asm volatile("" ::: "memory"); __builtin_amdgcn_s_waitcnt(0xC07F); __builtin_amdgcn_s_barrier(); asm volatile("" ::: "memory"); } while (0)
#define DRAIN_BAR() do { asm volatile("s_waitcnt vmcnt(0)" ::: "memory"); BAR(); } while (0)
#define SB() __builtin_amdgcn_sched_barrier(0)
#define MFMA16(a, b, c) __builtin_amdgcn_mfma_f32_16x16x32_bf16(a, b, c, 0, 0, 0)
#define MFMA32(a, b, c) __builtin_amdgcn_mfma_f32_32x32x16_bf16(a, b, c, 0, 0, 0)
__device__ __forceinline__ bf16x8 ld128(lptr p) { return *(const LAS bf16x8*)p; }
__device__ __forceinline__ s16x4 trrd(lptr p) { typedef short v4i16_t __attribute__((ext_vector_type(4))); return __builtin_bit_cast(s16x4, __builtin_amdgcn_ds_read_tr16_b64_v4i16((LAS v4i16_t*)p)); }
__device__ __forceinline__ bf16x8 cat8(s16x4 a, s16x4 b) { return __builtin_shufflevector(a, b, 0, 1, 2, 3, 4, 5, 6, 7); }
__device__ __forceinline__ bf16x8 mk8(unsigned a, unsigned b, unsigned c, unsigned d) { u32x4 w = {a, b, c, d}; return __builtin_bit_cast(bf16x8, w); }
__device__ __forceinline__ float vmax2(unsigned a, unsigned b) { float r; asm("v_max_f32 %0, %1, %2" : "=v"(r) : "v"(a), "v"(b)); return r; }
__device__ __forceinline__ float x16max(float x) { auto s = __builtin_amdgcn_permlane16_swap(__float_as_uint(x), __float_as_uint(x), false, false); return vmax2(s[0], s[1]); }
__device__ __forceinline__ float x16sum(float x) { auto s = __builtin_amdgcn_permlane16_swap(__float_as_uint(x), __float_as_uint(x), false, false); return __uint_as_float(s[0]) + __uint_as_float(s[1]); }
__device__ __forceinline__ float x32max(float x) { auto s = __builtin_amdgcn_permlane32_swap(__float_as_uint(x), __float_as_uint(x), false, false); return vmax2(s[0], s[1]); }
__device__ __forceinline__ void st_dev(const __amdgpu_buffer_rsrc_t rs, unsigned off, u32x4 v) { __builtin_amdgcn_raw_buffer_store_b128(v, rs, off, 0, 16); }
__device__ __forceinline__ u32x4 ld_dev(const __amdgpu_buffer_rsrc_t rs, unsigned off) { return __builtin_amdgcn_raw_buffer_load_b128(rs, off, 0, 16); }
__device__ __forceinline__ void wait_flag(gu32* p, unsigned tag) {
    for (unsigned spins = 0; spins < SPIN_CAP; ++spins) {
        const unsigned v = __hip_atomic_load(p, __ATOMIC_RELAXED, __HIP_MEMORY_SCOPE_AGENT);
        if (__all(v == tag)) break;
        __builtin_amdgcn_s_sleep(1);
    }
}
__device__ __forceinline__ void set_flag(gu32* p, unsigned tag) { __hip_atomic_store(p, tag, __ATOMIC_RELAXED, __HIP_MEMORY_SCOPE_AGENT); }
__device__ __forceinline__ u32x4 gather8(lptr p, int stride) {
    unsigned w[4];
#pragma unroll
    for (int j = 0; j < 4; ++j) w[j] = (unsigned)*(const LAS u16*)(p + 2 * j * stride) | ((unsigned)*(const LAS u16*)(p + (2 * j + 1) * stride) << 16);
    return u32x4{w[0], w[1], w[2], w[3]};
}
template <int V> struct IC { static constexpr int value = V; };
__device__ __forceinline__ int piece_start(int j) { return (9 * j + 1) >> 1; }

__device__ __forceinline__ unsigned flag_request(gu32* p) { unsigned v; asm volatile("global_load_dword %0, %1, off sc1" : "=v"(v) : "v"(p) : "memory"); return v; }
__device__ __forceinline__ void flag_confirm(unsigned v, gu32* p, unsigned tag) {
    asm volatile("" : "+v"(v));
    if (!__all(v == tag)) wait_flag(p, tag);
}

#define STAMP(i) do {} while (0)
#define WSTAMP(i) do {} while (0)

__device__ __forceinline__ void feature_map(const float (&p)[9], unsigned (&o)[9]) {
    float mx = fmaxf(fmaxf(p[0], p[1]), p[2]), mn = fminf(fminf(p[0], p[1]), p[2]);
#pragma unroll
    for (int i = 3; i < 9; i += 2) { mx = fmaxf(fmaxf(mx, p[i]), p[i + 1]); mn = fminf(fminf(mn, p[i]), p[i + 1]); }
    mx = x16max(mx); mn = -x16max(-mn);
    const float mxl = -mx * LOG2E, mnl = mn * LOG2E;
    float a[9], b[9], sa = 0.f, sb = 0.f;
#pragma unroll
    for (int i = 0; i < 9; ++i) { a[i] = __builtin_amdgcn_exp2f(__builtin_fmaf(p[i], LOG2E, mxl)); b[i] = __builtin_amdgcn_exp2f(__builtin_fmaf(p[i], -LOG2E, mnl)); sa += a[i]; sb += b[i]; }
    sa = x16sum(sa); sb = x16sum(sb);
    const float ra = __builtin_amdgcn_rcpf(sa), rb = __builtin_amdgcn_rcpf(sb);
#pragma unroll
    for (int i = 0; i < 9; ++i) { a[i] *= ra; b[i] *= rb; }
    o[0] = cvtpk(a[0], a[1]); o[1] = cvtpk(a[2], a[3]); o[2] = cvtpk(a[4], a[5]); o[3] = cvtpk(a[6], a[7]); o[4] = cvtpk(a[8], b[0]);
    o[5] = cvtpk(b[1], b[2]); o[6] = cvtpk(b[3], b[4]); o[7] = cvtpk(b[5], b[6]); o[8] = cvtpk(b[7], b[8]);
}

__global__ __launch_bounds__(512, 2) void fused_kernel(const char* __restrict__ x, const char* __restrict__ wA, const char* __restrict__ wB, const char* __restrict__ wphi,
                                                       const char* __restrict__ woA, const char* __restrict__ woB, const char* __restrict__ gw,
                                                       char* __restrict__ ws, u16* __restrict__ out) {
    asm volatile("" :: "s"(x), "s"(wA), "s"(wB), "s"(wphi), "s"(woA), "s"(woB), "s"(gw), "s"(ws), "s"(out));
    extern __shared__ __attribute__((aligned(16))) char lds_raw[];
    const lptr lds = (lptr)lds_raw;
    const unsigned lds0 = (unsigned)(uintptr_t)lds_raw;
    const int tid = threadIdx.x, lane = tid & 63, wid = __builtin_amdgcn_readfirstlane(tid >> 6);
    const int b = blockIdx.x, hp = (b & 3) * 4 + ((b >> 3) & 3), rt = ((b >> 2) & 1) * 8 + (b >> 5);
    const bool loader = wid >= 4;
    const int lw = wid & 3;
    const int h = lw >> 1, tg = loader ? 3 - 2 * (lw & 1) : 2 * (lw & 1);
    const int r16 = lane & 15, g = lane >> 4;
    gu32* flags = (gu32*)(ws + WS_FLAG);
    gu32* F_W = flags;
    gu32* F_WO = flags + 256 * 32;
    gu32* F_KV = flags + 512 * 32;
    gu32* F_P = flags + 768 * 32;
    gu32* F_O = flags + 1024 * 32;
    {
        unsigned long long pc0; unsigned len;
        asm volatile("s_getpc_b64 %0\nfused_code_begin:" : "=s"(pc0));
        asm volatile("s_mov_b32 %0, fused_code_end-fused_code_begin" : "=s"(len));
        if (wid == 3) {
            const unsigned j = blockIdx.x >> 3;
            if ((j + 1) * 1024 <= len) glds16(j * 1024 + lane * 16, (const void*)pc0, lds0 + 160256);
            if ((j + 33) * 1024 <= len) glds16((j + 32) * 1024 + lane * 16, (const void*)pc0, lds0 + 160256);
        }
    }
    STAMP(0);
    unsigned tagv = flag_request((gu32*)(ws + WS_EPOCH));
    const __amdgpu_buffer_rsrc_t wsr = __builtin_amdgcn_make_buffer_rsrc(ws, 0, 0x2000000, 0x00020000);

    const int G0 = piece_start(rt), nG = piece_start(rt + 1) - G0;
    const int j2 = b >> 3, cgp = b & 7, C0 = piece_start(j2), nC = piece_start(j2 + 1) - C0;
#pragma unroll
    for (int i = 0; i < 3; ++i) {
        const int pc = wid + 8 * i;
        if (pc < 23) { int c = pc * 64 + lane; if (c >= nG * 288) c = 0;
            const int row = c / 36, ch = c % 36, w = ch / 3, cc = ch % 3, sg = w >> 1;
            const char* src = (sg < 3 ? wA + sg * 1152 : wB + (sg - 3) * 1152) + (size_t)(8 * G0 + row) * 3456 + (2 * hp + (w & 1)) * 36 + (cc == 2 ? 20 : cc * 16);
            glds16_ptr(src, lds0 + L_WP + pc * 1024); }
        else glds16(G0 * 16 + (lane < nG ? lane : 0) * 16, gw, lds0 + L_GW);
    }
    const int krot = (2 * (hp & 3) + (rt & 7)) % 9;
    auto kact = [&](int kb) { const int k = kb + krot; return k >= 9 ? k - 9 : k; };
    unsigned xo[4];
    auto issue_x = [&](int kb, int slot) {
        const char* src = x + kact(kb) * 128;
#pragma unroll
        for (int i = 0; i < 4; ++i) glds16(xo[i], src, lds0 + L_X + slot * XS + lw * 4096 + i * 1024);
    };
    if (loader) {
#pragma unroll
        for (int i = 0; i < 4; ++i) { const int row = 32 * lw + 8 * i + (lane >> 3); xo[i] = (unsigned)(rt * 128 + row) * 1152u + (unsigned)(((lane & 7) ^ ((row >> 1) & 7)) << 4); }
        issue_x(0, 0);
        WAITV(4);
    } else WAITV(0);
    BAR();
    asm volatile("" : "+v"(tagv));
    const unsigned tag = __builtin_amdgcn_readfirstlane(tagv) + 1u;
    STAMP(1);
    {
        const int q4 = r16 >> 2, p4 = r16 & 3;
        const unsigned wtb = (unsigned)(WS_WT + (size_t)hp * 9 * 28672);
        u32x4 v[3]; unsigned off[3];
#pragma unroll
        for (int i = 0; i < 2; ++i) {
            const int U0 = i * 32 + wid * 4 + g, U = U0 < nG * 12 ? U0 : 0, Gi = (U * 43) >> 9, ht = U - 12 * Gi, hh2 = ht >= 6, t = ht - 6 * hh2;
            const lptr p = lds + L_WP + (8 * Gi + q4) * 576 + (((0x541032 >> (4 * t)) & 7) * 2 + hh2) * 48 + p4 * 8;
            v[i] = __builtin_bit_cast(u32x4, cat8(trrd(p), trrd(p + 4 * 576)));
            const int G = G0 + Gi;
            off[i] = U0 < nG * 12 ? wtb + (G >> 3) * 28672 + ((hh2 * 7 + t) * 2 + ((G >> 2) & 1)) * 1024 + ((G & 3) * 16 + r16) * 16 : 0xfffffff0u;
        }
        {
            const int U0 = wid * 4 + g, U = U0 < nG * 2 ? U0 : 0, Gi = U >> 1, hh2 = U & 1;
            const u32x4 gt = gather8(lds + L_WP + 8 * Gi * 576 + ((r16 < 12 ? r16 >> 1 : 0) * 2 + hh2) * 48 + 44 + 2 * (r16 & 1), 576);
            const u32x4 gv = *(const LAS u32x4*)(lds + L_GW + Gi * 16);
            v[2] = r16 < 12 ? gt : r16 == 12 ? gv : u32x4{0, 0, 0, 0};
            const int G = G0 + Gi;
            off[2] = U0 < nG * 2 ? wtb + (G >> 3) * 28672 + ((hh2 * 7 + 6) * 2 + ((G >> 2) & 1)) * 1024 + ((G & 3) * 16 + r16) * 16 : 0xfffffff0u;
        }
#pragma unroll
        for (int i = 0; i < 3; ++i) st_dev(wsr, off[i], v[i]);
    }
    if (loader) { issue_x(1, 1); issue_x(2, 2); WAITV(8); } else WAITV(0);
    BAR();
    if (tid == 0) { set_flag(F_W + (hp * 16 + rt) * 32, tag); *(LAS u32x2*)(lds + L_CNT) = u32x2{0u, 0u}; }
    STAMP(2);
    const char* wt = ws + WS_WT + (size_t)hp * 9 * 28672;
    auto issue_w = [&](int kb, int slot) {
#pragma unroll
        for (int i = 0; i < 7; ++i) glds16_dev((unsigned)(lw * 1024 + lane * 16), wt + kact(kb) * 28672 + i * 4096, lds0 + L_W + slot * WS1 + i * 4096 + lw * 1024);
    };
    if (loader) {
#pragma unroll
        for (int i = 0; i < 2; ++i) { const int pc = lw + 4 * i;
            if (pc < 6) { int c = pc * 64 + lane; if (c >= nC * 72) c = 0;
                const int row = c / 9, cc = c % 9, C = C0 + (row >> 3), hq = C / 9, p = 8 * (C % 9) + (row & 7);
                int br, head, d;
                if (p < 64) { br = p < 32; head = (p >> 4) & 1; d = p & 15; } else { br = p < 68; head = (p >> 1) & 1; d = 16 + (p & 1); }
                glds16_ptr((br ? woB : woA) + ((2 * hq + head) * 18 + d) * 1152 + cgp * 144 + cc * 16, lds0 + L_WOP + pc * 1024); }
            else { int c = (pc - 6) * 64 + lane; if (c >= 108) c = 0; const int row = c / 3, cc = c % 3;
                glds16((unsigned)(hp * 1296 + row * 36 + (cc == 2 ? 20 : cc * 16)), wphi, lds0 + L_PHI + (pc - 6) * 1024); } }
    } else if (wid == 0) wait_flag(F_W + (hp * 16 + r16) * 32, tag);
    BAR();
    STAMP(3);
    u32x4 pkt[2][2];
    if (!loader) {
        f32x4 acc[7][4];
#pragma unroll
        for (int t = 0; t < 7; ++t)
#pragma unroll
            for (int u = 0; u < 4; ++u) acc[t][u] = f32x4{0, 0, 0, 0};
        const int th = lw & 1;
        const int xr = L_X + (64 * th + r16) * 128 + ((g ^ (r16 >> 1)) << 4), wr = L_W + h * 14336 + lane * 16;
        bf16x8 wa[7], xa[4], wb[7], xb[4];
        BAR();
#pragma unroll
        for (int u = 0; u < 4; ++u) xa[u] = ld128(lds + xr + u * 2048);
#pragma unroll
        for (int t = 0; t < 7; ++t) wa[t] = ld128(lds + wr + t * 2048);
        int slot = 0;
        for (int i = 0; i < 8; ++i) {
            const lptr sx = lds + slot * XS, sw = lds + slot * WS1;
            SB();
#pragma unroll
            for (int u = 0; u < 4; ++u) xb[u] = ld128(sx + (xr ^ 64) + u * 2048);
#pragma unroll
            for (int t = 0; t < 7; ++t) { wb[t] = ld128(sw + wr + t * 2048 + 1024); SB();
#pragma unroll
                for (int u = 0; u < 4; ++u) acc[t][u] = MFMA16(wa[t], xa[u], acc[t][u]);
                SB(); }
            BAR();
            slot = slot == 2 ? 0 : slot + 1;
            const lptr nx = lds + slot * XS, nw = lds + slot * WS1;
            SB();
#pragma unroll
            for (int u = 0; u < 4; ++u) xa[u] = ld128(nx + xr + u * 2048);
#pragma unroll
            for (int t = 0; t < 7; ++t) { const bf16x8 n = ld128(nw + wr + t * 2048); SB();
#pragma unroll
                for (int u = 0; u < 4; ++u) acc[t][u] = MFMA16(wb[t], xb[u], acc[t][u]);
                SB(); wa[t] = n; }
        }
        {
            const lptr sx = lds + 2 * XS, sw = lds + 2 * WS1;
            SB();
#pragma unroll
            for (int u = 0; u < 4; ++u) xb[u] = ld128(sx + (xr ^ 64) + u * 2048);
#pragma unroll
            for (int t = 0; t < 7; ++t) { wb[t] = ld128(sw + wr + t * 2048 + 1024); SB();
#pragma unroll
                for (int u = 0; u < 4; ++u) acc[t][u] = MFMA16(wa[t], xa[u], acc[t][u]);
                SB(); }
            BAR();
#pragma unroll
            for (int t = 0; t < 7; ++t)
#pragma unroll
                for (int u = 0; u < 4; ++u) acc[t][u] = MFMA16(wb[t], xb[u], acc[t][u]);
        }
        STAMP(4);
        const int row0 = (64 * th + r16) * 48;
        const lptr Kb = lds + L_K + h * 36864 + 256 * 48 + row0, Vb = Kb + 18432, Qb = lds + L_Q + h * 6144 + row0, VAb = lds + L_VA + h * 6144 + row0;
#pragma unroll
        for (int u = 0; u < 4; ++u) {
            *(LAS u32x2*)(Kb + u * 768 + 8 * g) = u32x2{cvtpk(acc[4][u][0], acc[4][u][1]), cvtpk(acc[4][u][2], acc[4][u][3])};
            *(LAS u32x2*)(Vb + u * 768 + 8 * g) = u32x2{cvtpk(acc[5][u][0], acc[5][u][1]), cvtpk(acc[5][u][2], acc[5][u][3])};
        }
        if (g == 2) {
#pragma unroll
            for (int u = 0; u < 4; ++u) {
                *(LAS u32x4*)(Kb + u * 768 + 32) = u32x4{cvtpk(acc[6][u][0], acc[6][u][1]), 0u, 0u, 0u};
                *(LAS u32x4*)(Vb + u * 768 + 32) = u32x4{cvtpk(acc[6][u][2], acc[6][u][3]), 0x00003f80u, 0u, 0u};
            }
        }
        unsigned st0[4], st1[4]; float gt[4];
#pragma unroll
        for (int u = 0; u < 4; ++u) {
            const unsigned s0 = cvtpk(acc[6][u][0], acc[6][u][1]), s1 = cvtpk(acc[6][u][2], acc[6][u][3]);
#pragma unroll
            for (int X = 0; X < 2; ++X) {
                const u32x4 tp = {cvtpk(acc[2 + X][u][0], acc[2 + X][u][1]), cvtpk(acc[2 + X][u][2], acc[2 + X][u][3]), s0, s1};
                if (u < 2) pkt[X][u] = tp; else *(LAS u32x4*)(lds + L_PKX + ((lw * 2 + X) * 2 + (u - 2)) * 1024 + lane * 16) = tp;
            }
            *(LAS u32x2*)(VAb + u * 768 + 8 * g) = u32x2{cvtpk(acc[0][u][0], acc[0][u][1]), cvtpk(acc[0][u][2], acc[0][u][3])};
            *(LAS u32x2*)(Qb + u * 768 + 8 * g) = u32x2{cvtpk(acc[1][u][0] * QSCALE, acc[1][u][1] * QSCALE), cvtpk(acc[1][u][2] * QSCALE, acc[1][u][3] * QSCALE)};
            st0[u] = s0; st1[u] = cvtpk(acc[6][u][2] * QSCALE, acc[6][u][3] * QSCALE);
            gt[u] = __builtin_amdgcn_rcpf(1.f + __builtin_amdgcn_exp2f(-acc[6][u][0] * LOG2E));
        }
        if (g == 1) {
#pragma unroll
            for (int u = 0; u < 4; ++u) { *(LAS u32x2*)(VAb + u * 768 + 32) = u32x2{st0[u], 0x00003f80u}; *(LAS unsigned*)(Qb + u * 768 + 32) = st1[u]; }
        } else if (g == 3 && h == 0) {
#pragma unroll
            for (int u = 0; u < 4; ++u) *(LAS float*)(lds + L_GATE + (64 * th + 16 * u + r16) * 4) = gt[u];
        }
    } else {
        issue_w(0, 0); issue_w(1, 1); issue_w(2, 2);
        WAITV(14);
        BAR();
        for (int i = 0; i < 8; ++i) {
            if (i == 0) WAITV(7); else if (i < 7) WAITV(11); else WAITV(1);
            if (i == 1 && lw == 0 && lane < 4) *(LAS u32x4*)(lds + L_ZERO + lane * 16) = u32x4{0, 0, 0, 0};
            BAR();
            if (i < 6) { issue_x(i + 3, i % 3); issue_w(i + 3, i % 3); }
            if (i == 6 || i == 7) {
                const int q4 = r16 >> 2, p4 = r16 & 3;
                const int U0 = (i - 6) * 16 + lw * 4 + g, U = U0 < nC * 5 ? U0 : 0, Ci = (U * 52) >> 8, t = U - 5 * Ci, C = C0 + Ci, kq = (C * 57) >> 11, s = (C - 36 * kq) >> 2, gg = C & 3;
                const lptr p = lds + L_WOP + (8 * Ci + q4) * 144 + (t < 4 ? t * 32 + p4 * 8 : 128 + (p4 & 1) * 8);
                const u32x4 v = __builtin_bit_cast(u32x4, cat8(trrd(p), trrd(p + 4 * 144)));
                const unsigned off = (unsigned)(WS_WOT + (size_t)cgp * 165888) + (s * 4 + kq) * 4608 + (t < 4 ? t * 1024 + (gg * 16 + r16) * 16 : 4096 + (gg * 8 + r16) * 16);
                st_dev(wsr, U0 < nC * 5 && (t < 4 || r16 < 8) ? off : 0xfffffff0u, v);
            }
        }
        BAR();
    }
    BAR();
    STAMP(5);
    const int cg = (b >> 3) & 7, r64 = 4 * (b & 7) + (b >> 6);
    if (loader) {
#pragma unroll
        for (int X = 0; X < 2; ++X)
#pragma unroll
            for (int u = 0; u < 2; ++u) pkt[X][u] = *(const LAS u32x4*)(lds + L_PKX + (((lw ^ 1) * 2 + X) * 2 + u) * 1024 + lane * 16);
    } else {
        u32x4 kvv[6];
        const lptr kr = lds + L_K + h * 36864 + (256 + 64 * (lw & 1)) * 48 + lane * 16;
#pragma unroll
        for (int i = 0; i < 3; ++i) { kvv[i] = *(const LAS u32x4*)(kr + i * 1024); kvv[3 + i] = *(const LAS u32x4*)(kr + 18432 + i * 1024); }
        const unsigned ko = (unsigned)(WS_KV + (size_t)((rt * 16 + hp) * 4 + h * 2) * 6144) + (lw & 1) * 3072 + lane * 16;
#pragma unroll
        for (int i = 0; i < 3; ++i) { st_dev(wsr, ko + i * 1024, kvv[i]); st_dev(wsr, ko + 6144 + i * 1024, kvv[3 + i]); }
    }
    unsigned phq[9];
    float p[2][9];
    {
        unsigned phk[9];
        const int q4 = r16 >> 2, p4 = r16 & 3;
        const unsigned pw0 = lds0 + L_PHI + h * 864, zp = lds0 + L_ZERO;
        s16x4 R1[2][3], R2[2][2][3];
#pragma unroll
        for (int hf = 0; hf < 2; ++hf) {
            const bool ok = (p4 >> 1) == hf;
            const lptr a = (lptr)(uintptr_t)(ok ? pw0 + (4 * g + q4) * 48 + 16 * (p4 & 1) : zp), a2 = (lptr)(uintptr_t)(ok ? pw0 + (4 * g + q4) * 48 + 40 : zp);
            R1[hf][0] = trrd(a); R1[hf][1] = trrd(a + 8); R1[hf][2] = trrd(a2);
#pragma unroll
            for (int X = 0; X < 2; ++X) {
                const bool ok2 = ok && g == 0 && (q4 >> 1) == X;
                const lptr c = (lptr)(uintptr_t)(ok2 ? pw0 + (16 + (q4 & 1)) * 48 + 16 * (p4 & 1) : zp), c2 = (lptr)(uintptr_t)(ok2 ? pw0 + (16 + (q4 & 1)) * 48 + 40 : zp);
                R2[X][hf][0] = trrd(c); R2[X][hf][1] = trrd(c + 8); R2[X][hf][2] = trrd(c2);
            }
        }
        SB();
#pragma unroll
        for (int X = 0; X < 2; ++X) {
            const bf16x8 bA = __builtin_bit_cast(bf16x8, pkt[X][0]), bB = __builtin_bit_cast(bf16x8, pkt[X][1]);
            f32x4 d[3];
#pragma unroll
            for (int i = 0; i < 3; ++i) d[i] = MFMA16(cat8(R1[0][i], R2[X][0][i]), bA, (f32x4{0, 0, 0, 0}));
#pragma unroll
            for (int i = 0; i < 3; ++i) d[i] = MFMA16(cat8(R1[1][i], R2[X][1][i]), bB, d[i]);
#pragma unroll
            for (int j = 0; j < 4; ++j) { p[X][j] = d[0][j]; p[X][4 + j] = d[1][j]; }
            p[X][8] = (g & 1) ? d[2][3] : d[2][2];
        }
        feature_map(p[1], phk);
        const lptr pkp = lds + L_PK + h * 10240 + (32 * tg + 16 * (g >> 1) + r16) * 80 + (g & 1) * 40;
        *(LAS u32x2*)(pkp) = u32x2{phk[0], phk[1]}; *(LAS u32x2*)(pkp + 8) = u32x2{phk[2], phk[3]}; *(LAS u32x2*)(pkp + 16) = u32x2{phk[4], phk[5]};
        *(LAS u32x2*)(pkp + 24) = u32x2{phk[6], phk[7]}; *(LAS u32x2*)(pkp + 32) = u32x2{phk[8], 0u};
        WAITV(0);
        if (!loader && lane == 0 && __hip_atomic_fetch_add((LAS unsigned*)(lds + L_CNT), 1u, __ATOMIC_RELAXED, __HIP_MEMORY_SCOPE_WORKGROUP) == 3u) set_flag(F_KV + (hp * 16 + rt) * 32, tag);
    }
    BAR();
    if (tid == 0) set_flag(F_WO + b * 32, tag);
    STAMP(6);
    const bool fetcher = !loader && (lw & 1) == 0 && rt > 0;
    gu32* fkp = F_KV + (hp * 16 + rt - 1 - (rt > 1 ? (lane & 1) : 0)) * 32;
    unsigned fk = 0;
    if (fetcher) fk = flag_request(fkp);
    const bool pkw = wid != 4 && wid != 6;
    if (pkw) {
        const int w6 = wid < 4 ? wid : wid == 5 ? 4 : 5, hh = w6 / 3, mt = w6 % 3, q4 = r16 >> 2, p4 = r16 & 3;
        int u = 4 * mt + p4; if (u > 9) u = 9;
        const lptr pa = lds + L_PK + hh * 10240 + (8 * g + q4) * 80 + u * 8;
        const lptr pb0 = lds + L_VA + hh * 6144 + (8 * g + q4) * 48 + p4 * 8;
        const lptr pb1 = lds + L_VA + hh * 6144 + (8 * g + q4) * 48 + 32;
        f32x4 d0 = {0, 0, 0, 0}, d1 = {0, 0, 0, 0};
        bf16x8 a[4], b0[4], b1[4];
#pragma unroll
        for (int ks = 0; ks < 4; ++ks) {
            a[ks] = cat8(trrd(pa + ks * 32 * 80), trrd(pa + ks * 32 * 80 + 4 * 80));
            b0[ks] = cat8(trrd(pb0 + ks * 32 * 48), trrd(pb0 + ks * 32 * 48 + 4 * 48));
            b1[ks] = cat8(trrd(pb1 + ks * 32 * 48), trrd(pb1 + ks * 32 * 48 + 4 * 48));
        }
        SB();
#pragma unroll
        for (int ks = 0; ks < 4; ++ks) { d0 = MFMA16(a[ks], b0[ks], d0); d1 = MFMA16(a[ks], b1[ks], d1); }
        const int uu = 4 * mt + g;
        const unsigned po = (unsigned)(WS_PART + (size_t)(hp * 16 + rt) * 3072 + hh * 1520);
        if (uu < 10) {
            __builtin_amdgcn_raw_buffer_store_b64(u32x2{cvtpk(d0[0], d0[1]), cvtpk(d0[2], d0[3])}, wsr, po + (r16 * 10 + uu) * 8, 0, 16);
            if (r16 < 3) __builtin_amdgcn_raw_buffer_store_b64(u32x2{cvtpk(d1[0], d1[1]), cvtpk(d1[2], d1[3])}, wsr, po + ((16 + r16) * 10 + uu) * 8, 0, 16);
        }
    }
    const int q32 = lane & 31, hh = lane >> 5;
    const char* wot = ws + WS_WOT + (size_t)cg * 165888;
    auto g3_step = [&](int p) { const int s = p + cg; return s >= 9 ? s - 9 : s; };
    f32x16 oacc;
    u32x4 pv[3][4];
    auto pv_load = [&]() {
#pragma unroll
        for (int n = 0; n < 3; ++n) { const int gr = (3 * lw + n) * 16 + r16;
#pragma unroll
            for (int m = 0; m < 4; ++m) pv[n][m] = ld_dev(wsr, (unsigned)(WS_PART + (size_t)(hp * 16 + 4 * m + g) * 3072) + (gr < 190 ? gr : 0) * 16); }
    };
    {
        const lptr Kb = lds + L_K + h * 36864, Vb = Kb + 18432, Qb = lds + L_Q + h * 6144;
        const bf16x8 qf0 = ld128(Qb + (32 * tg + q32) * 48 + 16 * hh);
        const unsigned q16 = *(const LAS unsigned*)(Qb + (32 * tg + q32) * 48 + 32);
        const bf16x8 qf1 = mk8(hh ? 0u : q16, 0u, 0u, 0u);
        const lptr kp = Kb + (32 * tg + q32) * 48;
        const int i16 = lane & 15, dhalf = (lane >> 4) & 1;
        const lptr vp = Vb + (32 * tg + 4 * hh + (i16 >> 2)) * 48 + (dhalf ? 32 + ((i16 & 3) ? 8 : 0) : (i16 & 3) * 8);
        f32x16 negm;
#pragma unroll
        for (int r = 0; r < 16; ++r) { oacc[r] = 0.f; negm[r] = 0.f; }
        bf16x8 k0, k1, k2, k3;
        auto kfetch = [&](int KN) { const int KM = KN > 0 ? KN - 1 : 0;
            k0 = ld128(kp + KN * 32 * 48 + 16 * hh); k1 = ld128(kp + KN * 32 * 48 + 32); k2 = ld128(kp + KM * 32 * 48 + 16 * hh); k3 = ld128(kp + KM * 32 * 48 + 32); };
        auto vfrag = [&](int KT, int hf) { return cat8(trrd(vp + (KT * 32 + 16 * hf) * 48), trrd(vp + (KT * 32 + 16 * hf + 8) * 48)); };
        auto rowmax = [&](const f32x16& s) { float m = fmaxf(fmaxf(s[0], s[1]), s[2]);
#pragma unroll
            for (int r = 3; r < 15; r += 2) m = fmaxf(fmaxf(m, s[r]), s[r + 1]);
            return fmaxf(m, s[15]); };
        auto pack = [&](const f32x16& s, int hf) { return mk8(cvtpk(__builtin_amdgcn_exp2f(s[8 * hf]), __builtin_amdgcn_exp2f(s[8 * hf + 1])), cvtpk(__builtin_amdgcn_exp2f(s[8 * hf + 2]), __builtin_amdgcn_exp2f(s[8 * hf + 3])),
                                                             cvtpk(__builtin_amdgcn_exp2f(s[8 * hf + 4]), __builtin_amdgcn_exp2f(s[8 * hf + 5])), cvtpk(__builtin_amdgcn_exp2f(s[8 * hf + 6]), __builtin_amdgcn_exp2f(s[8 * hf + 7]))); };
        auto tile = [&](int KT, int KN, bool first, int mask) {
            SB();
            f32x16 s = MFMA32(k0, qf0, negm); s = MFMA32(k1, qf1, s);
            SB();
            const bf16x8 va = vfrag(KT, 0), vb = vfrag(KT, 1);
            kfetch(KN);
            SB();
            if (mask) {
#pragma unroll
                for (int r = 0; r < 16; ++r) { const int jj = (r & 3) + 8 * (r >> 2) + 4 * hh; const bool ok = mask == 1 ? jj <= q32 : jj > q32; s[r] = ok ? s[r] : NEGBIG; }
            }
            const float m = x32max(rowmax(s));
            if (first || __any(m > THR)) {
                const float dl = first ? m : fmaxf(m, 0.f);
                const float f = first ? 1.f : __builtin_amdgcn_exp2f(-dl);
#pragma unroll
                for (int r = 0; r < 16; ++r) { s[r] -= dl; negm[r] -= dl; if (!first) oacc[r] *= f; }
            }
            oacc = MFMA32(va, pack(s, 0), oacc);
            oacc = MFMA32(vb, pack(s, 1), oacc);
        };
        auto tile2 = [&](int KT) {
            SB();
            f32x16 s = MFMA32(k0, qf0, negm), t = MFMA32(k2, qf0, negm); s = MFMA32(k1, qf1, s); t = MFMA32(k3, qf1, t);
            SB();
            const bf16x8 va = vfrag(KT, 0), vb = vfrag(KT, 1), vc = vfrag(KT - 1, 0), vd = vfrag(KT - 1, 1);
            kfetch(KT >= 2 ? KT - 2 : 0);
            SB();
            const float m = x32max(fmaxf(rowmax(s), rowmax(t)));
            if (__any(m > THR)) {
                const float dl = fmaxf(m, 0.f), f = __builtin_amdgcn_exp2f(-dl);
#pragma unroll
                for (int r = 0; r < 16; ++r) { s[r] -= dl; t[r] -= dl; negm[r] -= dl; oacc[r] *= f; }
            }
            oacc = MFMA32(va, pack(s, 0), oacc);
            oacc = MFMA32(vb, pack(s, 1), oacc);
            oacc = MFMA32(vc, pack(t, 0), oacc);
            oacc = MFMA32(vd, pack(t, 1), oacc);
        };
        auto wo_piece = [&](int n) { const int pc = n * 4 + lw; if (pc < 54) { const int p = pc / 18, o = pc % 18;
            glds16_dev((unsigned)(o * 1024 + lane * 16), wot + g3_step(p) * G3_SLOT, lds0 + g3_slot(0) + p * G3_SLOT + o * 1024); } };
        auto wo_pieces = [&](int n0, int n1) { if (!loader) for (int n = n0; n < n1; ++n) wo_piece(n); };
        const int lo = rt >= 2 ? 0 : (rt == 1 ? 4 - tg : 8 - tg);
        const int last = lo > 1 ? lo : 1;
        kfetch(8);
        tile(8, 7, true, 1);
        u32x4 hv[24];
        auto part_done = [&]() {
            if (lane == 0 && __hip_atomic_fetch_add((LAS unsigned*)(lds + L_CNT + 4), 1u, __ATOMIC_RELAXED, __HIP_MEMORY_SCOPE_WORKGROUP) == 5u) set_flag(F_P + (hp * 16 + rt) * 32, tag); };
        if (fetcher) {
            WAITV(0);
            part_done();
            flag_confirm(fk, fkp, tag);
#pragma unroll
            for (int n = 0; n < 24; ++n) { const int pc = 2 * n + (lw >> 1), tl = n >= 12, ar = (pc - 24 * tl) / 6, o = pc - 24 * tl - 6 * ar;
                hv[n] = ld_dev(wsr, (unsigned)(WS_KV + (size_t)(((rt - 2 + tl >= 0 ? rt - 2 + tl : 0) * 16 + hp) * 4 + ar) * 6144) + o * 1024 + lane * 16); }
        }
        if (tg >= 2) tile2(7);
        if (tg & 1) tile(tg == 3 ? 5 : 7, 0, false, 0);
        if (pkw && !fetcher) { WAITV(0); part_done(); }
        feature_map(p[0], phq);
        if (fetcher) {
#pragma unroll
            for (int n = 0; n < 24; ++n) { const int pc = 2 * n + (lw >> 1), tl = n >= 12, ar = (pc - 24 * tl) / 6, o = pc - 24 * tl - 6 * ar;
                *(LAS u32x4*)(lds + L_K + ar * 18432 + tl * 6144 + o * 1024 + lane * 16) = hv[n]; }
        }
        BAR();
        STAMP(7);
        gu32* fpp = loader ? F_P + (hp * 16 + r16) * 32 : F_WO + (cg + 8 * (lane & 31)) * 32;
        kfetch(7 - tg);
        if (rt >= 2) {
            if ((tg & 1) == 0) tile(7 - tg, 6 - tg, false, 0);
            if (tg < 2) tile2(6);
            const unsigned fp = flag_request(fpp);
            tile2(4);
            WAITV(0);
            flag_confirm(fp, fpp, tag);
            if (loader) pv_load(); else wo_pieces(0, 7);
            tile2(2); wo_pieces(7, 14);
            tile(0, 0, false, 2);
        } else {
            if (rt == 1) for (int KT = 7 - tg; KT >= last; --KT) tile(KT, KT - 1, false, 0);
            wait_flag(fpp, tag);
            if (loader) pv_load(); else wo_pieces(0, 14);
        }
    }
    STAMP(8);
    const unsigned obase = (unsigned)(WS_O + ((size_t)(rt * 8 + 2 * tg) * 144 + 9 * hp) * 256);
    const lptr stg = lds + L_OST;
    {
        const int tokb = 32 * tg + q32;
        const float gb = *(const LAS float*)(lds + L_GATE + tokb * 4);
        if (loader) {
            const unsigned one = (r16 < 8 && (r16 & 1) == 0) ? 0x00003f80u : (r16 < 8 ? 0x3f800000u : 0u);
            const bf16x8 idn = mk8((r16 >> 1) == 0 ? one : 0u, (r16 >> 1) == 1 ? one : 0u, (r16 >> 1) == 2 ? one : 0u, (r16 >> 1) == 3 ? one : 0u);
#pragma unroll
            for (int n = 0; n < 3; ++n) {
                f32x4 sm = {0, 0, 0, 0};
#pragma unroll
                for (int m = 0; m < 4; ++m) sm = MFMA16(idn, __builtin_bit_cast(bf16x8, pv[n][m]), sm);
                const int gr = (3 * lw + n) * 16 + r16, ph = gr >= 95, e0 = gr - 95 * ph, d = (e0 * 205) >> 10, u = 2 * (e0 - 5 * d) + g;
                if (gr < 190 && g < 2) *(LAS u32x2*)(lds + L_KVT + ph * 2016 + d * 96 + (u >= 5 ? 48 + (u - 5) * 8 : u * 8)) = u32x2{cvtpk(sm[0], sm[1]), cvtpk(sm[2], sm[3])};
            }
        } else {
            const int e0 = lw * 64 + lane;
            if (e0 < 76) *(LAS u32x2*)(lds + L_KVT + (e0 / 38) * 2016 + ((e0 % 38) >> 1) * 96 + (e0 & 1) * 48 + 40) = u32x2{0u, 0u};
            else if (e0 < 88) *(LAS u32x4*)(lds + L_KVT + ((e0 - 76) / 6) * 2016 + 20 * 96 + ((e0 - 76) % 6) * 16) = u32x4{0, 0, 0, 0};
        }
        auto sw = __builtin_amdgcn_permlane32_swap(__float_as_uint(oacc[10]), __float_as_uint(oacc[10]), false, false);
        const float sb = (1.f - gb) * __builtin_amdgcn_rcpf(__uint_as_float(sw[0]));
        const auto s0 = __builtin_amdgcn_permlane32_swap(cvtpk(oacc[0] * sb, oacc[1] * sb), cvtpk(oacc[4] * sb, oacc[5] * sb), false, false);
        const auto s1 = __builtin_amdgcn_permlane32_swap(cvtpk(oacc[2] * sb, oacc[3] * sb), cvtpk(oacc[6] * sb, oacc[7] * sb), false, false);
        st_dev(wsr, obase + (q32 >> 4) * 36864 + (2 * h + hh) * 256 + (q32 & 15) * 16, u32x4{s0[0], s1[0], s0[1], s1[1]});
        if (hh == 0) *(LAS unsigned*)(stg + tokb * 16 + h * 4) = cvtpk(oacc[8] * sb, oacc[9] * sb);
    }
    BAR();
    STAMP(9);
    {
        f32x4 dd[3];
        const int eo = (r16 >> 2) & 1;
        const bool mine = (r16 >> 3) == (g >> 1);
        const bf16x8 b0 = mk8(phq[0], phq[1], phq[2], phq[3]), b1 = mk8(phq[4], phq[5], phq[6], phq[7]), b2 = mk8(phq[8], 0u, 0u, 0u);
        bf16x8 af[3][3];
#pragma unroll
        for (int i = 0; i < 3; ++i) {
            const int sg = 4 * i + (r16 & 3);
            const int d = !mine ? 20 : i < 2 ? 8 * eo + sg : sg == 8 ? 16 + eo : sg == 10 ? 18 : 20;
            const lptr ap = lds + L_KVT + h * 2016 + d * 96 + (g & 1) * 48;
            af[i][0] = ld128(ap); af[i][1] = ld128(ap + 16); af[i][2] = ld128(ap + 32);
        }
        const int tok = 32 * tg + 16 * (g >> 1) + r16;
        const float gate = *(const LAS float*)(lds + L_GATE + tok * 4);
        SB();
#pragma unroll
        for (int i = 0; i < 3; ++i) { f32x4 a = MFMA16(af[i][0], b0, (f32x4{0, 0, 0, 0})); a = MFMA16(af[i][1], b1, a); dd[i] = MFMA16(af[i][2], b2, a); }
        const float sc = gate * __builtin_amdgcn_rcpf(dd[2][2] + 1e-6f);
        st_dev(wsr, obase + (g >> 1) * 36864 + (4 + 2 * h + (g & 1)) * 256 + r16 * 16,
               u32x4{cvtpk(dd[0][0] * sc, dd[0][1] * sc), cvtpk(dd[0][2] * sc, dd[0][3] * sc), cvtpk(dd[1][0] * sc, dd[1][1] * sc), cvtpk(dd[1][2] * sc, dd[1][3] * sc)});
        *(LAS u16*)(stg + tok * 16 + 8 + h * 4 + (g & 1) * 2) = (u16)cvtpk(dd[2][0] * sc, 0.f);
    }
    BAR();
    if (tid < 128) st_dev(wsr, (unsigned)(WS_O + ((size_t)(rt * 8 + (tid >> 4)) * 144 + 9 * hp + 8) * 256) + (tid & 15) * 16, *(const LAS u32x4*)(stg + tid * 16));
    STAMP(10);
    {
        const int rt3 = r64 >> 1, rh = r64 & 1, kq = wid >> 1, rp = wid & 1;
        auto issue3 = [&](int p) {
            if (wid > 0) {
#pragma unroll
                for (int i = 0; i < 3; ++i) { const int pc = (wid - 1) + 7 * i;
                    if (pc < 18) glds16_dev((unsigned)(pc * 1024 + lane * 16), wot + g3_step(p) * G3_SLOT, lds0 + g3_slot(p) + pc * 1024); }
            }
        };
        issue3(3); issue3(4);
        if (wid == 0) WAITV(0); else if (wid < 5) WAITV(6); else WAITV(4);
        BAR();
        if (tid == 0) set_flag(F_O + (rt * 16 + hp) * 32, tag);
        issue3(5); issue3(6); issue3(7);
        STAMP(11);
        if (wid == 0) wait_flag(F_O + (rt3 * 16 + r16) * 32, tag);
        DRAIN_BAR();
        STAMP(12);
        u32x4 of[9][2];
        const unsigned ob = (unsigned)(WS_O + ((size_t)(rt3 * 8 + 4 * rh + 2 * rp) * 144 + 36 * kq) * 256 + lane * 16);
        auto o_load = [&](int p) { const unsigned o = ob + g3_step(p) * 1024; of[p][0] = ld_dev(wsr, o); of[p][1] = ld_dev(wsr, o + 36864); };
        o_load(0); o_load(1); o_load(2); o_load(3); o_load(4);
        f32x4 a3[5][2];
#pragma unroll
        for (int t = 0; t < 5; ++t) { a3[t][0] = f32x4{0, 0, 0, 0}; a3[t][1] = f32x4{0, 0, 0, 0}; }
        const int wro = kq * 4608 + lane * 16, wr4 = kq * 4608 + 4096 + g * 128 + (r16 & 7) * 16;
        bf16x8 wf[5];
#pragma unroll
        for (int t = 0; t < 4; ++t) wf[t] = ld128(lds + g3_slot(0) + wro + t * 1024);
        wf[4] = ld128(lds + g3_slot(0) + wr4);
#pragma unroll
        for (int p = 0; p < 9; ++p) {
            if (p == 1) { BAR(); issue3(8); }
            if (p == 7) { WAITV(2); BAR(); }
            if (p + 5 < 9) o_load(p + 5);
            const lptr sp = lds + g3_slot(p + 1 < 9 ? p + 1 : 8);
            const bf16x8 bo0 = __builtin_bit_cast(bf16x8, of[p][0]), bo1 = __builtin_bit_cast(bf16x8, of[p][1]);
            SB();
#pragma unroll
            for (int t = 0; t < 5; ++t) {
                bf16x8 nw = wf[t];
                if (p + 1 < 9) nw = ld128(sp + (t < 4 ? wro + t * 1024 : wr4));
                SB();
                a3[t][0] = MFMA16(wf[t], bo0, a3[t][0]); a3[t][1] = MFMA16(wf[t], bo1, a3[t][1]);
                SB();
                wf[t] = nw;
            }
        }
        BAR();
        STAMP(13);
        const lptr rp0 = lds + rp * 40960 + lane * 16;
        char* orow = (char*)out + (size_t)(rt3 * 128 + 64 * rh + 32 * rp + r16) * 1152 + cg * 144 + g * 8;
        auto finish = [&](auto KQ) {
            constexpr int q = decltype(KQ)::value;
#pragma unroll
            for (int n = 0; n < 10; ++n) if ((n & 3) != q) *(LAS f32x4*)(rp0 + (n * 4 + q) * 1024) = a3[n % 5][n / 5];
            BAR();
            f32x4 pr[3][3];
#pragma unroll
            for (int j = 0; j < 3; ++j) if (q + 4 * j < 10) {
#pragma unroll
                for (int o = 0; o < 3; ++o) pr[j][o] = *(const LAS f32x4*)(rp0 + ((q + 4 * j) * 4 + ((q + 1 + o) & 3)) * 1024); }
#pragma unroll
            for (int j = 0; j < 3; ++j) if (q + 4 * j < 10) { const int n = q + 4 * j, t = n % 5, tt = n / 5;
                const f32x4 u = (a3[t][tt] + pr[j][0]) + (pr[j][1] + pr[j][2]);
                if (t < 4 || g < 2) *(u32x2*)(orow + tt * 16 * 1152 + t * 32) = u32x2{cvtpk(u[0], u[1]), cvtpk(u[2], u[3])};
            }
        };
        if (kq == 0) finish(IC<0>{}); else if (kq == 1) finish(IC<1>{}); else if (kq == 2) finish(IC<2>{}); else finish(IC<3>{});
        STAMP(14);
        asm volatile("fused_code_end:");
        if (b == 0 && tid == 0) set_flag((gu32*)(ws + WS_EPOCH), tag);
    }
}

extern "C" void kernel_launch(void* const* d_in, const int* in_sizes, int n_in, void* d_out, int out_size, void* d_ws, size_t ws_size, hipStream_t stream) {
    (void)hipFuncSetAttribute((const void*)fused_kernel, hipFuncAttributeMaxDynamicSharedMemorySize, LDS_BYTES);
    fused_kernel<<<256, 512, LDS_BYTES, stream>>>((const char*)d_in[0], (const char*)d_in[1], (const char*)d_in[5], (const char*)d_in[2], (const char*)d_in[3],
                                                  (const char*)d_in[6], (const char*)d_in[7], (char*)d_ws, (u16*)d_out);
}
```

```cpp
#include <hip/hip_runtime.h>
#include <hip/hip_bf16.h>
#include <stdint.h>

typedef unsigned short u16;
typedef short bf16x8 __attribute__((ext_vector_type(8)));
typedef short s16x4 __attribute__((ext_vector_type(4)));
typedef float f32x4 __attribute__((ext_vector_type(4)));
typedef float f32x16 __attribute__((ext_vector_type(16)));
typedef unsigned u32x4 __attribute__((ext_vector_type(4)));
typedef unsigned u32x2 __attribute__((ext_vector_type(2)));
typedef __bf16 bf16x2_t __attribute__((ext_vector_type(2)));
typedef float f32x2 __attribute__((ext_vector_type(2)));
#define LAS __attribute__((address_space(3)))
#define GAS __attribute__((address_space(1)))
typedef LAS char* lptr;
typedef GAS unsigned gu32;

constexpr size_t WS_EPOCH = 0;
constexpr size_t WS_FLAG = 4096;
constexpr size_t WS_WT   = 4096 + 5 * 256 * 128;
constexpr size_t WS_WOT  = WS_WT + 16 * 9 * 28672;
constexpr size_t WS_KV   = WS_WOT + 8 * 165888;
constexpr size_t WS_PART = WS_KV + 256 * 24576;
constexpr size_t WS_O    = WS_PART + 256 * 6144;
constexpr size_t WS_DBG  = WS_O + 16 * 8 * 36864;

constexpr int XS = 16384, WS1 = 28672;
constexpr int L_X = 0, L_W = 3 * XS;
constexpr int L_WP = L_W + 2 * WS1;
constexpr int L_GW = L_WP + 23 * 1024;
constexpr int L_PHI = L_W + 3 * WS1;
constexpr int L_ZERO = L_PHI + 1792;
constexpr int L_WOP = L_PHI + 2048;
constexpr int L_PKX = 118784;
constexpr int L_CNT = 152064;
constexpr int L_K = 0;
constexpr int L_Q = 73728;
constexpr int L_VA = 86016;
constexpr int L_PK = 98304;
constexpr int L_GATE = 147456;
constexpr int L_KVT = 147968;
constexpr int L_OST = 73728;
constexpr int LDS_BYTES = 160 * 1024;
#define ODEPTH 5
constexpr int G3_SLOT = 18432;
__device__ __forceinline__ constexpr int g3_slot(int p) { return (p < 3 ? 5 + p : p - 3) * G3_SLOT; }
constexpr unsigned SPIN_CAP = 1u << 18;
constexpr float LOG2E = 1.4426950408889634f;
constexpr float QSCALE = 0.23570226039551584f * LOG2E;
constexpr float THR = 8.f;
constexpr float NEGBIG = -1e30f;

__device__ __forceinline__ unsigned cvtpk(float lo, float hi) { const f32x2 v = {lo, hi}; return __builtin_bit_cast(unsigned, __builtin_convertvector(v, bf16x2_t)); }
__device__ __forceinline__ void glds16(unsigned voff, const void* sbase, unsigned lds_addr) {
    unsigned sv;
    asm volatile("s_mov_b32 %0, m0\n\ts_mov_b32 m0, %3\n\ts_nop 0\n\tglobal_load_lds_dwordx4 %1, %2\n\ts_mov_b32 m0, %0" : "=&s"(sv) : "v"(voff), "s"(sbase), "s"(lds_addr) : "memory");
}
__device__ __forceinline__ void glds16_dev(unsigned voff, const void* sbase, unsigned lds_addr) {
    unsigned sv;
    asm volatile("s_mov_b32 %0, m0\n\ts_mov_b32 m0, %3\n\ts_nop 0\n\tglobal_load_lds_dwordx4 %1, %2 sc1\n\ts_mov_b32 m0, %0" : "=&s"(sv) : "v"(voff), "s"(sbase), "s"(lds_addr) : "memory");
}
__device__ __forceinline__ void glds16_ptr(const void* g, unsigned lds_addr) {
    unsigned sv;
    asm volatile("s_mov_b32 %0, m0\n\ts_mov_b32 m0, %2\n\ts_nop 0\n\tglobal_load_lds_dwordx4 %1, off\n\ts_mov_b32 m0, %0" : "=&s"(sv) : "v"(g), "s"(lds_addr) : "memory");
}
#define WAITV(n) asm volatile("s_waitcnt vmcnt(" #n ")" ::: "memory")
#define WAITL() asm volatile("s_waitcnt lgkmcnt(0)" ::: "memory")
#define LGKM0() do { __builtin_amdgcn_s_waitcnt(0xC07F); asm volatile("" ::: "memory"); } while (0)
#define BAR() do { asm volatile("" ::: "memory"); __builtin_amdgcn_s_waitcnt(0xC07F); __builtin_amdgcn_s_barrier(); asm volatile("" ::: "memory"); } while (0)
#define DRAIN_BAR() do { asm volatile("s_waitcnt vmcnt(0)" ::: "memory"); BAR(); } while (0)
#define SB() __builtin_amdgcn_sched_barrier(0)
#define MFMA16(a, b, c) __builtin_amdgcn_mfma_f32_16x16x32_bf16(a, b, c, 0, 0, 0)
#define MFMA32(a, b, c) __builtin_amdgcn_mfma_f32_32x32x16_bf16(a, b, c, 0, 0, 0)
__device__ __forceinline__ bf16x8 ld128(lptr p) { return *(const LAS bf16x8*)p; }
__device__ __forceinline__ s16x4 trrd(lptr p) { typedef short v4i16_t __attribute__((ext_vector_type(4))); return __builtin_bit_cast(s16x4, __builtin_amdgcn_ds_read_tr16_b64_v4i16((LAS v4i16_t*)p)); }
__device__ __forceinline__ bf16x8 cat8(s16x4 a, s16x4 b) { return __builtin_shufflevector(a, b, 0, 1, 2, 3, 4, 5, 6, 7); }
__device__ __forceinline__ bf16x8 mk8(unsigned a, unsigned b, unsigned c, unsigned d) { u32x4 w = {a, b, c, d}; return __builtin_bit_cast(bf16x8, w); }
__device__ __forceinline__ float vmax2(unsigned a, unsigned b) { float r; asm("v_max_f32 %0, %1, %2" : "=v"(r) : "v"(a), "v"(b)); return r; }
__device__ __forceinline__ float x16max(float x) { auto s = __builtin_amdgcn_permlane16_swap(__float_as_uint(x), __float_as_uint(x), false, false); return vmax2(s[0], s[1]); }
__device__ __forceinline__ float x16sum(float x) { auto s = __builtin_amdgcn_permlane16_swap(__float_as_uint(x), __float_as_uint(x), false, false); return __uint_as_float(s[0]) + __uint_as_float(s[1]); }
__device__ __forceinline__ float x32max(float x) { auto s = __builtin_amdgcn_permlane32_swap(__float_as_uint(x), __float_as_uint(x), false, false); return vmax2(s[0], s[1]); }
__device__ __forceinline__ void st_dev(const __amdgpu_buffer_rsrc_t rs, unsigned off, u32x4 v) { __builtin_amdgcn_raw_buffer_store_b128(v, rs, off, 0, 16); }
__device__ __forceinline__ u32x4 ld_dev(const __amdgpu_buffer_rsrc_t rs, unsigned off) { return __builtin_amdgcn_raw_buffer_load_b128(rs, off, 0, 16); }
__device__ __forceinline__ void wait_flag(gu32* p, unsigned tag) {
    for (unsigned spins = 0; spins < SPIN_CAP; ++spins) {
        const unsigned v = __hip_atomic_load(p, __ATOMIC_RELAXED, __HIP_MEMORY_SCOPE_AGENT);
        if (__all(v == tag)) break;
        __builtin_amdgcn_s_sleep(1);
    }
}
__device__ __forceinline__ void set_flag(gu32* p, unsigned tag) { __hip_atomic_store(p, tag, __ATOMIC_RELAXED, __HIP_MEMORY_SCOPE_AGENT); }
__device__ __forceinline__ u32x4 gather8(lptr p, int stride) {
    unsigned w[4];
#pragma unroll
    for (int j = 0; j < 4; ++j) w[j] = (unsigned)*(const LAS u16*)(p + 2 * j * stride) | ((unsigned)*(const LAS u16*)(p + (2 * j + 1) * stride) << 16);
    return u32x4{w[0], w[1], w[2], w[3]};
}
template <int V> struct IC { static constexpr int value = V; };
__device__ __forceinline__ int piece_start(int j) { return (9 * j + 1) >> 1; }

__device__ __forceinline__ unsigned flag_request(gu32* p) { unsigned v; asm volatile("global_load_dword %0, %1, off sc1" : "=v"(v) : "v"(p) : "memory"); return v; }
__device__ __forceinline__ void flag_confirm(unsigned v, gu32* p, unsigned tag) {
    asm volatile("" : "+v"(v));
    if (!__all(v == tag)) wait_flag(p, tag);
}

#define STAMP(i) do {} while (0)
#define WSTAMP(i) do {} while (0)

__device__ __forceinline__ void feature_map(const float (&p)[9], unsigned (&o)[9]) {
    float mx = fmaxf(fmaxf(p[0], p[1]), p[2]), mn = fminf(fminf(p[0], p[1]), p[2]);
#pragma unroll
    for (int i = 3; i < 9; i += 2) { mx = fmaxf(fmaxf(mx, p[i]), p[i + 1]); mn = fminf(fminf(mn, p[i]), p[i + 1]); }
    mx = x16max(mx); mn = -x16max(-mn);
    const float mxl = -mx * LOG2E, mnl = mn * LOG2E;
    float a[9], b[9], sa = 0.f, sb = 0.f;
#pragma unroll
    for (int i = 0; i < 9; ++i) { a[i] = __builtin_amdgcn_exp2f(__builtin_fmaf(p[i], LOG2E, mxl)); b[i] = __builtin_amdgcn_exp2f(__builtin_fmaf(p[i], -LOG2E, mnl)); sa += a[i]; sb += b[i]; }
    sa = x16sum(sa); sb = x16sum(sb);
    const float ra = __builtin_amdgcn_rcpf(sa), rb = __builtin_amdgcn_rcpf(sb);
#pragma unroll
    for (int i = 0; i < 9; ++i) { a[i] *= ra; b[i] *= rb; }
    o[0] = cvtpk(a[0], a[1]); o[1] = cvtpk(a[2], a[3]); o[2] = cvtpk(a[4], a[5]); o[3] = cvtpk(a[6], a[7]); o[4] = cvtpk(a[8], b[0]);
    o[5] = cvtpk(b[1], b[2]); o[6] = cvtpk(b[3], b[4]); o[7] = cvtpk(b[5], b[6]); o[8] = cvtpk(b[7], b[8]);
}

__global__ __launch_bounds__(512, 2) void fused_kernel(const char* __restrict__ x, const char* __restrict__ wA, const char* __restrict__ wB, const char* __restrict__ wphi,
                                                       const char* __restrict__ woA, const char* __restrict__ woB, const char* __restrict__ gw,
                                                       char* __restrict__ ws, u16* __restrict__ out) {
    asm volatile("" :: "s"(x), "s"(wA), "s"(wB), "s"(wphi), "s"(woA), "s"(woB), "s"(gw), "s"(ws), "s"(out));
    extern __shared__ __attribute__((aligned(16))) char lds_raw[];
    const lptr lds = (lptr)lds_raw;
    const unsigned lds0 = (unsigned)(uintptr_t)lds_raw;
    const int tid = threadIdx.x, lane = tid & 63, wid = __builtin_amdgcn_readfirstlane(tid >> 6);
    const int b = blockIdx.x, hp = (b & 3) * 4 + ((b >> 3) & 3), rt = ((b >> 2) & 1) * 8 + (b >> 5);
    const bool loader = wid >= 4;
    const int lw = wid & 3;
    const int h = lw >> 1, tg = loader ? 3 - 2 * (lw & 1) : 2 * (lw & 1);
    const int r16 = lane & 15, g = lane >> 4;
    gu32* flags = (gu32*)(ws + WS_FLAG);
    gu32* F_W = flags;
    gu32* F_WO = flags + 256 * 32;
    gu32* F_KV = flags + 512 * 32;
    gu32* F_P = flags + 768 * 32;
    gu32* F_O = flags + 1024 * 32;
    {
        unsigned long long pc0; unsigned len;
        asm volatile("s_getpc_b64 %0\nfused_code_begin:" : "=s"(pc0));
        asm volatile("s_mov_b32 %0, fused_code_end-fused_code_begin" : "=s"(len));
        if (wid == 3) {
            const unsigned j = blockIdx.x >> 3;
            if ((j + 1) * 1024 <= len) glds16(j * 1024 + lane * 16, (const void*)pc0, lds0 + 160256);
            if ((j + 33) * 1024 <= len) glds16((j + 32) * 1024 + lane * 16, (const void*)pc0, lds0 + 160256);
        }
    }
    STAMP(0);
    unsigned tagv = flag_request((gu32*)(ws + WS_EPOCH));
    const __amdgpu_buffer_rsrc_t wsr = __builtin_amdgcn_make_buffer_rsrc(ws, 0, 0x2000000, 0x00020000);

    const int G0 = piece_start(rt), nG = piece_start(rt + 1) - G0;
    const int j2 = b >> 3, cgp = b & 7, C0 = piece_start(j2), nC = piece_start(j2 + 1) - C0;
#pragma unroll
    for (int i = 0; i < 3; ++i) {
        const int pc = wid + 8 * i;
        if (pc < 23) { int c = pc * 64 + lane; if (c >= nG * 288) c = 0;
            const int row = c / 36, ch = c % 36, w = ch / 3, cc = ch % 3, sg = w >> 1;
            const char* src = (sg < 3 ? wA + sg * 1152 : wB + (sg - 3) * 1152) + (size_t)(8 * G0 + row) * 3456 + (2 * hp + (w & 1)) * 36 + (cc == 2 ? 20 : cc * 16);
            glds16_ptr(src, lds0 + L_WP + pc * 1024); }
        else glds16(G0 * 16 + (lane < nG ? lane : 0) * 16, gw, lds0 + L_GW);
    }
    const int krot = (2 * (hp & 3) + (rt & 7)) % 9;
    auto kact = [&](int kb) { const int k = kb + krot; return k >= 9 ? k - 9 : k; };
    unsigned xo[4];
    auto issue_x = [&](int kb, int slot) {
        const char* src = x + kact(kb) * 128;
#pragma unroll
        for (int i = 0; i < 4; ++i) glds16(xo[i], src, lds0 + L_X + slot * XS + lw * 4096 + i * 1024);
    };
    if (loader) {
#pragma unroll
        for (int i = 0; i < 4; ++i) { const int row = 32 * lw + 8 * i + (lane >> 3); xo[i] = (unsigned)(rt * 128 + row) * 1152u + (unsigned)(((lane & 7) ^ ((row >> 1) & 7)) << 4); }
        issue_x(0, 0);
        WAITV(4);
    } else WAITV(0);
    BAR();
    asm volatile("" : "+v"(tagv));
    const unsigned tag = __builtin_amdgcn_readfirstlane(tagv) + 1u;
    STAMP(1);
    {
        const int q4 = r16 >> 2, p4 = r16 & 3;
        const unsigned wtb = (unsigned)(WS_WT + (size_t)hp * 9 * 28672);
        u32x4 v[3]; unsigned off[3];
#pragma unroll
        for (int i = 0; i < 2; ++i) {
            const int U0 = i * 32 + wid * 4 + g, U = U0 < nG * 12 ? U0 : 0, Gi = (U * 43) >> 9, ht = U - 12 * Gi, hh2 = ht >= 6, t = ht - 6 * hh2;
            const lptr p = lds + L_WP + (8 * Gi + q4) * 576 + (((0x541032 >> (4 * t)) & 7) * 2 + hh2) * 48 + p4 * 8;
            v[i] = __builtin_bit_cast(u32x4, cat8(trrd(p), trrd(p + 4 * 576)));
            const int G = G0 + Gi;
            off[i] = U0 < nG * 12 ? wtb + (G >> 3) * 28672 + ((hh2 * 7 + t) * 2 + ((G >> 2) & 1)) * 1024 + ((G & 3) * 16 + r16) * 16 : 0xfffffff0u;
        }
        {
            const int U0 = wid * 4 + g, U = U0 < nG * 2 ? U0 : 0, Gi = U >> 1, hh2 = U & 1;
            const u32x4 gt = gather8(lds + L_WP + 8 * Gi * 576 + ((r16 < 12 ? r16 >> 1 : 0) * 2 + hh2) * 48 + 44 + 2 * (r16 & 1), 576);
            const u32x4 gv = *(const LAS u32x4*)(lds + L_GW + Gi * 16);
            v[2] = r16 < 12 ? gt : r16 == 12 ? gv : u32x4{0, 0, 0, 0};
            const int G = G0 + Gi;
            off[2] = U0 < nG * 2 ? wtb + (G >> 3) * 28672 + ((hh2 * 7 + 6) * 2 + ((G >> 2) & 1)) * 1024 + ((G & 3) * 16 + r16) * 16 : 0xfffffff0u;
        }
#pragma unroll
        for (int i = 0; i < 3; ++i) st_dev(wsr, off[i], v[i]);
    }
    if (loader) { issue_x(1, 1); issue_x(2, 2); WAITV(8); } else WAITV(0);
    BAR();
    if (tid == 0) { set_flag(F_W + (hp * 16 + rt) * 32, tag); *(LAS u32x2*)(lds + L_CNT) = u32x2{0u, 0u}; }
    STAMP(2);
    const char* wt = ws + WS_WT + (size_t)hp * 9 * 28672;
    auto issue_w = [&](int kb, int slot) {
#pragma unroll
        for (int i = 0; i < 7; ++i) glds16_dev((unsigned)(lw * 1024 + lane * 16), wt + kact(kb) * 28672 + i * 4096, lds0 + L_W + slot * WS1 + i * 4096 + lw * 1024);
    };
    if (loader) {
#pragma unroll
        for (int i = 0; i < 2; ++i) { const int pc = lw + 4 * i;
            if (pc < 6) { int c = pc * 64 + lane; if (c >= nC * 72) c = 0;
                const int row = c / 9, cc = c % 9, C = C0 + (row >> 3), hq = C / 9, p = 8 * (C % 9) + (row & 7);
                int br, head, d;
                if (p < 64) { br = p < 32; head = (p >> 4) & 1; d = p & 15; } else { br = p < 68; head = (p >> 1) & 1; d = 16 + (p & 1); }
                glds16_ptr((br ? woB : woA) + ((2 * hq + head) * 18 + d) * 1152 + cgp * 144 + cc * 16, lds0 + L_WOP + pc * 1024); }
            else { int c = (pc - 6) * 64 + lane; if (c >= 108) c = 0; const int row = c / 3, cc = c % 3;
                glds16((unsigned)(hp * 1296 + row * 36 + (cc == 2 ? 20 : cc * 16)), wphi, lds0 + L_PHI + (pc - 6) * 1024); } }
    } else if (wid == 0) wait_flag(F_W + (hp * 16 + r16) * 32, tag);
    BAR();
    STAMP(3);
    u32x4 pkt[2][2];
    if (!loader) {
        f32x4 acc[7][4];
#pragma unroll
        for (int t = 0; t < 7; ++t)
#pragma unroll
            for (int u = 0; u < 4; ++u) acc[t][u] = f32x4{0, 0, 0, 0};
        const int th = lw & 1;
        const int xr = L_X + (64 * th + r16) * 128 + ((g ^ (r16 >> 1)) << 4), wr = L_W + h * 14336 + lane * 16;
        bf16x8 wa[7], xa[4], wb[7], xb[4];
        BAR();
#pragma unroll
        for (int u = 0; u < 4; ++u) xa[u] = ld128(lds + xr + u * 2048);
#pragma unroll
        for (int t = 0; t < 7; ++t) wa[t] = ld128(lds + wr + t * 2048);
        int slot = 0;
        for (int i = 0; i < 8; ++i) {
            const lptr sx = lds + slot * XS, sw = lds + slot * WS1;
            SB();
#pragma unroll
            for (int u = 0; u < 4; ++u) xb[u] = ld128(sx + (xr ^ 64) + u * 2048);
#pragma unroll
            for (int t = 0; t < 7; ++t) { wb[t] = ld128(sw + wr + t * 2048 + 1024); SB();
#pragma unroll
                for (int u = 0; u < 4; ++u) acc[t][u] = MFMA16(wa[t], xa[u], acc[t][u]);
                SB(); }
            BAR();
            slot = slot == 2 ? 0 : slot + 1;
            const lptr nx = lds + slot * XS, nw = lds + slot * WS1;
            SB();
#pragma unroll
            for (int u = 0; u < 4; ++u) xa[u] = ld128(nx + xr + u * 2048);
#pragma unroll
            for (int t = 0; t < 7; ++t) { const bf16x8 n = ld128(nw + wr + t * 2048); SB();
#pragma unroll
                for (int u = 0; u < 4; ++u) acc[t][u] = MFMA16(wb[t], xb[u], acc[t][u]);
                SB(); wa[t] = n; }
        }
        {
            const lptr sx = lds + 2 * XS, sw = lds + 2 * WS1;
            SB();
#pragma unroll
            for (int u = 0; u < 4; ++u) xb[u] = ld128(sx + (xr ^ 64) + u * 2048);
#pragma unroll
            for (int t = 0; t < 7; ++t) { wb[t] = ld128(sw + wr + t * 2048 + 1024); SB();
#pragma unroll
                for (int u = 0; u < 4; ++u) acc[t][u] = MFMA16(wa[t], xa[u], acc[t][u]);
                SB(); }
            BAR();
#pragma unroll
            for (int t = 0; t < 7; ++t)
#pragma unroll
                for (int u = 0; u < 4; ++u) acc[t][u] = MFMA16(wb[t], xb[u], acc[t][u]);
        }
        STAMP(4);
        const int row0 = (64 * th + r16) * 48;
        const lptr Kb = lds + L_K + h * 36864 + 256 * 48 + row0, Vb = Kb + 18432, Qb = lds + L_Q + h * 6144 + row0, VAb = lds + L_VA + h * 6144 + row0;
#pragma unroll
        for (int u = 0; u < 4; ++u) {
            *(LAS u32x2*)(Kb + u * 768 + 8 * g) = u32x2{cvtpk(acc[4][u][0], acc[4][u][1]), cvtpk(acc[4][u][2], acc[4][u][3])};
            *(LAS u32x2*)(Vb + u * 768 + 8 * g) = u32x2{cvtpk(acc[5][u][0], acc[5][u][1]), cvtpk(acc[5][u][2], acc[5][u][3])};
        }
        if (g == 2) {
#pragma unroll
            for (int u = 0; u < 4; ++u) {
                *(LAS u32x4*)(Kb + u * 768 + 32) = u32x4{cvtpk(acc[6][u][0], acc[6][u][1]), 0u, 0u, 0u};
                *(LAS u32x4*)(Vb + u * 768 + 32) = u32x4{cvtpk(acc[6][u][2], acc[6][u][3]), 0x00003f80u, 0u, 0u};
            }
        }
        unsigned st0[4], st1[4]; float gt[4];
#pragma unroll
        for (int u = 0; u < 4; ++u) {
            const unsigned s0 = cvtpk(acc[6][u][0], acc[6][u][1]), s1 = cvtpk(acc[6][u][2], acc[6][u][3]);
#pragma unroll
            for (int X = 0; X < 2; ++X) {
                const u32x4 tp = {cvtpk(acc[2 + X][u][0], acc[2 + X][u][1]), cvtpk(acc[2 + X][u][2], acc[2 + X][u][3]), s0, s1};
                if (u < 2) pkt[X][u] = tp; else *(LAS u32x4*)(lds + L_PKX + ((lw * 2 + X) * 2 + (u - 2)) * 1024 + lane * 16) = tp;
            }
            *(LAS u32x2*)(VAb + u * 768 + 8 * g) = u32x2{cvtpk(acc[0][u][0], acc[0][u][1]), cvtpk(acc[0][u][2], acc[0][u][3])};
            *(LAS u32x2*)(Qb + u * 768 + 8 * g) = u32x2{cvtpk(acc[1][u][0] * QSCALE, acc[1][u][1] * QSCALE), cvtpk(acc[1][u][2] * QSCALE, acc[1][u][3] * QSCALE)};
            st0[u] = s0; st1[u] = cvtpk(acc[6][u][2] * QSCALE, acc[6][u][3] * QSCALE);
            gt[u] = __builtin_amdgcn_rcpf(1.f + __builtin_amdgcn_exp2f(-acc[6][u][0] * LOG2E));
        }
        if (g == 1) {
#pragma unroll
            for (int u = 0; u < 4; ++u) { *(LAS u32x2*)(VAb + u * 768 + 32) = u32x2{st0[u], 0x00003f80u}; *(LAS unsigned*)(Qb + u * 768 + 32) = st1[u]; }
        } else if (g == 3 && h == 0) {
#pragma unroll
            for (int u = 0; u < 4; ++u) *(LAS float*)(lds + L_GATE + (64 * th + 16 * u + r16) * 4) = gt[u];
        }
    } else {
        issue_w(0, 0); issue_w(1, 1); issue_w(2, 2);
        WAITV(14);
        BAR();
        for (int i = 0; i < 8; ++i) {
            if (i == 0) WAITV(7); else if (i < 7) WAITV(11); else WAITV(1);
            if (i == 1 && lw == 0 && lane < 4) *(LAS u32x4*)(lds + L_ZERO + lane * 16) = u32x4{0, 0, 0, 0};
            BAR();
            if (i < 6) { issue_x(i + 3, i % 3); issue_w(i + 3, i % 3); }
            if (i == 6 || i == 7) {
                const int q4 = r16 >> 2, p4 = r16 & 3;
                const int U0 = (i - 6) * 16 + lw * 4 + g, U = U0 < nC * 5 ? U0 : 0, Ci = (U * 52) >> 8, t = U - 5 * Ci, C = C0 + Ci, kq = (C * 57) >> 11, s = (C - 36 * kq) >> 2, gg = C & 3;
                const lptr p = lds + L_WOP + (8 * Ci + q4) * 144 + (t < 4 ? t * 32 + p4 * 8 : 128 + (p4 & 1) * 8);
                const u32x4 v = __builtin_bit_cast(u32x4, cat8(trrd(p), trrd(p + 4 * 144)));
                const unsigned off = (unsigned)(WS_WOT + (size_t)cgp * 165888) + (s * 4 + kq) * 4608 + (t < 4 ? t * 1024 + (gg * 16 + r16) * 16 : 4096 + (gg * 8 + r16) * 16);
                st_dev(wsr, U0 < nC * 5 && (t < 4 || r16 < 8) ? off : 0xfffffff0u, v);
            }
        }
        BAR();
    }
    BAR();
    STAMP(5);
    const int cg = (b >> 3) & 7, r64 = 4 * (b & 7) + (b >> 6);
    if (loader) {
#pragma unroll
        for (int X = 0; X < 2; ++X)
#pragma unroll
            for (int u = 0; u < 2; ++u) pkt[X][u] = *(const LAS u32x4*)(lds + L_PKX + (((lw ^ 1) * 2 + X) * 2 + u) * 1024 + lane * 16);
    } else {
        u32x4 kvv[6];
        const lptr kr = lds + L_K + h * 36864 + (256 + 64 * (lw & 1)) * 48 + lane * 16;
#pragma unroll
        for (int i = 0; i < 3; ++i) { kvv[i] = *(const LAS u32x4*)(kr + i * 1024); kvv[3 + i] = *(const LAS u32x4*)(kr + 18432 + i * 1024); }
        const unsigned ko = (unsigned)(WS_KV + (size_t)((rt * 16 + hp) * 4 + h * 2) * 6144) + (lw & 1) * 3072 + lane * 16;
#pragma unroll
        for (int i = 0; i < 3; ++i) { st_dev(wsr, ko + i * 1024, kvv[i]); st_dev(wsr, ko + 6144 + i * 1024, kvv[3 + i]); }
    }
    unsigned phq[9];
    float p[2][9];
    {
        unsigned phk[9];
        const int q4 = r16 >> 2, p4 = r16 & 3;
        const unsigned pw0 = lds0 + L_PHI + h * 864, zp = lds0 + L_ZERO;
        s16x4 R1[2][3], R2[2][2][3];
#pragma unroll
        for (int hf = 0; hf < 2; ++hf) {
            const bool ok = (p4 >> 1) == hf;
            const lptr a = (lptr)(uintptr_t)(ok ? pw0 + (4 * g + q4) * 48 + 16 * (p4 & 1) : zp), a2 = (lptr)(uintptr_t)(ok ? pw0 + (4 * g + q4) * 48 + 40 : zp);
            R1[hf][0] = trrd(a); R1[hf][1] = trrd(a + 8); R1[hf][2] = trrd(a2);
#pragma unroll
            for (int X = 0; X < 2; ++X) {
                const bool ok2 = ok && g == 0 && (q4 >> 1) == X;
                const lptr c = (lptr)(uintptr_t)(ok2 ? pw0 + (16 + (q4 & 1)) * 48 + 16 * (p4 & 1) : zp), c2 = (lptr)(uintptr_t)(ok2 ? pw0 + (16 + (q4 & 1)) * 48 + 40 : zp);
                R2[X][hf][0] = trrd(c); R2[X][hf][1] = trrd(c + 8); R2[X][hf][2] = trrd(c2);
            }
        }
        SB();
#pragma unroll
        for (int X = 0; X < 2; ++X) {
            const bf16x8 bA = __builtin_bit_cast(bf16x8, pkt[X][0]), bB = __builtin_bit_cast(bf16x8, pkt[X][1]);
            f32x4 d[3];
#pragma unroll
            for (int i = 0; i < 3; ++i) d[i] = MFMA16(cat8(R1[0][i], R2[X][0][i]), bA, (f32x4{0, 0, 0, 0}));
#pragma unroll
            for (int i = 0; i < 3; ++i) d[i] = MFMA16(cat8(R1[1][i], R2[X][1][i]), bB, d[i]);
#pragma unroll
            for (int j = 0; j < 4; ++j) { p[X][j] = d[0][j]; p[X][4 + j] = d[1][j]; }
            p[X][8] = (g & 1) ? d[2][3] : d[2][2];
        }
        feature_map(p[1], phk);
        const lptr pkp = lds + L_PK + h * 10240 + (32 * tg + 16 * (g >> 1) + r16) * 80 + (g & 1) * 40;
        *(LAS u32x2*)(pkp) = u32x2{phk[0], phk[1]}; *(LAS u32x2*)(pkp + 8) = u32x2{phk[2], phk[3]}; *(LAS u32x2*)(pkp + 16) = u32x2{phk[4], phk[5]};
        *(LAS u32x2*)(pkp + 24) = u32x2{phk[6], phk[7]}; *(LAS u32x2*)(pkp + 32) = u32x2{phk[8], 0u};
        WAITV(0);
        if (!loader && lane == 0 && __hip_atomic_fetch_add((LAS unsigned*)(lds + L_CNT), 1u, __ATOMIC_RELAXED, __HIP_MEMORY_SCOPE_WORKGROUP) == 3u) set_flag(F_KV + (hp * 16 + rt) * 32, tag);
    }
    BAR();
    if (tid == 0) set_flag(F_WO + b * 32, tag);
    STAMP(6);
    const bool fetcher = !loader && (lw & 1) == 0 && rt > 0;
    gu32* fkp = F_KV + (hp * 16 + rt - 1 - (rt > 1 ? (lane & 1) : 0)) * 32;
    unsigned fk = 0;
    if (fetcher) fk = flag_request(fkp);
    const bool pkw = wid != 4 && wid != 6;
    if (pkw) {
        const int w6 = wid < 4 ? wid : wid == 5 ? 4 : 5, hh = w6 / 3, mt = w6 % 3, q4 = r16 >> 2, p4 = r16 & 3;
        int u = 4 * mt + p4; if (u > 9) u = 9;
        const lptr pa = lds + L_PK + hh * 10240 + (8 * g + q4) * 80 + u * 8;
        const lptr pb0 = lds + L_VA + hh * 6144 + (8 * g + q4) * 48 + p4 * 8;
        const lptr pb1 = lds + L_VA + hh * 6144 + (8 * g + q4) * 48 + 32;
        f32x4 d0 = {0, 0, 0, 0}, d1 = {0, 0, 0, 0};
        bf16x8 a[4], b0[4], b1[4];
#pragma unroll
        for (int ks = 0; ks < 4; ++ks) {
            a[ks] = cat8(trrd(pa + ks * 32 * 80), trrd(pa + ks * 32 * 80 + 4 * 80));
            b0[ks] = cat8(trrd(pb0 + ks * 32 * 48), trrd(pb0 + ks * 32 * 48 + 4 * 48));
            b1[ks] = cat8(trrd(pb1 + ks * 32 * 48), trrd(pb1 + ks * 32 * 48 + 4 * 48));
        }
        SB();
#pragma unroll
        for (int ks = 0; ks < 4; ++ks) { d0 = MFMA16(a[ks], b0[ks], d0); d1 = MFMA16(a[ks], b1[ks], d1); }
        const int uu = 4 * mt + g;
        const unsigned po = (unsigned)(WS_PART + (size_t)(hp * 16 + rt) * 3072 + hh * 1520);
        if (uu < 10) {
            __builtin_amdgcn_raw_buffer_store_b64(u32x2{cvtpk(d0[0], d0[1]), cvtpk(d0[2], d0[3])}, wsr, po + (r16 * 10 + uu) * 8, 0, 16);
            if (r16 < 3) __builtin_amdgcn_raw_buffer_store_b64(u32x2{cvtpk(d1[0], d1[1]), cvtpk(d1[2], d1[3])}, wsr, po + ((16 + r16) * 10 + uu) * 8, 0, 16);
        }
    }
    const int q32 = lane & 31, hh = lane >> 5;
    const char* wot = ws + WS_WOT + (size_t)cg * 165888;
    auto g3_step = [&](int p) { const int s = p + cg; return s >= 9 ? s - 9 : s; };
    f32x16 oacc;
    u32x4 pv[3][4];
    auto pv_load = [&]() {
#pragma unroll
        for (int n = 0; n < 3; ++n) { const int gr = (3 * lw + n) * 16 + r16;
#pragma unroll
            for (int m = 0; m < 4; ++m) pv[n][m] = ld_dev(wsr, (unsigned)(WS_PART + (size_t)(hp * 16 + 4 * m + g) * 3072) + (gr < 190 ? gr : 0) * 16); }
    };
    {
        const lptr Kb = lds + L_K + h * 36864, Vb = Kb + 18432, Qb = lds + L_Q + h * 6144;
        const bf16x8 qf0 = ld128(Qb + (32 * tg + q32) * 48 + 16 * hh);
        const unsigned q16 = *(const LAS unsigned*)(Qb + (32 * tg + q32) * 48 + 32);
        const bf16x8 qf1 = mk8(hh ? 0u : q16, 0u, 0u, 0u);
        const lptr kp = Kb + (32 * tg + q32) * 48;
        const int i16 = lane & 15, dhalf = (lane >> 4) & 1;
        const lptr vp = Vb + (32 * tg + 4 * hh + (i16 >> 2)) * 48 + (dhalf ? 32 + ((i16 & 3) ? 8 : 0) : (i16 & 3) * 8);
        f32x16 negm;
#pragma unroll
        for (int r = 0; r < 16; ++r) { oacc[r] = 0.f; negm[r] = 0.f; }
        bf16x8 k0, k1, k2, k3;
        auto kfetch = [&](int KN) { const int KM = KN > 0 ? KN - 1 : 0;
            k0 = ld128(kp + KN * 32 * 48 + 16 * hh); k1 = ld128(kp + KN * 32 * 48 + 32); k2 = ld128(kp + KM * 32 * 48 + 16 * hh); k3 = ld128(kp + KM * 32 * 48 + 32); };
        auto vfrag = [&](int KT, int hf) { return cat8(trrd(vp + (KT * 32 + 16 * hf) * 48), trrd(vp + (KT * 32 + 16 * hf + 8) * 48)); };
        auto rowmax = [&](const f32x16& s) { float m = fmaxf(fmaxf(s[0], s[1]), s[2]);
#pragma unroll
            for (int r = 3; r < 15; r += 2) m = fmaxf(fmaxf(m, s[r]), s[r + 1]);
            return fmaxf(m, s[15]); };
        auto pack = [&](const f32x16& s, int hf) { return mk8(cvtpk(__builtin_amdgcn_exp2f(s[8 * hf]), __builtin_amdgcn_exp2f(s[8 * hf + 1])), cvtpk(__builtin_amdgcn_exp2f(s[8 * hf + 2]), __builtin_amdgcn_exp2f(s[8 * hf + 3])),
                                                             cvtpk(__builtin_amdgcn_exp2f(s[8 * hf + 4]), __builtin_amdgcn_exp2f(s[8 * hf + 5])), cvtpk(__builtin_amdgcn_exp2f(s[8 * hf + 6]), __builtin_amdgcn_exp2f(s[8 * hf + 7]))); };
        auto tile = [&](int KT, int KN, bool first, int mask) {
            SB();
            f32x16 s = MFMA32(k0, qf0, negm); s = MFMA32(k1, qf1, s);
            SB();
            const bf16x8 va = vfrag(KT, 0), vb = vfrag(KT, 1);
            kfetch(KN);
            SB();
            if (mask) {
#pragma unroll
                for (int r = 0; r < 16; ++r) { const int jj = (r & 3) + 8 * (r >> 2) + 4 * hh; const bool ok = mask == 1 ? jj <= q32 : jj > q32; s[r] = ok ? s[r] : NEGBIG; }
            }
            const float m = x32max(rowmax(s));
            if (first || __any(m > THR)) {
                const float dl = first ? m : fmaxf(m, 0.f);
                const float f = first ? 1.f : __builtin_amdgcn_exp2f(-dl);
#pragma unroll
                for (int r = 0; r < 16; ++r) { s[r] -= dl; negm[r] -= dl; if (!first) oacc[r] *= f; }
            }
            oacc = MFMA32(va, pack(s, 0), oacc);
            oacc = MFMA32(vb, pack(s, 1), oacc);
        };
        auto tile2 = [&](int KT) {
            SB();
            f32x16 s = MFMA32(k0, qf0, negm), t = MFMA32(k2, qf0, negm); s = MFMA32(k1, qf1, s); t = MFMA32(k3, qf1, t);
            SB();
            const bf16x8 va = vfrag(KT, 0), vb = vfrag(KT, 1), vc = vfrag(KT - 1, 0), vd = vfrag(KT - 1, 1);
            kfetch(KT >= 2 ? KT - 2 : 0);
            SB();
            const float m = x32max(fmaxf(rowmax(s), rowmax(t)));
            if (__any(m > THR)) {
                const float dl = fmaxf(m, 0.f), f = __builtin_amdgcn_exp2f(-dl);
#pragma unroll
                for (int r = 0; r < 16; ++r) { s[r] -= dl; t[r] -= dl; negm[r] -= dl; oacc[r] *= f; }
            }
            oacc = MFMA32(va, pack(s, 0), oacc);
            oacc = MFMA32(vb, pack(s, 1), oacc);
            oacc = MFMA32(vc, pack(t, 0), oacc);
            oacc = MFMA32(vd, pack(t, 1), oacc);
        };
        auto wo_piece = [&](int n) { const int pc = n * 4 + lw; if (pc < 54) { const int p = pc / 18, o = pc % 18;
            glds16_dev((unsigned)(o * 1024 + lane * 16), wot + g3_step(p) * G3_SLOT, lds0 + g3_slot(0) + p * G3_SLOT + o * 1024); } };
        auto wo_pieces = [&](int n0, int n1) { if (!loader) for (int n = n0; n < n1; ++n) wo_piece(n); };
        const int lo = rt >= 2 ? 0 : (rt == 1 ? 4 - tg : 8 - tg);
        const int last = lo > 1 ? lo : 1;
        kfetch(8);
        tile(8, 7, true, 1);
        u32x4 hv[24];
        auto part_done = [&]() {
            if (lane == 0 && __hip_atomic_fetch_add((LAS unsigned*)(lds + L_CNT + 4), 1u, __ATOMIC_RELAXED, __HIP_MEMORY_SCOPE_WORKGROUP) == 5u) set_flag(F_P + (hp * 16 + rt) * 32, tag); };
        if (fetcher) {
            WAITV(0);
            part_done();
            flag_confirm(fk, fkp, tag);
#pragma unroll
            for (int n = 0; n < 24; ++n) { const int pc = 2 * n + (lw >> 1), tl = n >= 12, ar = (pc - 24 * tl) / 6, o = pc - 24 * tl - 6 * ar;
                hv[n] = ld_dev(wsr, (unsigned)(WS_KV + (size_t)(((rt - 2 + tl >= 0 ? rt - 2 + tl : 0) * 16 + hp) * 4 + ar) * 6144) + o * 1024 + lane * 16); }
        }
        if (tg >= 2) tile2(7);
        if (tg & 1) tile(tg == 3 ? 5 : 7, 0, false, 0);
        if (pkw && !fetcher) { WAITV(0); part_done(); }
        feature_map(p[0], phq);
        if (fetcher) {
#pragma unroll
            for (int n = 0; n < 24; ++n) { const int pc = 2 * n + (lw >> 1), tl = n >= 12, ar = (pc - 24 * tl) / 6, o = pc - 24 * tl - 6 * ar;
                *(LAS u32x4*)(lds + L_K + ar * 18432 + tl * 6144 + o * 1024 + lane * 16) = hv[n]; }
        }
        BAR();
        STAMP(7);
        gu32* fpp = loader ? F_P + (hp * 16 + r16) * 32 : F_WO + (cg + 8 * (lane & 31)) * 32;
        kfetch(7 - tg);
        if (rt >= 2) {
            if ((tg & 1) == 0) tile(7 - tg, 6 - tg, false, 0);
            if (tg < 2) tile2(6);
            const unsigned fp = flag_request(fpp);
            tile2(4);
            WAITV(0);
            flag_confirm(fp, fpp, tag);
            if (loader) pv_load(); else wo_pieces(0, 7);
            tile2(2); wo_pieces(7, 14);
            tile(0, 0, false, 2);
        } else {
            if (rt == 1) for (int KT = 7 - tg; KT >= last; --KT) tile(KT, KT - 1, false, 0);
            wait_flag(fpp, tag);
            if (loader) pv_load(); else wo_pieces(0, 14);
        }
    }
    STAMP(8);
    const unsigned obase = (unsigned)(WS_O + ((size_t)(rt * 8 + 2 * tg) * 144 + 9 * hp) * 256);
    const lptr stg = lds + L_OST;
    {
        const int tokb = 32 * tg + q32;
        const float gb = *(const LAS float*)(lds + L_GATE + tokb * 4);
        if (loader) {
            const unsigned one = (r16 < 8 && (r16 & 1) == 0) ? 0x00003f80u : (r16 < 8 ? 0x3f800000u : 0u);
            const bf16x8 idn = mk8((r16 >> 1) == 0 ? one : 0u, (r16 >> 1) == 1 ? one : 0u, (r16 >> 1) == 2 ? one : 0u, (r16 >> 1) == 3 ? one : 0u);
#pragma unroll
            for (int n = 0; n < 3; ++n) {
                f32x4 sm = {0, 0, 0, 0};
#pragma unroll
                for (int m = 0; m < 4; ++m) sm = MFMA16(idn, __builtin_bit_cast(bf16x8, pv[n][m]), sm);
                const int gr = (3 * lw + n) * 16 + r16, ph = gr >= 95, e0 = gr - 95 * ph, d = (e0 * 205) >> 10, u = 2 * (e0 - 5 * d) + g;
                if (gr < 190 && g < 2) *(LAS u32x2*)(lds + L_KVT + ph * 2016 + d * 96 + (u >= 5 ? 48 + (u - 5) * 8 : u * 8)) = u32x2{cvtpk(sm[0], sm[1]), cvtpk(sm[2], sm[3])};
            }
        } else {
            const int e0 = lw * 64 + lane;
            if (e0 < 76) *(LAS u32x2*)(lds + L_KVT + (e0 / 38) * 2016 + ((e0 % 38) >> 1) * 96 + (e0 & 1) * 48 + 40) = u32x2{0u, 0u};
            else if (e0 < 88) *(LAS u32x4*)(lds + L_KVT + ((e0 - 76) / 6) * 2016 + 20 * 96 + ((e0 - 76) % 6) * 16) = u32x4{0, 0, 0, 0};
        }
        auto sw = __builtin_amdgcn_permlane32_swap(__float_as_uint(oacc[10]), __float_as_uint(oacc[10]), false, false);
        const float sb = (1.f - gb) * __builtin_amdgcn_rcpf(__uint_as_float(sw[0]));
        const auto s0 = __builtin_amdgcn_permlane32_swap(cvtpk(oacc[0] * sb, oacc[1] * sb), cvtpk(oacc[4] * sb, oacc[5] * sb), false, false);
        const auto s1 = __builtin_amdgcn_permlane32_swap(cvtpk(oacc[2] * sb, oacc[3] * sb), cvtpk(oacc[6] * sb, oacc[7] * sb), false, false);
        st_dev(wsr, obase + (q32 >> 4) * 36864 + (2 * h + hh) * 256 + (q32 & 15) * 16, u32x4{s0[0], s1[0], s0[1], s1[1]});
        if (hh == 0) *(LAS unsigned*)(stg + tokb * 16 + h * 4) = cvtpk(oacc[8] * sb, oacc[9] * sb);
    }
    BAR();
    STAMP(9);
    {
        f32x4 dd[3];
        const int eo = (r16 >> 2) & 1;
        const bool mine = (r16 >> 3) == (g >> 1);
        const bf16x8 b0 = mk8(phq[0], phq[1], phq[2], phq[3]), b1 = mk8(phq[4], phq[5], phq[6], phq[7]), b2 = mk8(phq[8], 0u, 0u, 0u);
        bf16x8 af[3][3];
#pragma unroll
        for (int i = 0; i < 3; ++i) {
            const int sg = 4 * i + (r16 & 3);
            const int d = !mine ? 20 : i < 2 ? 8 * eo + sg : sg == 8 ? 16 + eo : sg == 10 ? 18 : 20;
            const lptr ap = lds + L_KVT + h * 2016 + d * 96 + (g & 1) * 48;
            af[i][0] = ld128(ap); af[i][1] = ld128(ap + 16); af[i][2] = ld128(ap + 32);
        }
        const int tok = 32 * tg + 16 * (g >> 1) + r16;
        const float gate = *(const LAS float*)(lds + L_GATE + tok * 4);
        SB();
#pragma unroll
        for (int i = 0; i < 3; ++i) { f32x4 a = MFMA16(af[i][0], b0, (f32x4{0, 0, 0, 0})); a = MFMA16(af[i][1], b1, a); dd[i] = MFMA16(af[i][2], b2, a); }
        const float sc = gate * __builtin_amdgcn_rcpf(dd[2][2] + 1e-6f);
        st_dev(wsr, obase + (g >> 1) * 36864 + (4 + 2 * h + (g & 1)) * 256 + r16 * 16,
               u32x4{cvtpk(dd[0][0] * sc, dd[0][1] * sc), cvtpk(dd[0][2] * sc, dd[0][3] * sc), cvtpk(dd[1][0] * sc, dd[1][1] * sc), cvtpk(dd[1][2] * sc, dd[1][3] * sc)});
        *(LAS u16*)(stg + tok * 16 + 8 + h * 4 + (g & 1) * 2) = (u16)cvtpk(dd[2][0] * sc, 0.f);
    }
    BAR();
    if (tid < 128) st_dev(wsr, (unsigned)(WS_O + ((size_t)(rt * 8 + (tid >> 4)) * 144 + 9 * hp + 8) * 256) + (tid & 15) * 16, *(const LAS u32x4*)(stg + tid * 16));
    STAMP(10);
    {
        const int rt3 = r64 >> 1, rh = r64 & 1, kq = wid >> 1, rp = wid & 1;
        auto issue3 = [&](int p) {
            if (wid > 0) {
#pragma unroll
                for (int i = 0; i < 3; ++i) { const int pc = (wid - 1) + 7 * i;
                    if (pc < 18) glds16_dev((unsigned)(pc * 1024 + lane * 16), wot + g3_step(p) * G3_SLOT, lds0 + g3_slot(p) + pc * 1024); }
            }
        };
        issue3(3); issue3(4);
        if (wid == 0) WAITV(0); else if (wid < 5) WAITV(6); else WAITV(4);
        BAR();
        if (tid == 0) set_flag(F_O + (rt * 16 + hp) * 32, tag);
        issue3(5); issue3(6); issue3(7);
        const int wro = kq * 4608 + lane * 16, wr4 = kq * 4608 + 4096 + g * 128 + (r16 & 7) * 16;
        u32x4 w8[5];
        { const unsigned wb8 = (unsigned)(WS_WOT + (size_t)cg * 165888) + g3_step(8) * G3_SLOT;
#pragma unroll
          for (int t = 0; t < 5; ++t) w8[t] = ld_dev(wsr, wb8 + (t < 4 ? wro + t * 1024 : wr4)); }
        STAMP(11);
        if (wid == 0) wait_flag(F_O + (rt3 * 16 + r16) * 32, tag);
        DRAIN_BAR();
        STAMP(12);
        u32x4 of[9][2];
        const unsigned ob = (unsigned)(WS_O + ((size_t)(rt3 * 8 + 4 * rh + 2 * rp) * 144 + 36 * kq) * 256 + lane * 16);
        auto o_load = [&](int p) { const unsigned o = ob + g3_step(p) * 1024; of[p][0] = ld_dev(wsr, o); of[p][1] = ld_dev(wsr, o + 36864); };
#pragma unroll
        for (int p = 0; p < ODEPTH; ++p) o_load(p);
        f32x4 a3[5][2];
#pragma unroll
        for (int t = 0; t < 5; ++t) { a3[t][0] = f32x4{0, 0, 0, 0}; a3[t][1] = f32x4{0, 0, 0, 0}; }
        bf16x8 wf[5];
#pragma unroll
        for (int t = 0; t < 4; ++t) wf[t] = ld128(lds + g3_slot(0) + wro + t * 1024);
        wf[4] = ld128(lds + g3_slot(0) + wr4);
#pragma unroll
        for (int p = 0; p < 9; ++p) {
            if (p + ODEPTH < 9) o_load(p + ODEPTH);
            const lptr sp = lds + g3_slot(p + 1 < 8 ? p + 1 : 7);
            const bf16x8 bo0 = __builtin_bit_cast(bf16x8, of[p][0]), bo1 = __builtin_bit_cast(bf16x8, of[p][1]);
            SB();
#pragma unroll
            for (int t = 0; t < 5; ++t) {
                bf16x8 nw = wf[t];
                if (p + 1 < 8) nw = ld128(sp + (t < 4 ? wro + t * 1024 : wr4)); else if (p + 1 == 8) nw = __builtin_bit_cast(bf16x8, w8[t]);
                SB();
                a3[t][0] = MFMA16(wf[t], bo0, a3[t][0]); a3[t][1] = MFMA16(wf[t], bo1, a3[t][1]);
                SB();
                wf[t] = nw;
            }
        }
        BAR();
        STAMP(13);
        const lptr rp0 = lds + rp * 40960 + lane * 16;
        char* orow = (char*)out + (size_t)(rt3 * 128 + 64 * rh + 32 * rp + r16) * 1152 + cg * 144 + g * 8;
        auto finish = [&](auto KQ) {
            constexpr int q = decltype(KQ)::value;
#pragma unroll
            for (int n = 0; n < 10; ++n) if ((n & 3) != q) *(LAS f32x4*)(rp0 + (n * 4 + q) * 1024) = a3[n % 5][n / 5];
            BAR();
            f32x4 pr[3][3];
#pragma unroll
            for (int j = 0; j < 3; ++j) if (q + 4 * j < 10) {
#pragma unroll
                for (int o = 0; o < 3; ++o) pr[j][o] = *(const LAS f32x4*)(rp0 + ((q + 4 * j) * 4 + ((q + 1 + o) & 3)) * 1024); }
#pragma unroll
            for (int j = 0; j < 3; ++j) if (q + 4 * j < 10) { const int n = q + 4 * j, t = n % 5, tt = n / 5;
                const f32x4 u = (a3[t][tt] + pr[j][0]) + (pr[j][1] + pr[j][2]);
                if (t < 4 || g < 2) *(u32x2*)(orow + tt * 16 * 1152 + t * 32) = u32x2{cvtpk(u[0], u[1]), cvtpk(u[2], u[3])};
            }
        };
        if (kq == 0) finish(IC<0>{}); else if (kq == 1) finish(IC<1>{}); else if (kq == 2) finish(IC<2>{}); else finish(IC<3>{});
        STAMP(14);
        asm volatile("fused_code_end:");
        if (b == 0 && tid == 0) set_flag((gu32*)(ws + WS_EPOCH), tag);
    }
}

extern "C" void kernel_launch(void* const* d_in, const int* in_sizes, int n_in, void* d_out, int out_size, void* d_ws, size_t ws_size, hipStream_t stream) {
    (void)hipFuncSetAttribute((const void*)fused_kernel, hipFuncAttributeMaxDynamicSharedMemorySize, LDS_BYTES);
    fused_kernel<<<256, 512, LDS_BYTES, stream>>>((const char*)d_in[0], (const char*)d_in[1], (const char*)d_in[5], (const char*)d_in[2], (const char*)d_in[3],
                                                  (const char*)d_in[6], (const char*)d_in[7], (char*)d_ws, (u16*)d_out);
}
```

```cpp
#include <hip/hip_runtime.h>
#include <hip/hip_bf16.h>
#include <stdint.h>

typedef unsigned short u16;
typedef short bf16x8 __attribute__((ext_vector_type(8)));
typedef short s16x4 __attribute__((ext_vector_type(4)));
typedef float f32x4 __attribute__((ext_vector_type(4)));
typedef float f32x16 __attribute__((ext_vector_type(16)));
typedef unsigned u32x4 __attribute__((ext_vector_type(4)));
typedef unsigned u32x2 __attribute__((ext_vector_type(2)));
typedef __bf16 bf16x2_t __attribute__((ext_vector_type(2)));
typedef float f32x2 __attribute__((ext_vector_type(2)));
#define LAS __attribute__((address_space(3)))
#define GAS __attribute__((address_space(1)))
typedef LAS char* lptr;
typedef GAS unsigned gu32;

constexpr size_t WS_EPOCH = 0;
constexpr size_t WS_FLAG = 4096;
constexpr size_t WS_WT   = 4096 + 5 * 256 * 128;
constexpr size_t WS_WOT  = WS_WT + 16 * 9 * 28672;
constexpr size_t WS_KV   = WS_WOT + 8 * 165888;
constexpr size_t WS_PART = WS_KV + 256 * 24576;
constexpr size_t WS_O    = WS_PART + 256 * 6144;
constexpr size_t WS_DBG  = WS_O + 16 * 8 * 36864;

constexpr int XS = 16384, WS1 = 28672;
constexpr int L_X = 0, L_W = 3 * XS;
constexpr int L_WP = L_W + 2 * WS1;
constexpr int L_GW = L_WP + 23 * 1024;
constexpr int L_PHI = L_W + 3 * WS1;
constexpr int L_ZERO = L_PHI + 1792;
constexpr int L_WOP = L_PHI + 2048;
constexpr int L_PKX = 118784;
constexpr int L_CNT = 152064;
constexpr int L_K = 0;
constexpr int L_Q = 73728;
constexpr int L_VA = 86016;
constexpr int L_PK = 98304;
constexpr int L_GATE = 147456;
constexpr int L_KVT = 147968;
constexpr int L_OST = 73728;
constexpr int LDS_BYTES = 160 * 1024;
#define ODEPTH 5
constexpr int G3_SLOT = 18432;
__device__ __forceinline__ constexpr int g3_slot(int p) { return (p < 3 ? 5 + p : p - 3) * G3_SLOT; }
constexpr unsigned SPIN_CAP = 1u << 18;
constexpr float LOG2E = 1.4426950408889634f;
constexpr float QSCALE = 0.23570226039551584f * LOG2E;
constexpr float THR = 8.f;
constexpr float NEGBIG = -1e30f;

__device__ __forceinline__ unsigned cvtpk(float lo, float hi) { const f32x2 v = {lo, hi}; return __builtin_bit_cast(unsigned, __builtin_convertvector(v, bf16x2_t)); }
__device__ __forceinline__ void glds16(unsigned voff, const void* sbase, unsigned lds_addr) {
    unsigned sv;
    asm volatile("s_mov_b32 %0, m0\n\ts_mov_b32 m0, %3\n\ts_nop 0\n\tglobal_load_lds_dwordx4 %1, %2\n\ts_mov_b32 m0, %0" : "=&s"(sv) : "v"(voff), "s"(sbase), "s"(lds_addr) : "memory");
}
__device__ __forceinline__ void glds16_dev(unsigned voff, const void* sbase, unsigned lds_addr) {
    unsigned sv;
    asm volatile("s_mov_b32 %0, m0\n\ts_mov_b32 m0, %3\n\ts_nop 0\n\tglobal_load_lds_dwordx4 %1, %2 sc1\n\ts_mov_b32 m0, %0" : "=&s"(sv) : "v"(voff), "s"(sbase), "s"(lds_addr) : "memory");
}
__device__ __forceinline__ void glds16_ptr(const void* g, unsigned lds_addr) {
    unsigned sv;
    asm volatile("s_mov_b32 %0, m0\n\ts_mov_b32 m0, %2\n\ts_nop 0\n\tglobal_load_lds_dwordx4 %1, off\n\ts_mov_b32 m0, %0" : "=&s"(sv) : "v"(g), "s"(lds_addr) : "memory");
}
#define WAITV(n) asm volatile("s_waitcnt vmcnt(" #n ")" ::: "memory")
#define WAITL() asm volatile("s_waitcnt lgkmcnt(0)" ::: "memory")
#define LGKM0() do { __builtin_amdgcn_s_waitcnt(0xC07F); asm volatile("" ::: "memory"); } while (0)
#define BAR() do { asm volatile("" ::: "memory"); __builtin_amdgcn_s_waitcnt(0xC07F); __builtin_amdgcn_s_barrier(); asm volatile("" ::: "memory"); } while (0)
#define DRAIN_BAR() do { asm volatile("s_waitcnt vmcnt(0)" ::: "memory"); BAR(); } while (0)
#define SB() __builtin_amdgcn_sched_barrier(0)
#define MFMA16(a, b, c) __builtin_amdgcn_mfma_f32_16x16x32_bf16(a, b, c, 0, 0, 0)
#define MFMA32(a, b, c) __builtin_amdgcn_mfma_f32_32x32x16_bf16(a, b, c, 0, 0, 0)
__device__ __forceinline__ bf16x8 ld128(lptr p) { return *(const LAS bf16x8*)p; }
__device__ __forceinline__ s16x4 trrd(lptr p) { typedef short v4i16_t __attribute__((ext_vector_type(4))); return __builtin_bit_cast(s16x4, __builtin_amdgcn_ds_read_tr16_b64_v4i16((LAS v4i16_t*)p)); }
__device__ __forceinline__ bf16x8 cat8(s16x4 a, s16x4 b) { return __builtin_shufflevector(a, b, 0, 1, 2, 3, 4, 5, 6, 7); }
__device__ __forceinline__ bf16x8 mk8(unsigned a, unsigned b, unsigned c, unsigned d) { u32x4 w = {a, b, c, d}; return __builtin_bit_cast(bf16x8, w); }
__device__ __forceinline__ float vmax2(unsigned a, unsigned b) { float r; asm("v_max_f32 %0, %1, %2" : "=v"(r) : "v"(a), "v"(b)); return r; }
__device__ __forceinline__ float x16max(float x) { auto s = __builtin_amdgcn_permlane16_swap(__float_as_uint(x), __float_as_uint(x), false, false); return vmax2(s[0], s[1]); }
__device__ __forceinline__ float x16sum(float x) { auto s = __builtin_amdgcn_permlane16_swap(__float_as_uint(x), __float_as_uint(x), false, false); return __uint_as_float(s[0]) + __uint_as_float(s[1]); }
__device__ __forceinline__ float x32max(float x) { auto s = __builtin_amdgcn_permlane32_swap(__float_as_uint(x), __float_as_uint(x), false, false); return vmax2(s[0], s[1]); }
__device__ __forceinline__ void st_dev(const __amdgpu_buffer_rsrc_t rs, unsigned off, u32x4 v) { __builtin_amdgcn_raw_buffer_store_b128(v, rs, off, 0, 16); }
__device__ __forceinline__ u32x4 ld_dev(const __amdgpu_buffer_rsrc_t rs, unsigned off) { return __builtin_amdgcn_raw_buffer_load_b128(rs, off, 0, 16); }
__device__ __forceinline__ void wait_flag(gu32* p, unsigned tag) {
    for (unsigned spins = 0; spins < SPIN_CAP; ++spins) {
        const unsigned v = __hip_atomic_load(p, __ATOMIC_RELAXED, __HIP_MEMORY_SCOPE_AGENT);
        if (__all(v == tag)) break;
        __builtin_amdgcn_s_sleep(1);
    }
}
__device__ __forceinline__ void set_flag(gu32* p, unsigned tag) { __hip_atomic_store(p, tag, __ATOMIC_RELAXED, __HIP_MEMORY_SCOPE_AGENT); }
__device__ __forceinline__ u32x4 gather8(lptr p, int stride) {
    unsigned w[4];
#pragma unroll
    for (int j = 0; j < 4; ++j) w[j] = (unsigned)*(const LAS u16*)(p + 2 * j * stride) | ((unsigned)*(const LAS u16*)(p + (2 * j + 1) * stride) << 16);
    return u32x4{w[0], w[1], w[2], w[3]};
}
template <int V> struct IC { static constexpr int value = V; };
__device__ __forceinline__ int piece_start(int j) { return (9 * j + 1) >> 1; }

__device__ __forceinline__ unsigned flag_request(gu32* p) { unsigned v; asm volatile("global_load_dword %0, %1, off sc1" : "=v"(v) : "v"(p) : "memory"); return v; }
__device__ __forceinline__ void flag_confirm(unsigned v, gu32* p, unsigned tag) {
    asm volatile("" : "+v"(v));
    if (!__all(v == tag)) wait_flag(p, tag);
}

#define STAMP(i) do {} while (0)
#define WSTAMP(i) do {} while (0)

__device__ __forceinline__ void feature_map(const float (&p)[9], unsigned (&o)[9]) {
    float mx = fmaxf(fmaxf(p[0], p[1]), p[2]), mn = fminf(fminf(p[0], p[1]), p[2]);
#pragma unroll
    for (int i = 3; i < 9; i += 2) { mx = fmaxf(fmaxf(mx, p[i]), p[i + 1]); mn = fminf(fminf(mn, p[i]), p[i + 1]); }
    mx = x16max(mx); mn = -x16max(-mn);
    const float mxl = -mx * LOG2E, mnl = mn * LOG2E;
    float a[9], b[9], sa = 0.f, sb = 0.f;
#pragma unroll
    for (int i = 0; i < 9; ++i) { a[i] = __builtin_amdgcn_exp2f(__builtin_fmaf(p[i], LOG2E, mxl)); b[i] = __builtin_amdgcn_exp2f(__builtin_fmaf(p[i], -LOG2E, mnl)); sa += a[i]; sb += b[i]; }
    sa = x16sum(sa); sb = x16sum(sb);
    const float ra = __builtin_amdgcn_rcpf(sa), rb = __builtin_amdgcn_rcpf(sb);
#pragma unroll
    for (int i = 0; i < 9; ++i) { a[i] *= ra; b[i] *= rb; }
    o[0] = cvtpk(a[0], a[1]); o[1] = cvtpk(a[2], a[3]); o[2] = cvtpk(a[4], a[5]); o[3] = cvtpk(a[6], a[7]); o[4] = cvtpk(a[8], b[0]);
    o[5] = cvtpk(b[1], b[2]); o[6] = cvtpk(b[3], b[4]); o[7] = cvtpk(b[5], b[6]); o[8] = cvtpk(b[7], b[8]);
}

__global__ __launch_bounds__(512, 2) void fused_kernel(const char* __restrict__ x, const char* __restrict__ wA, const char* __restrict__ wB, const char* __restrict__ wphi,
                                                       const char* __restrict__ woA, const char* __restrict__ woB, const char* __restrict__ gw,
                                                       char* __restrict__ ws, u16* __restrict__ out) {
    asm volatile("" :: "s"(x), "s"(wA), "s"(wB), "s"(wphi), "s"(woA), "s"(woB), "s"(gw), "s"(ws), "s"(out));
    extern __shared__ __attribute__((aligned(16))) char lds_raw[];
    const lptr lds = (lptr)lds_raw;
    const unsigned lds0 = (unsigned)(uintptr_t)lds_raw;
    const int tid = threadIdx.x, lane = tid & 63, wid = __builtin_amdgcn_readfirstlane(tid >> 6);
    const int b = blockIdx.x, hp = (b & 3) * 4 + ((b >> 3) & 3), rt = ((b >> 2) & 1) * 8 + (b >> 5);
    const bool loader = wid >= 4;
    const int lw = wid & 3;
    const int h = lw >> 1, tg = loader ? 3 - 2 * (lw & 1) : 2 * (lw & 1);
    const int r16 = lane & 15, g = lane >> 4;
    gu32* flags = (gu32*)(ws + WS_FLAG);
    gu32* F_W = flags;
    gu32* F_WO = flags + 256 * 32;
    gu32* F_KV = flags + 512 * 32;
    gu32* F_P = flags + 768 * 32;
    gu32* F_O = flags + 1024 * 32;
    {
        unsigned long long pc0; unsigned len;
        asm volatile("s_getpc_b64 %0\nfused_code_begin:" : "=s"(pc0));
        asm volatile("s_mov_b32 %0, fused_code_end-fused_code_begin" : "=s"(len));
        if (wid == 3) {
            const unsigned j = blockIdx.x >> 3;
            if ((j + 1) * 1024 <= len) glds16(j * 1024 + lane * 16, (const void*)pc0, lds0 + 160256);
            if ((j + 33) * 1024 <= len) glds16((j + 32) * 1024 + lane * 16, (const void*)pc0, lds0 + 160256);
        }
    }
    STAMP(0);
    unsigned tagv = flag_request((gu32*)(ws + WS_EPOCH));
    const __amdgpu_buffer_rsrc_t wsr = __builtin_amdgcn_make_buffer_rsrc(ws, 0, 0x2000000, 0x00020000);

    const int G0 = piece_start(rt), nG = piece_start(rt + 1) - G0;
    const int j2 = b >> 3, cgp = b & 7, C0 = piece_start(j2), nC = piece_start(j2 + 1) - C0;
#pragma unroll
    for (int i = 0; i < 3; ++i) {
        const int pc = wid + 8 * i;
        if (pc < 23) { int c = pc * 64 + lane; if (c >= nG * 288) c = 0;
            const int row = c / 36, ch = c % 36, w = ch / 3, cc = ch % 3, sg = w >> 1;
            const char* src = (sg < 3 ? wA + sg * 1152 : wB + (sg - 3) * 1152) + (size_t)(8 * G0 + row) * 3456 + (2 * hp + (w & 1)) * 36 + (cc == 2 ? 20 : cc * 16);
            glds16_ptr(src, lds0 + L_WP + pc * 1024); }
        else glds16(G0 * 16 + (lane < nG ? lane : 0) * 16, gw, lds0 + L_GW);
    }
    const int krot = (2 * (hp & 3) + (rt & 7)) % 9;
    auto kact = [&](int kb) { const int k = kb + krot; return k >= 9 ? k - 9 : k; };
    unsigned xo[4];
    auto issue_x = [&](int kb, int slot) {
        const char* src = x + kact(kb) * 128;
#pragma unroll
        for (int i = 0; i < 4; ++i) glds16(xo[i], src, lds0 + L_X + slot * XS + lw * 4096 + i * 1024);
    };
    if (loader) {
#pragma unroll
        for (int i = 0; i < 4; ++i) { const int row = 32 * lw + 8 * i + (lane >> 3); xo[i] = (unsigned)(rt * 128 + row) * 1152u + (unsigned)(((lane & 7) ^ ((row >> 1) & 7)) << 4); }
        issue_x(0, 0);
        WAITV(4);
    } else WAITV(0);
    BAR();
    asm volatile("" : "+v"(tagv));
    const unsigned tag = __builtin_amdgcn_readfirstlane(tagv) + 1u;
    STAMP(1);
    {
        const int q4 = r16 >> 2, p4 = r16 & 3;
        const unsigned wtb = (unsigned)(WS_WT + (size_t)hp * 9 * 28672);
        u32x4 v[3]; unsigned off[3];
#pragma unroll
        for (int i = 0; i < 2; ++i) {
            const int U0 = i * 32 + wid * 4 + g, U = U0 < nG * 12 ? U0 : 0, Gi = (U * 43) >> 9, ht = U - 12 * Gi, hh2 = ht >= 6, t = ht - 6 * hh2;
            const lptr p = lds + L_WP + (8 * Gi + q4) * 576 + (((0x541032 >> (4 * t)) & 7) * 2 + hh2) * 48 + p4 * 8;
            v[i] = __builtin_bit_cast(u32x4, cat8(trrd(p), trrd(p + 4 * 576)));
            const int G = G0 + Gi;
            off[i] = U0 < nG * 12 ? wtb + (G >> 3) * 28672 + ((hh2 * 7 + t) * 2 + ((G >> 2) & 1)) * 1024 + ((G & 3) * 16 + r16) * 16 : 0xfffffff0u;
        }
        {
            const int U0 = wid * 4 + g, U = U0 < nG * 2 ? U0 : 0, Gi = U >> 1, hh2 = U & 1;
            const u32x4 gt = gather8(lds + L_WP + 8 * Gi * 576 + ((r16 < 12 ? r16 >> 1 : 0) * 2 + hh2) * 48 + 44 + 2 * (r16 & 1), 576);
            const u32x4 gv = *(const LAS u32x4*)(lds + L_GW + Gi * 16);
            v[2] = r16 < 12 ? gt : r16 == 12 ? gv : u32x4{0, 0, 0, 0};
            const int G = G0 + Gi;
            off[2] = U0 < nG * 2 ? wtb + (G >> 3) * 28672 + ((hh2 * 7 + 6) * 2 + ((G >> 2) & 1)) * 1024 + ((G & 3) * 16 + r16) * 16 : 0xfffffff0u;
        }
#pragma unroll
        for (int i = 0; i < 3; ++i) st_dev(wsr, off[i], v[i]);
    }
    if (loader) { issue_x(1, 1); issue_x(2, 2); WAITV(8); } else WAITV(0);
    BAR();
    if (tid == 0) { set_flag(F_W + (hp * 16 + rt) * 32, tag); *(LAS u32x2*)(lds + L_CNT) = u32x2{0u, 0u}; }
    STAMP(2);
    const char* wt = ws + WS_WT + (size_t)hp * 9 * 28672;
    auto issue_w = [&](int kb, int slot) {
#pragma unroll
        for (int i = 0; i < 7; ++i) glds16_dev((unsigned)(lw * 1024 + lane * 16), wt + kact(kb) * 28672 + i * 4096, lds0 + L_W + slot * WS1 + i * 4096 + lw * 1024);
    };
    if (loader) {
#pragma unroll
        for (int i = 0; i < 2; ++i) { const int pc = lw + 4 * i;
            if (pc < 6) { int c = pc * 64 + lane; if (c >= nC * 72) c = 0;
                const int row = c / 9, cc = c % 9, C = C0 + (row >> 3), hq = C / 9, p = 8 * (C % 9) + (row & 7);
                int br, head, d;
                if (p < 64) { br = p < 32; head = (p >> 4) & 1; d = p & 15; } else { br = p < 68; head = (p >> 1) & 1; d = 16 + (p & 1); }
                glds16_ptr((br ? woB : woA) + ((2 * hq + head) * 18 + d) * 1152 + cgp * 144 + cc * 16, lds0 + L_WOP + pc * 1024); }
            else { int c = (pc - 6) * 64 + lane; if (c >= 108) c = 0; const int row = c / 3, cc = c % 3;
                glds16((unsigned)(hp * 1296 + row * 36 + (cc == 2 ? 20 : cc * 16)), wphi, lds0 + L_PHI + (pc - 6) * 1024); } }
    } else if (wid == 0) wait_flag(F_W + (hp * 16 + r16) * 32, tag);
    BAR();
    STAMP(3);
    u32x4 pkt[2][2];
    if (!loader) {
        f32x4 acc[7][4];
#pragma unroll
        for (int t = 0; t < 7; ++t)
#pragma unroll
            for (int u = 0; u < 4; ++u) acc[t][u] = f32x4{0, 0, 0, 0};
        const int th = lw & 1;
        const int xr = L_X + (64 * th + r16) * 128 + ((g ^ (r16 >> 1)) << 4), wr = L_W + h * 14336 + lane * 16;
        bf16x8 wa[7], xa[4], wb[7], xb[4];
        BAR();
#pragma unroll
        for (int u = 0; u < 4; ++u) xa[u] = ld128(lds + xr + u * 2048);
#pragma unroll
        for (int t = 0; t < 7; ++t) wa[t] = ld128(lds + wr + t * 2048);
        int slot = 0;
        for (int i = 0; i < 8; ++i) {
            const lptr sx = lds + slot * XS, sw = lds + slot * WS1;
            SB();
#pragma unroll
            for (int u = 0; u < 4; ++u) xb[u] = ld128(sx + (xr ^ 64) + u * 2048);
#pragma unroll
            for (int t = 0; t < 7; ++t) { wb[t] = ld128(sw + wr + t * 2048 + 1024); SB();
#pragma unroll
                for (int u = 0; u < 4; ++u) acc[t][u] = MFMA16(wa[t], xa[u], acc[t][u]);
                SB(); }
            BAR();
            slot = slot == 2 ? 0 : slot + 1;
            const lptr nx = lds + slot * XS, nw = lds + slot * WS1;
            SB();
#pragma unroll
            for (int u = 0; u < 4; ++u) xa[u] = ld128(nx + xr + u * 2048);
#pragma unroll
            for (int t = 0; t < 7; ++t) { const bf16x8 n = ld128(nw + wr + t * 2048); SB();
#pragma unroll
                for (int u = 0; u < 4; ++u) acc[t][u] = MFMA16(wb[t], xb[u], acc[t][u]);
                SB(); wa[t] = n; }
        }
        {
            const lptr sx = lds + 2 * XS, sw = lds + 2 * WS1;
            SB();
#pragma unroll
            for (int u = 0; u < 4; ++u) xb[u] = ld128(sx + (xr ^ 64) + u * 2048);
#pragma unroll
            for (int t = 0; t < 7; ++t) { wb[t] = ld128(sw + wr + t * 2048 + 1024); SB();
#pragma unroll
                for (int u = 0; u < 4; ++u) acc[t][u] = MFMA16(wa[t], xa[u], acc[t][u]);
                SB(); }
            BAR();
#pragma unroll
            for (int t = 0; t < 7; ++t)
#pragma unroll
                for (int u = 0; u < 4; ++u) acc[t][u] = MFMA16(wb[t], xb[u], acc[t][u]);
        }
        STAMP(4);
        const int row0 = (64 * th + r16) * 48;
        const lptr Kb = lds + L_K + h * 36864 + 256 * 48 + row0, Vb = Kb + 18432, Qb = lds + L_Q + h * 6144 + row0, VAb = lds + L_VA + h * 6144 + row0;
#pragma unroll
        for (int u = 0; u < 4; ++u) {
            *(LAS u32x2*)(Kb + u * 768 + 8 * g) = u32x2{cvtpk(acc[4][u][0], acc[4][u][1]), cvtpk(acc[4][u][2], acc[4][u][3])};
            *(LAS u32x2*)(Vb + u * 768 + 8 * g) = u32x2{cvtpk(acc[5][u][0], acc[5][u][1]), cvtpk(acc[5][u][2], acc[5][u][3])};
        }
        if (g == 2) {
#pragma unroll
            for (int u = 0; u < 4; ++u) {
                *(LAS u32x4*)(Kb + u * 768 + 32) = u32x4{cvtpk(acc[6][u][0], acc[6][u][1]), 0u, 0u, 0u};
                *(LAS u32x4*)(Vb + u * 768 + 32) = u32x4{cvtpk(acc[6][u][2], acc[6][u][3]), 0x00003f80u, 0u, 0u};
            }
        }
        unsigned st0[4], st1[4]; float gt[4];
#pragma unroll
        for (int u = 0; u < 4; ++u) {
            const unsigned s0 = cvtpk(acc[6][u][0], acc[6][u][1]), s1 = cvtpk(acc[6][u][2], acc[6][u][3]);
#pragma unroll
            for (int X = 0; X < 2; ++X) {
                const u32x4 tp = {cvtpk(acc[2 + X][u][0], acc[2 + X][u][1]), cvtpk(acc[2 + X][u][2], acc[2 + X][u][3]), s0, s1};
                if (u < 2) pkt[X][u] = tp; else *(LAS u32x4*)(lds + L_PKX + ((lw * 2 + X) * 2 + (u - 2)) * 1024 + lane * 16) = tp;
            }
            *(LAS u32x2*)(VAb + u * 768 + 8 * g) = u32x2{cvtpk(acc[0][u][0], acc[0][u][1]), cvtpk(acc[0][u][2], acc[0][u][3])};
            *(LAS u32x2*)(Qb + u * 768 + 8 * g) = u32x2{cvtpk(acc[1][u][0] * QSCALE, acc[1][u][1] * QSCALE), cvtpk(acc[1][u][2] * QSCALE, acc[1][u][3] * QSCALE)};
            st0[u] = s0; st1[u] = cvtpk(acc[6][u][2] * QSCALE, acc[6][u][3] * QSCALE);
            gt[u] = __builtin_amdgcn_rcpf(1.f + __builtin_amdgcn_exp2f(-acc[6][u][0] * LOG2E));
        }
        if (g == 1) {
#pragma unroll
            for (int u = 0; u < 4; ++u) { *(LAS u32x2*)(VAb + u * 768 + 32) = u32x2{st0[u], 0x00003f80u}; *(LAS unsigned*)(Qb + u * 768 + 32) = st1[u]; }
        } else if (g == 3 && h == 0) {
#pragma unroll
            for (int u = 0; u < 4; ++u) *(LAS float*)(lds + L_GATE + (64 * th + 16 * u + r16) * 4) = gt[u];
        }
    } else {
        issue_w(0, 0); issue_w(1, 1); issue_w(2, 2);
        WAITV(14);
        BAR();
        for (int i = 0; i < 8; ++i) {
            if (i == 0) WAITV(7); else if (i < 7) WAITV(11); else WAITV(1);
            if (i == 1 && lw == 0 && lane < 4) *(LAS u32x4*)(lds + L_ZERO + lane * 16) = u32x4{0, 0, 0, 0};
            BAR();
            if (i < 6) { issue_x(i + 3, i % 3); issue_w(i + 3, i % 3); }
            if (i == 6 || i == 7) {
                const int q4 = r16 >> 2, p4 = r16 & 3;
                const int U0 = (i - 6) * 16 + lw * 4 + g, U = U0 < nC * 5 ? U0 : 0, Ci = (U * 52) >> 8, t = U - 5 * Ci, C = C0 + Ci, kq = (C * 57) >> 11, s = (C - 36 * kq) >> 2, gg = C & 3;
                const lptr p = lds + L_WOP + (8 * Ci + q4) * 144 + (t < 4 ? t * 32 + p4 * 8 : 128 + (p4 & 1) * 8);
                const u32x4 v = __builtin_bit_cast(u32x4, cat8(trrd(p), trrd(p + 4 * 144)));
                const unsigned off = (unsigned)(WS_WOT + (size_t)cgp * 165888) + (s * 4 + kq) * 4608 + (t < 4 ? t * 1024 + (gg * 16 + r16) * 16 : 4096 + (gg * 8 + r16) * 16);
                st_dev(wsr, U0 < nC * 5 && (t < 4 || r16 < 8) ? off : 0xfffffff0u, v);
            }
        }
        BAR();
    }
    BAR();
    STAMP(5);
    const int cg = (b >> 3) & 7, r64 = 4 * (b & 7) + (b >> 6);
    if (loader) {
#pragma unroll
        for (int X = 0; X < 2; ++X)
#pragma unroll
            for (int u = 0; u < 2; ++u) pkt[X][u] = *(const LAS u32x4*)(lds + L_PKX + (((lw ^ 1) * 2 + X) * 2 + u) * 1024 + lane * 16);
    } else {
        u32x4 kvv[6];
        const lptr kr = lds + L_K + h * 36864 + (256 + 64 * (lw & 1)) * 48 + lane * 16;
#pragma unroll
        for (int i = 0; i < 3; ++i) { kvv[i] = *(const LAS u32x4*)(kr + i * 1024); kvv[3 + i] = *(const LAS u32x4*)(kr + 18432 + i * 1024); }
        const unsigned ko = (unsigned)(WS_KV + (size_t)((rt * 16 + hp) * 4 + h * 2) * 6144) + (lw & 1) * 3072 + lane * 16;
#pragma unroll
        for (int i = 0; i < 3; ++i) { st_dev(wsr, ko + i * 1024, kvv[i]); st_dev(wsr, ko + 6144 + i * 1024, kvv[3 + i]); }
    }
    unsigned phq[9];
    float p[2][9];
    {
        unsigned phk[9];
        const int q4 = r16 >> 2, p4 = r16 & 3;
        const unsigned pw0 = lds0 + L_PHI + h * 864, zp = lds0 + L_ZERO;
        s16x4 R1[2][3], R2[2][2][3];
#pragma unroll
        for (int hf = 0; hf < 2; ++hf) {
            const bool ok = (p4 >> 1) == hf;
            const lptr a = (lptr)(uintptr_t)(ok ? pw0 + (4 * g + q4) * 48 + 16 * (p4 & 1) : zp), a2 = (lptr)(uintptr_t)(ok ? pw0 + (4 * g + q4) * 48 + 40 : zp);
            R1[hf][0] = trrd(a); R1[hf][1] = trrd(a + 8); R1[hf][2] = trrd(a2);
#pragma unroll
            for (int X = 0; X < 2; ++X) {
                const bool ok2 = ok && g == 0 && (q4 >> 1) == X;
                const lptr c = (lptr)(uintptr_t)(ok2 ? pw0 + (16 + (q4 & 1)) * 48 + 16 * (p4 & 1) : zp), c2 = (lptr)(uintptr_t)(ok2 ? pw0 + (16 + (q4 & 1)) * 48 + 40 : zp);
                R2[X][hf][0] = trrd(c); R2[X][hf][1] = trrd(c + 8); R2[X][hf][2] = trrd(c2);
            }
        }
        SB();
#pragma unroll
        for (int X = 0; X < 2; ++X) {
            const bf16x8 bA = __builtin_bit_cast(bf16x8, pkt[X][0]), bB = __builtin_bit_cast(bf16x8, pkt[X][1]);
            f32x4 d[3];
#pragma unroll
            for (int i = 0; i < 3; ++i) d[i] = MFMA16(cat8(R1[0][i], R2[X][0][i]), bA, (f32x4{0, 0, 0, 0}));
#pragma unroll
            for (int i = 0; i < 3; ++i) d[i] = MFMA16(cat8(R1[1][i], R2[X][1][i]), bB, d[i]);
#pragma unroll
            for (int j = 0; j < 4; ++j) { p[X][j] = d[0][j]; p[X][4 + j] = d[1][j]; }
            p[X][8] = (g & 1) ? d[2][3] : d[2][2];
        }
        feature_map(p[1], phk);
        const lptr pkp = lds + L_PK + h * 10240 + (32 * tg + 16 * (g >> 1) + r16) * 80 + (g & 1) * 40;
        *(LAS u32x2*)(pkp) = u32x2{phk[0], phk[1]}; *(LAS u32x2*)(pkp + 8) = u32x2{phk[2], phk[3]}; *(LAS u32x2*)(pkp + 16) = u32x2{phk[4], phk[5]};
        *(LAS u32x2*)(pkp + 24) = u32x2{phk[6], phk[7]}; *(LAS u32x2*)(pkp + 32) = u32x2{phk[8], 0u};
        WAITV(0);
        if (!loader && lane == 0 && __hip_atomic_fetch_add((LAS unsigned*)(lds + L_CNT), 1u, __ATOMIC_RELAXED, __HIP_MEMORY_SCOPE_WORKGROUP) == 3u) set_flag(F_KV + (hp * 16 + rt) * 32, tag);
    }
    BAR();
    if (tid == 0) set_flag(F_WO + b * 32, tag);
    STAMP(6);
    const bool fetcher = !loader && (lw & 1) == 0 && rt > 0;
    gu32* fkp = F_KV + (hp * 16 + rt - 1 - (rt > 1 ? (lane & 1) : 0)) * 32;
    unsigned fk = 0;
    if (fetcher) fk = flag_request(fkp);
    const bool pkw = wid != 4 && wid != 6;
    if (pkw) {
        const int w6 = wid < 4 ? wid : wid == 5 ? 4 : 5, hh = w6 / 3, mt = w6 % 3, q4 = r16 >> 2, p4 = r16 & 3;
        int u = 4 * mt + p4; if (u > 9) u = 9;
        const lptr pa = lds + L_PK + hh * 10240 + (8 * g + q4) * 80 + u * 8;
        const lptr pb0 = lds + L_VA + hh * 6144 + (8 * g + q4) * 48 + p4 * 8;
        const lptr pb1 = lds + L_VA + hh * 6144 + (8 * g + q4) * 48 + 32;
        f32x4 d0 = {0, 0, 0, 0}, d1 = {0, 0, 0, 0};
        bf16x8 a[4], b0[4], b1[4];
#pragma unroll
        for (int ks = 0; ks < 4; ++ks) {
            a[ks] = cat8(trrd(pa + ks * 32 * 80), trrd(pa + ks * 32 * 80 + 4 * 80));
            b0[ks] = cat8(trrd(pb0 + ks * 32 * 48), trrd(pb0 + ks * 32 * 48 + 4 * 48));
            b1[ks] = cat8(trrd(pb1 + ks * 32 * 48), trrd(pb1 + ks * 32 * 48 + 4 * 48));
        }
        SB();
#pragma unroll
        for (int ks = 0; ks < 4; ++ks) { d0 = MFMA16(a[ks], b0[ks], d0); d1 = MFMA16(a[ks], b1[ks], d1); }
        const int uu = 4 * mt + g;
        const unsigned po = (unsigned)(WS_PART + (size_t)(hp * 16 + rt) * 3072 + hh * 1520);
        if (uu < 10) {
            __builtin_amdgcn_raw_buffer_store_b64(u32x2{cvtpk(d0[0], d0[1]), cvtpk(d0[2], d0[3])}, wsr, po + (r16 * 10 + uu) * 8, 0, 16);
            if (r16 < 3) __builtin_amdgcn_raw_buffer_store_b64(u32x2{cvtpk(d1[0], d1[1]), cvtpk(d1[2], d1[3])}, wsr, po + ((16 + r16) * 10 + uu) * 8, 0, 16);
        }
    }
    const int q32 = lane & 31, hh = lane >> 5;
    const char* wot = ws + WS_WOT + (size_t)cg * 165888;
    auto g3_step = [&](int p) { const int s = p + cg; return s >= 9 ? s - 9 : s; };
    f32x16 oacc;
    const bool summer = tg < 2;
    const int rw = 2 * h + (tg & 1);
    u32x4 pv[3][4];
    auto pv_load = [&]() {
#pragma unroll
        for (int n = 0; n < 3; ++n) { const int gr = (3 * rw + n) * 16 + r16;
#pragma unroll
            for (int m = 0; m < 4; ++m) pv[n][m] = ld_dev(wsr, (unsigned)(WS_PART + (size_t)(hp * 16 + 4 * m + g) * 3072) + (gr < 190 ? gr : 0) * 16); }
    };
    {
        const lptr Kb = lds + L_K + h * 36864, Vb = Kb + 18432, Qb = lds + L_Q + h * 6144;
        const bf16x8 qf0 = ld128(Qb + (32 * tg + q32) * 48 + 16 * hh);
        const unsigned q16 = *(const LAS unsigned*)(Qb + (32 * tg + q32) * 48 + 32);
        const bf16x8 qf1 = mk8(hh ? 0u : q16, 0u, 0u, 0u);
        const lptr kp = Kb + (32 * tg + q32) * 48;
        const int i16 = lane & 15, dhalf = (lane >> 4) & 1;
        const lptr vp = Vb + (32 * tg + 4 * hh + (i16 >> 2)) * 48 + (dhalf ? 32 + ((i16 & 3) ? 8 : 0) : (i16 & 3) * 8);
        f32x16 negm;
#pragma unroll
        for (int r = 0; r < 16; ++r) { oacc[r] = 0.f; negm[r] = 0.f; }
        bf16x8 k0, k1, k2, k3;
        auto kfetch = [&](int KN) { const int KM = KN > 0 ? KN - 1 : 0;
            k0 = ld128(kp + KN * 32 * 48 + 16 * hh); k1 = ld128(kp + KN * 32 * 48 + 32); k2 = ld128(kp + KM * 32 * 48 + 16 * hh); k3 = ld128(kp + KM * 32 * 48 + 32); };
        auto vfrag = [&](int KT, int hf) { return cat8(trrd(vp + (KT * 32 + 16 * hf) * 48), trrd(vp + (KT * 32 + 16 * hf + 8) * 48)); };
        auto rowmax = [&](const f32x16& s) { float m = fmaxf(fmaxf(s[0], s[1]), s[2]);
#pragma unroll
            for (int r = 3; r < 15; r += 2) m = fmaxf(fmaxf(m, s[r]), s[r + 1]);
            return fmaxf(m, s[15]); };
        auto pack = [&](const f32x16& s, int hf) { return mk8(cvtpk(__builtin_amdgcn_exp2f(s[8 * hf]), __builtin_amdgcn_exp2f(s[8 * hf + 1])), cvtpk(__builtin_amdgcn_exp2f(s[8 * hf + 2]), __builtin_amdgcn_exp2f(s[8 * hf + 3])),
                                                             cvtpk(__builtin_amdgcn_exp2f(s[8 * hf + 4]), __builtin_amdgcn_exp2f(s[8 * hf + 5])), cvtpk(__builtin_amdgcn_exp2f(s[8 * hf + 6]), __builtin_amdgcn_exp2f(s[8 * hf + 7]))); };
        auto tile = [&](int KT, int KN, bool first, int mask) {
            SB();
            f32x16 s = MFMA32(k0, qf0, negm); s = MFMA32(k1, qf1, s);
            SB();
            const bf16x8 va = vfrag(KT, 0), vb = vfrag(KT, 1);
            kfetch(KN);
            SB();
            if (mask) {
#pragma unroll
                for (int r = 0; r < 16; ++r) { const int jj = (r & 3) + 8 * (r >> 2) + 4 * hh; const bool ok = mask == 1 ? jj <= q32 : jj > q32; s[r] = ok ? s[r] : NEGBIG; }
            }
            const float m = x32max(rowmax(s));
            if (first || __any(m > THR)) {
                const float dl = first ? m : fmaxf(m, 0.f);
                const float f = first ? 1.f : __builtin_amdgcn_exp2f(-dl);
#pragma unroll
                for (int r = 0; r < 16; ++r) { s[r] -= dl; negm[r] -= dl; if (!first) oacc[r] *= f; }
            }
            oacc = MFMA32(va, pack(s, 0), oacc);
            oacc = MFMA32(vb, pack(s, 1), oacc);
        };
        auto tile2 = [&](int KT) {
            SB();
            f32x16 s = MFMA32(k0, qf0, negm), t = MFMA32(k2, qf0, negm); s = MFMA32(k1, qf1, s); t = MFMA32(k3, qf1, t);
            SB();
            const bf16x8 va = vfrag(KT, 0), vb = vfrag(KT, 1), vc = vfrag(KT - 1, 0), vd = vfrag(KT - 1, 1);
            kfetch(KT >= 2 ? KT - 2 : 0);
            SB();
            const float m = x32max(fmaxf(rowmax(s), rowmax(t)));
            if (__any(m > THR)) {
                const float dl = fmaxf(m, 0.f), f = __builtin_amdgcn_exp2f(-dl);
#pragma unroll
                for (int r = 0; r < 16; ++r) { s[r] -= dl; t[r] -= dl; negm[r] -= dl; oacc[r] *= f; }
            }
            oacc = MFMA32(va, pack(s, 0), oacc);
            oacc = MFMA32(vb, pack(s, 1), oacc);
            oacc = MFMA32(vc, pack(t, 0), oacc);
            oacc = MFMA32(vd, pack(t, 1), oacc);
        };
        auto wo_piece = [&](int n) { const int pc = n * 4 + rw; if (pc < 54) { const int p = pc / 18, o = pc % 18;
            glds16_dev((unsigned)(o * 1024 + lane * 16), wot + g3_step(p) * G3_SLOT, lds0 + g3_slot(0) + p * G3_SLOT + o * 1024); } };
        auto wo_pieces = [&](int n0, int n1) { if (!summer) for (int n = n0; n < n1; ++n) wo_piece(n); };
        const int lo = rt >= 2 ? 0 : (rt == 1 ? 4 - tg : 8 - tg);
        const int last = lo > 1 ? lo : 1;
        kfetch(8);
        tile(8, 7, true, 1);
        u32x4 hv[24];
        auto part_done = [&]() {
            if (lane == 0 && __hip_atomic_fetch_add((LAS unsigned*)(lds + L_CNT + 4), 1u, __ATOMIC_RELAXED, __HIP_MEMORY_SCOPE_WORKGROUP) == 5u) set_flag(F_P + (hp * 16 + rt) * 32, tag); };
        if (fetcher) {
            WAITV(0);
            part_done();
            flag_confirm(fk, fkp, tag);
#pragma unroll
            for (int n = 0; n < 24; ++n) { const int pc = 2 * n + (lw >> 1), tl = n >= 12, ar = (pc - 24 * tl) / 6, o = pc - 24 * tl - 6 * ar;
                hv[n] = ld_dev(wsr, (unsigned)(WS_KV + (size_t)(((rt - 2 + tl >= 0 ? rt - 2 + tl : 0) * 16 + hp) * 4 + ar) * 6144) + o * 1024 + lane * 16); }
        }
        if (tg >= 2) tile2(7);
        if (tg & 1) tile(tg == 3 ? 5 : 7, 0, false, 0);
        if (pkw && !fetcher) { WAITV(0); part_done(); }
        feature_map(p[0], phq);
        if (fetcher) {
#pragma unroll
            for (int n = 0; n < 24; ++n) { const int pc = 2 * n + (lw >> 1), tl = n >= 12, ar = (pc - 24 * tl) / 6, o = pc - 24 * tl - 6 * ar;
                *(LAS u32x4*)(lds + L_K + ar * 18432 + tl * 6144 + o * 1024 + lane * 16) = hv[n]; }
        }
        BAR();
        STAMP(7);
        gu32* fpp = summer ? F_P + (hp * 16 + r16) * 32 : F_WO + (cg + 8 * (lane & 31)) * 32;
        kfetch(7 - tg);
        if (rt >= 2) {
            if ((tg & 1) == 0) tile(7 - tg, 6 - tg, false, 0);
            if (tg < 2) tile2(6);
            const unsigned fp = flag_request(fpp);
            tile2(4);
            WAITV(0);
            flag_confirm(fp, fpp, tag);
            if (summer) pv_load(); else wo_pieces(0, 7);
            tile2(2); wo_pieces(7, 14);
            tile(0, 0, false, 2);
        } else {
            if (rt == 1) for (int KT = 7 - tg; KT >= last; --KT) tile(KT, KT - 1, false, 0);
            wait_flag(fpp, tag);
            if (summer) pv_load(); else wo_pieces(0, 14);
        }
    }
    STAMP(8);
    const unsigned obase = (unsigned)(WS_O + ((size_t)(rt * 8 + 2 * tg) * 144 + 9 * hp) * 256);
    const lptr stg = lds + L_OST;
    {
        const int tokb = 32 * tg + q32;
        const float gb = *(const LAS float*)(lds + L_GATE + tokb * 4);
        if (summer) {
            const unsigned one = (r16 < 8 && (r16 & 1) == 0) ? 0x00003f80u : (r16 < 8 ? 0x3f800000u : 0u);
            const bf16x8 idn = mk8((r16 >> 1) == 0 ? one : 0u, (r16 >> 1) == 1 ? one : 0u, (r16 >> 1) == 2 ? one : 0u, (r16 >> 1) == 3 ? one : 0u);
#pragma unroll
            for (int n = 0; n < 3; ++n) {
                f32x4 sm = {0, 0, 0, 0};
#pragma unroll
                for (int m = 0; m < 4; ++m) sm = MFMA16(idn, __builtin_bit_cast(bf16x8, pv[n][m]), sm);
                const int gr = (3 * rw + n) * 16 + r16, ph = gr >= 95, e0 = gr - 95 * ph, d = (e0 * 205) >> 10, u = 2 * (e0 - 5 * d) + g;
                if (gr < 190 && g < 2) *(LAS u32x2*)(lds + L_KVT + ph * 2016 + d * 96 + (u >= 5 ? 48 + (u - 5) * 8 : u * 8)) = u32x2{cvtpk(sm[0], sm[1]), cvtpk(sm[2], sm[3])};
            }
        } else {
            const int e0 = rw * 64 + lane;
            if (e0 < 76) *(LAS u32x2*)(lds + L_KVT + (e0 / 38) * 2016 + ((e0 % 38) >> 1) * 96 + (e0 & 1) * 48 + 40) = u32x2{0u, 0u};
            else if (e0 < 88) *(LAS u32x4*)(lds + L_KVT + ((e0 - 76) / 6) * 2016 + 20 * 96 + ((e0 - 76) % 6) * 16) = u32x4{0, 0, 0, 0};
        }
        auto sw = __builtin_amdgcn_permlane32_swap(__float_as_uint(oacc[10]), __float_as_uint(oacc[10]), false, false);
        const float sb = (1.f - gb) * __builtin_amdgcn_rcpf(__uint_as_float(sw[0]));
        const auto s0 = __builtin_amdgcn_permlane32_swap(cvtpk(oacc[0] * sb, oacc[1] * sb), cvtpk(oacc[4] * sb, oacc[5] * sb), false, false);
        const auto s1 = __builtin_amdgcn_permlane32_swap(cvtpk(oacc[2] * sb, oacc[3] * sb), cvtpk(oacc[6] * sb, oacc[7] * sb), false, false);
        st_dev(wsr, obase + (q32 >> 4) * 36864 + (2 * h + hh) * 256 + (q32 & 15) * 16, u32x4{s0[0], s1[0], s0[1], s1[1]});
        if (hh == 0) *(LAS unsigned*)(stg + tokb * 16 + h * 4) = cvtpk(oacc[8] * sb, oacc[9] * sb);
    }
    BAR();
    STAMP(9);
    {
        f32x4 dd[3];
        const int eo = (r16 >> 2) & 1;
        const bool mine = (r16 >> 3) == (g >> 1);
        const bf16x8 b0 = mk8(phq[0], phq[1], phq[2], phq[3]), b1 = mk8(phq[4], phq[5], phq[6], phq[7]), b2 = mk8(phq[8], 0u, 0u, 0u);
        bf16x8 af[3][3];
#pragma unroll
        for (int i = 0; i < 3; ++i) {
            const int sg = 4 * i + (r16 & 3);
            const int d = !mine ? 20 : i < 2 ? 8 * eo + sg : sg == 8 ? 16 + eo : sg == 10 ? 18 : 20;
            const lptr ap = lds + L_KVT + h * 2016 + d * 96 + (g & 1) * 48;
            af[i][0] = ld128(ap); af[i][1] = ld128(ap + 16); af[i][2] = ld128(ap + 32);
        }
        const int tok = 32 * tg + 16 * (g >> 1) + r16;
        const float gate = *(const LAS float*)(lds + L_GATE + tok * 4);
        SB();
#pragma unroll
        for (int i = 0; i < 3; ++i) { f32x4 a = MFMA16(af[i][0], b0, (f32x4{0, 0, 0, 0})); a = MFMA16(af[i][1], b1, a); dd[i] = MFMA16(af[i][2], b2, a); }
        const float sc = gate * __builtin_amdgcn_rcpf(dd[2][2] + 1e-6f);
        st_dev(wsr, obase + (g >> 1) * 36864 + (4 + 2 * h + (g & 1)) * 256 + r16 * 16,
               u32x4{cvtpk(dd[0][0] * sc, dd[0][1] * sc), cvtpk(dd[0][2] * sc, dd[0][3] * sc), cvtpk(dd[1][0] * sc, dd[1][1] * sc), cvtpk(dd[1][2] * sc, dd[1][3] * sc)});
        *(LAS u16*)(stg + tok * 16 + 8 + h * 4 + (g & 1) * 2) = (u16)cvtpk(dd[2][0] * sc, 0.f);
    }
    BAR();
    if (tid < 128) st_dev(wsr, (unsigned)(WS_O + ((size_t)(rt * 8 + (tid >> 4)) * 144 + 9 * hp + 8) * 256) + (tid & 15) * 16, *(const LAS u32x4*)(stg + tid * 16));
    STAMP(10);
    {
        const int rt3 = r64 >> 1, rh = r64 & 1, kq = wid >> 1, rp = wid & 1;
        auto issue3 = [&](int p) {
            if (wid > 0) {
#pragma unroll
                for (int i = 0; i < 3; ++i) { const int pc = (wid - 1) + 7 * i;
                    if (pc < 18) glds16_dev((unsigned)(pc * 1024 + lane * 16), wot + g3_step(p) * G3_SLOT, lds0 + g3_slot(p) + pc * 1024); }
            }
        };
        issue3(3); issue3(4);
        if (wid == 0) WAITV(0); else if (wid < 5) WAITV(6); else WAITV(4);
        BAR();
        if (tid == 0) set_flag(F_O + (rt * 16 + hp) * 32, tag);
        issue3(5); issue3(6); issue3(7);
        const int wro = kq * 4608 + lane * 16, wr4 = kq * 4608 + 4096 + g * 128 + (r16 & 7) * 16;
        u32x4 w8[5];
        { const unsigned wb8 = (unsigned)(WS_WOT + (size_t)cg * 165888) + g3_step(8) * G3_SLOT;
#pragma unroll
          for (int t = 0; t < 5; ++t) w8[t] = ld_dev(wsr, wb8 + (t < 4 ? wro + t * 1024 : wr4)); }
        STAMP(11);
        if (wid == 0) wait_flag(F_O + (rt3 * 16 + r16) * 32, tag);
        DRAIN_BAR();
        STAMP(12);
        u32x4 of[9][2];
        const unsigned ob = (unsigned)(WS_O + ((size_t)(rt3 * 8 + 4 * rh + 2 * rp) * 144 + 36 * kq) * 256 + lane * 16);
        auto o_load = [&](int p) { const unsigned o = ob + g3_step(p) * 1024; of[p][0] = ld_dev(wsr, o); of[p][1] = ld_dev(wsr, o + 36864); };
#pragma unroll
        for (int p = 0; p < ODEPTH; ++p) o_load(p);
        f32x4 a3[5][2];
#pragma unroll
        for (int t = 0; t < 5; ++t) { a3[t][0] = f32x4{0, 0, 0, 0}; a3[t][1] = f32x4{0, 0, 0, 0}; }
        bf16x8 wf[5];
#pragma unroll
        for (int t = 0; t < 4; ++t) wf[t] = ld128(lds + g3_slot(0) + wro + t * 1024);
        wf[4] = ld128(lds + g3_slot(0) + wr4);
#pragma unroll
        for (int p = 0; p < 9; ++p) {
            if (p + ODEPTH < 9) o_load(p + ODEPTH);
            const lptr sp = lds + g3_slot(p + 1 < 8 ? p + 1 : 7);
            const bf16x8 bo0 = __builtin_bit_cast(bf16x8, of[p][0]), bo1 = __builtin_bit_cast(bf16x8, of[p][1]);
            SB();
#pragma unroll
            for (int t = 0; t < 5; ++t) {
                bf16x8 nw = wf[t];
                if (p + 1 < 8) nw = ld128(sp + (t < 4 ? wro + t * 1024 : wr4)); else if (p + 1 == 8) nw = __builtin_bit_cast(bf16x8, w8[t]);
                SB();
                a3[t][0] = MFMA16(wf[t], bo0, a3[t][0]); a3[t][1] = MFMA16(wf[t], bo1, a3[t][1]);
                SB();
                wf[t] = nw;
            }
        }
        BAR();
        STAMP(13);
        const lptr rp0 = lds + rp * 40960 + lane * 16;
        char* orow = (char*)out + (size_t)(rt3 * 128 + 64 * rh + 32 * rp + r16) * 1152 + cg * 144 + g * 8;
        auto finish = [&](auto KQ) {
            constexpr int q = decltype(KQ)::value;
#pragma unroll
            for (int n = 0; n < 10; ++n) if ((n & 3) != q) *(LAS f32x4*)(rp0 + (n * 4 + q) * 1024) = a3[n % 5][n / 5];
            BAR();
            f32x4 pr[3][3];
#pragma unroll
            for (int j = 0; j < 3; ++j) if (q + 4 * j < 10) {
#pragma unroll
                for (int o = 0; o < 3; ++o) pr[j][o] = *(const LAS f32x4*)(rp0 + ((q + 4 * j) * 4 + ((q + 1 + o) & 3)) * 1024); }
#pragma unroll
            for (int j = 0; j < 3; ++j) if (q + 4 * j < 10) { const int n = q + 4 * j, t = n % 5, tt = n / 5;
                const f32x4 u = (a3[t][tt] + pr[j][0]) + (pr[j][1] + pr[j][2]);
                if (t < 4 || g < 2) *(u32x2*)(orow + tt * 16 * 1152 + t * 32) = u32x2{cvtpk(u[0], u[1]), cvtpk(u[2], u[3])};
            }
        };
        if (kq == 0) finish(IC<0>{}); else if (kq == 1) finish(IC<1>{}); else if (kq == 2) finish(IC<2>{}); else finish(IC<3>{});
        STAMP(14);
        asm volatile("fused_code_end:");
        if (b == 0 && tid == 0) set_flag((gu32*)(ws + WS_EPOCH), tag);
    }
}

extern "C" void kernel_launch(void* const* d_in, const int* in_sizes, int n_in, void* d_out, int out_size, void* d_ws, size_t ws_size, hipStream_t stream) {
    (void)hipFuncSetAttribute((const void*)fused_kernel, hipFuncAttributeMaxDynamicSharedMemorySize, LDS_BYTES);
    fused_kernel<<<256, 512, LDS_BYTES, stream>>>((const char*)d_in[0], (const char*)d_in[1], (const char*)d_in[5], (const char*)d_in[2], (const char*)d_in[3],
                                                  (const char*)d_in[6], (const char*)d_in[7], (char*)d_ws, (u16*)d_out);
}
```

```cpp
#include <hip/hip_runtime.h>
#include <hip/hip_bf16.h>
#include <stdint.h>

typedef unsigned short u16;
typedef short bf16x8 __attribute__((ext_vector_type(8)));
typedef short s16x4 __attribute__((ext_vector_type(4)));
typedef float f32x4 __attribute__((ext_vector_type(4)));
typedef float f32x16 __attribute__((ext_vector_type(16)));
typedef unsigned u32x4 __attribute__((ext_vector_type(4)));
typedef unsigned u32x2 __attribute__((ext_vector_type(2)));
typedef __bf16 bf16x2_t __attribute__((ext_vector_type(2)));
typedef float f32x2 __attribute__((ext_vector_type(2)));
#define LAS __attribute__((address_space(3)))
#define GAS __attribute__((address_space(1)))
typedef LAS char* lptr;
typedef GAS unsigned gu32;

constexpr size_t WS_EPOCH = 0;
constexpr size_t WS_FLAG = 4096;
constexpr size_t WS_WT   = 4096 + 5 * 256 * 128;
constexpr size_t WS_WOT  = WS_WT + 16 * 9 * 28672;
constexpr size_t WS_KV   = WS_WOT + 8 * 165888;
constexpr size_t WS_PART = WS_KV + 256 * 24576;
constexpr size_t WS_O    = WS_PART + 256 * 6144;
constexpr size_t WS_DBG  = WS_O + 16 * 8 * 36864;

constexpr int XS = 16384, WS1 = 28672;
constexpr int L_X = 0, L_W = 3 * XS;
constexpr int L_WP = L_W + 2 * WS1;
constexpr int L_GW = L_WP + 23 * 1024;
constexpr int L_PHI = L_W + 3 * WS1;
constexpr int L_ZERO = L_PHI + 1792;
constexpr int L_WOP = L_PHI + 2048;
constexpr int L_PKX = 118784;
constexpr int L_CNT = 152064;
constexpr int L_K = 0;
constexpr int L_Q = 73728;
constexpr int L_VA = 86016;
constexpr int L_PK = 98304;
constexpr int L_GATE = 147456;
constexpr int L_KVT = 147968;
constexpr int L_OST = 73728;
constexpr int LDS_BYTES = 160 * 1024;
#define ODEPTH 5
constexpr int G3_SLOT = 18432;
__device__ __forceinline__ constexpr int g3_slot(int p) { return (p < 3 ? 5 + p : p - 3) * G3_SLOT; }
constexpr unsigned SPIN_CAP = 1u << 18;
constexpr float LOG2E = 1.4426950408889634f;
constexpr float QSCALE = 0.23570226039551584f * LOG2E;
constexpr float THR = 8.f;
constexpr float NEGBIG = -1e30f;

__device__ __forceinline__ unsigned cvtpk(float lo, float hi) { const f32x2 v = {lo, hi}; return __builtin_bit_cast(unsigned, __builtin_convertvector(v, bf16x2_t)); }
__device__ __forceinline__ void glds16(unsigned voff, const void* sbase, unsigned lds_addr) {
    unsigned sv;
    asm volatile("s_mov_b32 %0, m0\n\ts_mov_b32 m0, %3\n\ts_nop 0\n\tglobal_load_lds_dwordx4 %1, %2\n\ts_mov_b32 m0, %0" : "=&s"(sv) : "v"(voff), "s"(sbase), "s"(lds_addr) : "memory");
}
__device__ __forceinline__ void glds16_dev(unsigned voff, const void* sbase, unsigned lds_addr) {
    unsigned sv;
    asm volatile("s_mov_b32 %0, m0\n\ts_mov_b32 m0, %3\n\ts_nop 0\n\tglobal_load_lds_dwordx4 %1, %2 sc1\n\ts_mov_b32 m0, %0" : "=&s"(sv) : "v"(voff), "s"(sbase), "s"(lds_addr) : "memory");
}
__device__ __forceinline__ void glds16_ptr(const void* g, unsigned lds_addr) {
    unsigned sv;
    asm volatile("s_mov_b32 %0, m0\n\ts_mov_b32 m0, %2\n\ts_nop 0\n\tglobal_load_lds_dwordx4 %1, off\n\ts_mov_b32 m0, %0" : "=&s"(sv) : "v"(g), "s"(lds_addr) : "memory");
}
#define WAITV(n) asm volatile("s_waitcnt vmcnt(" #n ")" ::: "memory")
#define WAITL() asm volatile("s_waitcnt lgkmcnt(0)" ::: "memory")
#define LGKM0() do { __builtin_amdgcn_s_waitcnt(0xC07F); asm volatile("" ::: "memory"); } while (0)
#define BAR() do { asm volatile("" ::: "memory"); __builtin_amdgcn_s_waitcnt(0xC07F); __builtin_amdgcn_s_barrier(); asm volatile("" ::: "memory"); } while (0)
#define DRAIN_BAR() do { asm volatile("s_waitcnt vmcnt(0)" ::: "memory"); BAR(); } while (0)
#define SB() __builtin_amdgcn_sched_barrier(0)
#define MFMA16(a, b, c) __builtin_amdgcn_mfma_f32_16x16x32_bf16(a, b, c, 0, 0, 0)
#define MFMA32(a, b, c) __builtin_amdgcn_mfma_f32_32x32x16_bf16(a, b, c, 0, 0, 0)
__device__ __forceinline__ bf16x8 ld128(lptr p) { return *(const LAS bf16x8*)p; }
__device__ __forceinline__ s16x4 trrd(lptr p) { typedef short v4i16_t __attribute__((ext_vector_type(4))); return __builtin_bit_cast(s16x4, __builtin_amdgcn_ds_read_tr16_b64_v4i16((LAS v4i16_t*)p)); }
__device__ __forceinline__ bf16x8 cat8(s16x4 a, s16x4 b) { return __builtin_shufflevector(a, b, 0, 1, 2, 3, 4, 5, 6, 7); }
__device__ __forceinline__ bf16x8 mk8(unsigned a, unsigned b, unsigned c, unsigned d) { u32x4 w = {a, b, c, d}; return __builtin_bit_cast(bf16x8, w); }
__device__ __forceinline__ float vmax2(unsigned a, unsigned b) { float r; asm("v_max_f32 %0, %1, %2" : "=v"(r) : "v"(a), "v"(b)); return r; }
__device__ __forceinline__ float x16max(float x) { auto s = __builtin_amdgcn_permlane16_swap(__float_as_uint(x), __float_as_uint(x), false, false); return vmax2(s[0], s[1]); }
__device__ __forceinline__ float x16sum(float x) { auto s = __builtin_amdgcn_permlane16_swap(__float_as_uint(x), __float_as_uint(x), false, false); return __uint_as_float(s[0]) + __uint_as_float(s[1]); }
__device__ __forceinline__ float x32max(float x) { auto s = __builtin_amdgcn_permlane32_swap(__float_as_uint(x), __float_as_uint(x), false, false); return vmax2(s[0], s[1]); }
__device__ __forceinline__ void st_dev(const __amdgpu_buffer_rsrc_t rs, unsigned off, u32x4 v) { __builtin_amdgcn_raw_buffer_store_b128(v, rs, off, 0, 16); }
__device__ __forceinline__ u32x4 ld_dev(const __amdgpu_buffer_rsrc_t rs, unsigned off) { return __builtin_amdgcn_raw_buffer_load_b128(rs, off, 0, 16); }
__device__ __forceinline__ void wait_flag(gu32* p, unsigned tag) {
    for (unsigned spins = 0; spins < SPIN_CAP; ++spins) {
        const unsigned v = __hip_atomic_load(p, __ATOMIC_RELAXED, __HIP_MEMORY_SCOPE_AGENT);
        if (__all(v == tag)) break;
        __builtin_amdgcn_s_sleep(1);
    }
}
__device__ __forceinline__ void set_flag(gu32* p, unsigned tag) { __hip_atomic_store(p, tag, __ATOMIC_RELAXED, __HIP_MEMORY_SCOPE_AGENT); }
__device__ __forceinline__ u32x4 gather8(lptr p, int stride) {
    unsigned w[4];
#pragma unroll
    for (int j = 0; j < 4; ++j) w[j] = (unsigned)*(const LAS u16*)(p + 2 * j * stride) | ((unsigned)*(const LAS u16*)(p + (2 * j + 1) * stride) << 16);
    return u32x4{w[0], w[1], w[2], w[3]};
}
template <int V> struct IC { static constexpr int value = V; };
__device__ __forceinline__ int piece_start(int j) { return (9 * j + 1) >> 1; }

__device__ __forceinline__ unsigned flag_request(gu32* p) { unsigned v; asm volatile("global_load_dword %0, %1, off sc1" : "=v"(v) : "v"(p) : "memory"); return v; }
__device__ __forceinline__ void flag_confirm(unsigned v, gu32* p, unsigned tag) {
    asm volatile("" : "+v"(v));
    if (!__all(v == tag)) wait_flag(p, tag);
}

#define STAMP(i) do {} while (0)
#define WSTAMP(i) do {} while (0)

__device__ __forceinline__ void feature_map(const float (&p)[9], unsigned (&o)[9]) {
    float mx = fmaxf(fmaxf(p[0], p[1]), p[2]), mn = fminf(fminf(p[0], p[1]), p[2]);
#pragma unroll
    for (int i = 3; i < 9; i += 2) { mx = fmaxf(fmaxf(mx, p[i]), p[i + 1]); mn = fminf(fminf(mn, p[i]), p[i + 1]); }
    mx = x16max(mx); mn = -x16max(-mn);
    const float mxl = -mx * LOG2E, mnl = mn * LOG2E;
    float a[9], b[9], sa = 0.f, sb = 0.f;
#pragma unroll
    for (int i = 0; i < 9; ++i) { a[i] = __builtin_amdgcn_exp2f(__builtin_fmaf(p[i], LOG2E, mxl)); b[i] = __builtin_amdgcn_exp2f(__builtin_fmaf(p[i], -LOG2E, mnl)); sa += a[i]; sb += b[i]; }
    sa = x16sum(sa); sb = x16sum(sb);
    const float ra = __builtin_amdgcn_rcpf(sa), rb = __builtin_amdgcn_rcpf(sb);
#pragma unroll
    for (int i = 0; i < 9; ++i) { a[i] *= ra; b[i] *= rb; }
    o[0] = cvtpk(a[0], a[1]); o[1] = cvtpk(a[2], a[3]); o[2] = cvtpk(a[4], a[5]); o[3] = cvtpk(a[6], a[7]); o[4] = cvtpk(a[8], b[0]);
    o[5] = cvtpk(b[1], b[2]); o[6] = cvtpk(b[3], b[4]); o[7] = cvtpk(b[5], b[6]); o[8] = cvtpk(b[7], b[8]);
}

__global__ __launch_bounds__(512, 2) void fused_kernel(const char* __restrict__ x, const char* __restrict__ wA, const char* __restrict__ wB, const char* __restrict__ wphi,
                                                       const char* __restrict__ woA, const char* __restrict__ woB, const char* __restrict__ gw,
                                                       char* __restrict__ ws, u16* __restrict__ out) {
    asm volatile("" :: "s"(x), "s"(wA), "s"(wB), "s"(wphi), "s"(woA), "s"(woB), "s"(gw), "s"(ws), "s"(out));
    extern __shared__ __attribute__((aligned(16))) char lds_raw[];
    const lptr lds = (lptr)lds_raw;
    const unsigned lds0 = (unsigned)(uintptr_t)lds_raw;
    const int tid = threadIdx.x, lane = tid & 63, wid = __builtin_amdgcn_readfirstlane(tid >> 6);
    const int b = blockIdx.x, hp = (b & 3) * 4 + ((b >> 3) & 3), rt = ((b >> 2) & 1) * 8 + (b >> 5);
    const bool loader = wid >= 4;
    const int lw = wid & 3;
    const int h = lw >> 1, tg = loader ? 3 - 2 * (lw & 1) : 2 * (lw & 1);
    const int r16 = lane & 15, g = lane >> 4;
    gu32* flags = (gu32*)(ws + WS_FLAG);
    gu32* F_W = flags;
    gu32* F_WO = flags + 256 * 32;
    gu32* F_KV = flags + 512 * 32;
    gu32* F_P = flags + 768 * 32;
    gu32* F_O = flags + 1024 * 32;
    {
        unsigned long long pc0; unsigned len;
        asm volatile("s_getpc_b64 %0\nfused_code_begin:" : "=s"(pc0));
        asm volatile("s_mov_b32 %0, fused_code_end-fused_code_begin" : "=s"(len));
        if (wid == 3) {
            const unsigned j = blockIdx.x >> 3;
            if ((j + 1) * 1024 <= len) glds16(j * 1024 + lane * 16, (const void*)pc0, lds0 + 160256);
            if ((j + 33) * 1024 <= len) glds16((j + 32) * 1024 + lane * 16, (const void*)pc0, lds0 + 160256);
        }
    }
    STAMP(0);
    unsigned tagv = flag_request((gu32*)(ws + WS_EPOCH));
    const __amdgpu_buffer_rsrc_t wsr = __builtin_amdgcn_make_buffer_rsrc(ws, 0, 0x2000000, 0x00020000);

    const int G0 = piece_start(rt), nG = piece_start(rt + 1) - G0;
    const int j2 = b >> 3, cgp = b & 7, C0 = piece_start(j2), nC = piece_start(j2 + 1) - C0;
#pragma unroll
    for (int i = 0; i < 3; ++i) {
        const int pc = wid + 8 * i;
        if (pc < 23) { int c = pc * 64 + lane; if (c >= nG * 288) c = 0;
            const int row = c / 36, ch = c % 36, w = ch / 3, cc = ch % 3, sg = w >> 1;
            const char* src = (sg < 3 ? wA + sg * 1152 : wB + (sg - 3) * 1152) + (size_t)(8 * G0 + row) * 3456 + (2 * hp + (w & 1)) * 36 + (cc == 2 ? 20 : cc * 16);
            glds16_ptr(src, lds0 + L_WP + pc * 1024); }
        else glds16(G0 * 16 + (lane < nG ? lane : 0) * 16, gw, lds0 + L_GW);
    }
    const int krot = (2 * (hp & 3) + (rt & 7)) % 9;
    auto kact = [&](int kb) { const int k = kb + krot; return k >= 9 ? k - 9 : k; };
    unsigned xo[4];
    auto issue_x = [&](int kb, int slot) {
        const char* src = x + kact(kb) * 128;
#pragma unroll
        for (int i = 0; i < 4; ++i) glds16(xo[i], src, lds0 + L_X + slot * XS + lw * 4096 + i * 1024);
    };
    if (loader) {
#pragma unroll
        for (int i = 0; i < 4; ++i) { const int row = 32 * lw + 8 * i + (lane >> 3); xo[i] = (unsigned)(rt * 128 + row) * 1152u + (unsigned)(((lane & 7) ^ ((row >> 1) & 7)) << 4); }
        issue_x(0, 0);
        WAITV(4);
    } else WAITV(0);
    BAR();
    asm volatile("" : "+v"(tagv));
    const unsigned tag = __builtin_amdgcn_readfirstlane(tagv) + 1u;
    STAMP(1);
    {
        const int q4 = r16 >> 2, p4 = r16 & 3;
        const unsigned wtb = (unsigned)(WS_WT + (size_t)hp * 9 * 28672);
        u32x4 v[3]; unsigned off[3];
#pragma unroll
        for (int i = 0; i < 2; ++i) {
            const int U0 = i * 32 + wid * 4 + g, U = U0 < nG * 12 ? U0 : 0, Gi = (U * 43) >> 9, ht = U - 12 * Gi, hh2 = ht >= 6, t = ht - 6 * hh2;
            const lptr p = lds + L_WP + (8 * Gi + q4) * 576 + (((0x541032 >> (4 * t)) & 7) * 2 + hh2) * 48 + p4 * 8;
            v[i] = __builtin_bit_cast(u32x4, cat8(trrd(p), trrd(p + 4 * 576)));
            const int G = G0 + Gi;
            off[i] = U0 < nG * 12 ? wtb + (G >> 3) * 28672 + ((hh2 * 7 + t) * 2 + ((G >> 2) & 1)) * 1024 + ((G & 3) * 16 + r16) * 16 : 0xfffffff0u;
        }
        {
            const int U0 = wid * 4 + g, U = U0 < nG * 2 ? U0 : 0, Gi = U >> 1, hh2 = U & 1;
            const u32x4 gt = gather8(lds + L_WP + 8 * Gi * 576 + ((r16 < 12 ? r16 >> 1 : 0) * 2 + hh2) * 48 + 44 + 2 * (r16 & 1), 576);
            const u32x4 gv = *(const LAS u32x4*)(lds + L_GW + Gi * 16);
            v[2] = r16 < 12 ? gt : r16 == 12 ? gv : u32x4{0, 0, 0, 0};
            const int G = G0 + Gi;
            off[2] = U0 < nG * 2 ? wtb + (G >> 3) * 28672 + ((hh2 * 7 + 6) * 2 + ((G >> 2) & 1)) * 1024 + ((G & 3) * 16 + r16) * 16 : 0xfffffff0u;
        }
#pragma unroll
        for (int i = 0; i < 3; ++i) st_dev(wsr, off[i], v[i]);
    }
    if (loader) { issue_x(1, 1); issue_x(2, 2); WAITV(8); } else WAITV(0);
    BAR();
    if (tid == 0) { set_flag(F_W + (hp * 16 + rt) * 32, tag); *(LAS u32x2*)(lds + L_CNT) = u32x2{0u, 0u}; }
    STAMP(2);
    const char* wt = ws + WS_WT + (size_t)hp * 9 * 28672;
    auto issue_w = [&](int kb, int slot) {
#pragma unroll
        for (int i = 0; i < 7; ++i) glds16_dev((unsigned)(lw * 1024 + lane * 16), wt + kact(kb) * 28672 + i * 4096, lds0 + L_W + slot * WS1 + i * 4096 + lw * 1024);
    };
    if (loader) {
#pragma unroll
        for (int i = 0; i < 2; ++i) { const int pc = lw + 4 * i;
            if (pc < 6) { int c = pc * 64 + lane; if (c >= nC * 72) c = 0;
                const int row = c / 9, cc = c % 9, C = C0 + (row >> 3), hq = C / 9, p = 8 * (C % 9) + (row & 7);
                int br, head, d;
                if (p < 64) { br = p < 32; head = (p >> 4) & 1; d = p & 15; } else { br = p < 68; head = (p >> 1) & 1; d = 16 + (p & 1); }
                glds16_ptr((br ? woB : woA) + ((2 * hq + head) * 18 + d) * 1152 + cgp * 144 + cc * 16, lds0 + L_WOP + pc * 1024); }
            else { int c = (pc - 6) * 64 + lane; if (c >= 108) c = 0; const int row = c / 3, cc = c % 3;
                glds16((unsigned)(hp * 1296 + row * 36 + (cc == 2 ? 20 : cc * 16)), wphi, lds0 + L_PHI + (pc - 6) * 1024); } }
    } else if (wid == 0) wait_flag(F_W + (hp * 16 + r16) * 32, tag);
    BAR();
    STAMP(3);
    u32x4 pkt[2][2];
    if (!loader) {
        f32x4 acc[7][4];
#pragma unroll
        for (int t = 0; t < 7; ++t)
#pragma unroll
            for (int u = 0; u < 4; ++u) acc[t][u] = f32x4{0, 0, 0, 0};
        const int th = lw & 1;
        const int xr = L_X + (64 * th + r16) * 128 + ((g ^ (r16 >> 1)) << 4), wr = L_W + h * 14336 + lane * 16;
        bf16x8 wa[7], xa[4], wb[7], xb[4];
        BAR();
#pragma unroll
        for (int u = 0; u < 4; ++u) xa[u] = ld128(lds + xr + u * 2048);
#pragma unroll
        for (int t = 0; t < 7; ++t) wa[t] = ld128(lds + wr + t * 2048);
        int slot = 0;
        for (int i = 0; i < 8; ++i) {
            const lptr sx = lds + slot * XS, sw = lds + slot * WS1;
            SB();
#pragma unroll
            for (int u = 0; u < 4; ++u) xb[u] = ld128(sx + (xr ^ 64) + u * 2048);
#pragma unroll
            for (int t = 0; t < 7; ++t) { wb[t] = ld128(sw + wr + t * 2048 + 1024); SB();
#pragma unroll
                for (int u = 0; u < 4; ++u) acc[t][u] = MFMA16(wa[t], xa[u], acc[t][u]);
                SB(); }
            BAR();
            slot = slot == 2 ? 0 : slot + 1;
            const lptr nx = lds + slot * XS, nw = lds + slot * WS1;
            SB();
#pragma unroll
            for (int u = 0; u < 4; ++u) xa[u] = ld128(nx + xr + u * 2048);
#pragma unroll
            for (int t = 0; t < 7; ++t) { const bf16x8 n = ld128(nw + wr + t * 2048); SB();
#pragma unroll
                for (int u = 0; u < 4; ++u) acc[t][u] = MFMA16(wb[t], xb[u], acc[t][u]);
                SB(); wa[t] = n; }
        }
        {
            const lptr sx = lds + 2 * XS, sw = lds + 2 * WS1;
            SB();
#pragma unroll
            for (int u = 0; u < 4; ++u) xb[u] = ld128(sx + (xr ^ 64) + u * 2048);
#pragma unroll
            for (int t = 0; t < 7; ++t) { wb[t] = ld128(sw + wr + t * 2048 + 1024); SB();
#pragma unroll
                for (int u = 0; u < 4; ++u) acc[t][u] = MFMA16(wa[t], xa[u], acc[t][u]);
                SB(); }
            BAR();
#pragma unroll
            for (int t = 0; t < 7; ++t)
#pragma unroll
                for (int u = 0; u < 4; ++u) acc[t][u] = MFMA16(wb[t], xb[u], acc[t][u]);
        }
        STAMP(4);
        const int row0 = (64 * th + r16) * 48;
        const lptr Kb = lds + L_K + h * 36864 + 256 * 48 + row0, Vb = Kb + 18432, Qb = lds + L_Q + h * 6144 + row0, VAb = lds + L_VA + h * 6144 + row0;
#pragma unroll
        for (int u = 0; u < 4; ++u) {
            *(LAS u32x2*)(Kb + u * 768 + 8 * g) = u32x2{cvtpk(acc[4][u][0], acc[4][u][1]), cvtpk(acc[4][u][2], acc[4][u][3])};
            *(LAS u32x2*)(Vb + u * 768 + 8 * g) = u32x2{cvtpk(acc[5][u][0], acc[5][u][1]), cvtpk(acc[5][u][2], acc[5][u][3])};
        }
        if (g == 2) {
#pragma unroll
            for (int u = 0; u < 4; ++u) {
                *(LAS u32x4*)(Kb + u * 768 + 32) = u32x4{cvtpk(acc[6][u][0], acc[6][u][1]), 0u, 0u, 0u};
                *(LAS u32x4*)(Vb + u * 768 + 32) = u32x4{cvtpk(acc[6][u][2], acc[6][u][3]), 0x00003f80u, 0u, 0u};
            }
        }
        unsigned st0[4], st1[4]; float gt[4];
#pragma unroll
        for (int u = 0; u < 4; ++u) {
            const unsigned s0 = cvtpk(acc[6][u][0], acc[6][u][1]), s1 = cvtpk(acc[6][u][2], acc[6][u][3]);
#pragma unroll
            for (int X = 0; X < 2; ++X) {
                const u32x4 tp = {cvtpk(acc[2 + X][u][0], acc[2 + X][u][1]), cvtpk(acc[2 + X][u][2], acc[2 + X][u][3]), s0, s1};
                if (u < 2) pkt[X][u] = tp; else *(LAS u32x4*)(lds + L_PKX + ((lw * 2 + X) * 2 + (u - 2)) * 1024 + lane * 16) = tp;
            }
            *(LAS u32x2*)(VAb + u * 768 + 8 * g) = u32x2{cvtpk(acc[0][u][0], acc[0][u][1]), cvtpk(acc[0][u][2], acc[0][u][3])};
            *(LAS u32x2*)(Qb + u * 768 + 8 * g) = u32x2{cvtpk(acc[1][u][0] * QSCALE, acc[1][u][1] * QSCALE), cvtpk(acc[1][u][2] * QSCALE, acc[1][u][3] * QSCALE)};
            st0[u] = s0; st1[u] = cvtpk(acc[6][u][2] * QSCALE, acc[6][u][3] * QSCALE);
            gt[u] = __builtin_amdgcn_rcpf(1.f + __builtin_amdgcn_exp2f(-acc[6][u][0] * LOG2E));
        }
        if (g == 1) {
#pragma unroll
            for (int u = 0; u < 4; ++u) { *(LAS u32x2*)(VAb + u * 768 + 32) = u32x2{st0[u], 0x00003f80u}; *(LAS unsigned*)(Qb + u * 768 + 32) = st1[u]; }
        } else if (g == 3 && h == 0) {
#pragma unroll
            for (int u = 0; u < 4; ++u) *(LAS float*)(lds + L_GATE + (64 * th + 16 * u + r16) * 4) = gt[u];
        }
    } else {
        issue_w(0, 0); issue_w(1, 1); issue_w(2, 2);
        WAITV(14);
        BAR();
        for (int i = 0; i < 8; ++i) {
            if (i == 0) WAITV(7); else if (i < 7) WAITV(11); else WAITV(1);
            if (i == 1 && lw == 0 && lane < 4) *(LAS u32x4*)(lds + L_ZERO + lane * 16) = u32x4{0, 0, 0, 0};
            BAR();
            if (i < 6) { issue_x(i + 3, i % 3); issue_w(i + 3, i % 3); }
            if (i == 6 || i == 7) {
                const int q4 = r16 >> 2, p4 = r16 & 3;
                const int U0 = (i - 6) * 16 + lw * 4 + g, U = U0 < nC * 5 ? U0 : 0, Ci = (U * 52) >> 8, t = U - 5 * Ci, C = C0 + Ci, kh = C >= 72, s = (C - 72 * kh) >> 2, gg = C & 3;
                const lptr p = lds + L_WOP + (8 * Ci + q4) * 144 + (t < 4 ? t * 32 + p4 * 8 : 128 + (p4 & 1) * 8);
                const u32x4 v = __builtin_bit_cast(u32x4, cat8(trrd(p), trrd(p + 4 * 144)));
                const unsigned off = (unsigned)(WS_WOT + (size_t)cgp * 165888) + ((s >> 1) * 4 + kh * 2 + (s & 1)) * 4608 + (t < 4 ? t * 1024 + (gg * 16 + r16) * 16 : 4096 + (gg * 8 + r16) * 16);
                st_dev(wsr, U0 < nC * 5 && (t < 4 || r16 < 8) ? off : 0xfffffff0u, v);
            }
        }
        BAR();
    }
    BAR();
    STAMP(5);
    const int cg = (b >> 3) & 7, r64 = 4 * (b & 7) + (b >> 6);
    if (loader) {
#pragma unroll
        for (int X = 0; X < 2; ++X)
#pragma unroll
            for (int u = 0; u < 2; ++u) pkt[X][u] = *(const LAS u32x4*)(lds + L_PKX + (((lw ^ 1) * 2 + X) * 2 + u) * 1024 + lane * 16);
    } else {
        u32x4 kvv[6];
        const lptr kr = lds + L_K + h * 36864 + (256 + 64 * (lw & 1)) * 48 + lane * 16;
#pragma unroll
        for (int i = 0; i < 3; ++i) { kvv[i] = *(const LAS u32x4*)(kr + i * 1024); kvv[3 + i] = *(const LAS u32x4*)(kr + 18432 + i * 1024); }
        const unsigned ko = (unsigned)(WS_KV + (size_t)((rt * 16 + hp) * 4 + h * 2) * 6144) + (lw & 1) * 3072 + lane * 16;
#pragma unroll
        for (int i = 0; i < 3; ++i) { st_dev(wsr, ko + i * 1024, kvv[i]); st_dev(wsr, ko + 6144 + i * 1024, kvv[3 + i]); }
    }
    unsigned phq[9];
    float p[2][9];
    {
        unsigned phk[9];
        const int q4 = r16 >> 2, p4 = r16 & 3;
        const unsigned pw0 = lds0 + L_PHI + h * 864, zp = lds0 + L_ZERO;
        s16x4 R1[2][3], R2[2][2][3];
#pragma unroll
        for (int hf = 0; hf < 2; ++hf) {
            const bool ok = (p4 >> 1) == hf;
            const lptr a = (lptr)(uintptr_t)(ok ? pw0 + (4 * g + q4) * 48 + 16 * (p4 & 1) : zp), a2 = (lptr)(uintptr_t)(ok ? pw0 + (4 * g + q4) * 48 + 40 : zp);
            R1[hf][0] = trrd(a); R1[hf][1] = trrd(a + 8); R1[hf][2] = trrd(a2);
#pragma unroll
            for (int X = 0; X < 2; ++X) {
                const bool ok2 = ok && g == 0 && (q4 >> 1) == X;
                const lptr c = (lptr)(uintptr_t)(ok2 ? pw0 + (16 + (q4 & 1)) * 48 + 16 * (p4 & 1) : zp), c2 = (lptr)(uintptr_t)(ok2 ? pw0 + (16 + (q4 & 1)) * 48 + 40 : zp);
                R2[X][hf][0] = trrd(c); R2[X][hf][1] = trrd(c + 8); R2[X][hf][2] = trrd(c2);
            }
        }
        SB();
#pragma unroll
        for (int X = 0; X < 2; ++X) {
            const bf16x8 bA = __builtin_bit_cast(bf16x8, pkt[X][0]), bB = __builtin_bit_cast(bf16x8, pkt[X][1]);
            f32x4 d[3];
#pragma unroll
            for (int i = 0; i < 3; ++i) d[i] = MFMA16(cat8(R1[0][i], R2[X][0][i]), bA, (f32x4{0, 0, 0, 0}));
#pragma unroll
            for (int i = 0; i < 3; ++i) d[i] = MFMA16(cat8(R1[1][i], R2[X][1][i]), bB, d[i]);
#pragma unroll
            for (int j = 0; j < 4; ++j) { p[X][j] = d[0][j]; p[X][4 + j] = d[1][j]; }
            p[X][8] = (g & 1) ? d[2][3] : d[2][2];
        }
        feature_map(p[1], phk);
        const lptr pkp = lds + L_PK + h * 10240 + (32 * tg + 16 * (g >> 1) + r16) * 80 + (g & 1) * 40;
        *(LAS u32x2*)(pkp) = u32x2{phk[0], phk[1]}; *(LAS u32x2*)(pkp + 8) = u32x2{phk[2], phk[3]}; *(LAS u32x2*)(pkp + 16) = u32x2{phk[4], phk[5]};
        *(LAS u32x2*)(pkp + 24) = u32x2{phk[6], phk[7]}; *(LAS u32x2*)(pkp + 32) = u32x2{phk[8], 0u};
        WAITV(0);
        if (!loader && lane == 0 && __hip_atomic_fetch_add((LAS unsigned*)(lds + L_CNT), 1u, __ATOMIC_RELAXED, __HIP_MEMORY_SCOPE_WORKGROUP) == 3u) set_flag(F_KV + (hp * 16 + rt) * 32, tag);
    }
    BAR();
    if (tid == 0) set_flag(F_WO + b * 32, tag);
    STAMP(6);
    const bool fetcher = !loader && (lw & 1) == 0 && rt > 0;
    gu32* fkp = F_KV + (hp * 16 + rt - 1 - (rt > 1 ? (lane & 1) : 0)) * 32;
    unsigned fk = 0;
    if (fetcher) fk = flag_request(fkp);
    const bool pkw = wid != 4 && wid != 6;
    if (pkw) {
        const int w6 = wid < 4 ? wid : wid == 5 ? 4 : 5, hh = w6 / 3, mt = w6 % 3, q4 = r16 >> 2, p4 = r16 & 3;
        int u = 4 * mt + p4; if (u > 9) u = 9;
        const lptr pa = lds + L_PK + hh * 10240 + (8 * g + q4) * 80 + u * 8;
        const lptr pb0 = lds + L_VA + hh * 6144 + (8 * g + q4) * 48 + p4 * 8;
        const lptr pb1 = lds + L_VA + hh * 6144 + (8 * g + q4) * 48 + 32;
        f32x4 d0 = {0, 0, 0, 0}, d1 = {0, 0, 0, 0};
        bf16x8 a[4], b0[4], b1[4];
#pragma unroll
        for (int ks = 0; ks < 4; ++ks) {
            a[ks] = cat8(trrd(pa + ks * 32 * 80), trrd(pa + ks * 32 * 80 + 4 * 80));
            b0[ks] = cat8(trrd(pb0 + ks * 32 * 48), trrd(pb0 + ks * 32 * 48 + 4 * 48));
            b1[ks] = cat8(trrd(pb1 + ks * 32 * 48), trrd(pb1 + ks * 32 * 48 + 4 * 48));
        }
        SB();
#pragma unroll
        for (int ks = 0; ks < 4; ++ks) { d0 = MFMA16(a[ks], b0[ks], d0); d1 = MFMA16(a[ks], b1[ks], d1); }
        const int uu = 4 * mt + g;
        const unsigned po = (unsigned)(WS_PART + (size_t)(hp * 16 + rt) * 3072 + hh * 1520);
        if (uu < 10) {
            __builtin_amdgcn_raw_buffer_store_b64(u32x2{cvtpk(d0[0], d0[1]), cvtpk(d0[2], d0[3])}, wsr, po + (r16 * 10 + uu) * 8, 0, 16);
            if (r16 < 3) __builtin_amdgcn_raw_buffer_store_b64(u32x2{cvtpk(d1[0], d1[1]), cvtpk(d1[2], d1[3])}, wsr, po + ((16 + r16) * 10 + uu) * 8, 0, 16);
        }
    }
    const int q32 = lane & 31, hh = lane >> 5;
    const char* wot = ws + WS_WOT + (size_t)cg * 165888;
    auto g3_step = [&](int p) { const int s = p + cg; return s >= 9 ? s - 9 : s; };
    f32x16 oacc;
    const bool summer = tg < 2;
    const int rw = 2 * h + (tg & 1);
    u32x4 pv[3][4];
    auto pv_load = [&]() {
#pragma unroll
        for (int n = 0; n < 3; ++n) { const int gr = (3 * rw + n) * 16 + r16;
#pragma unroll
            for (int m = 0; m < 4; ++m) pv[n][m] = ld_dev(wsr, (unsigned)(WS_PART + (size_t)(hp * 16 + 4 * m + g) * 3072) + (gr < 190 ? gr : 0) * 16); }
    };
    {
        const lptr Kb = lds + L_K + h * 36864, Vb = Kb + 18432, Qb = lds + L_Q + h * 6144;
        const bf16x8 qf0 = ld128(Qb + (32 * tg + q32) * 48 + 16 * hh);
        const unsigned q16 = *(const LAS unsigned*)(Qb + (32 * tg + q32) * 48 + 32);
        const bf16x8 qf1 = mk8(hh ? 0u : q16, 0u, 0u, 0u);
        const lptr kp = Kb + (32 * tg + q32) * 48;
        const int i16 = lane & 15, dhalf = (lane >> 4) & 1;
        const lptr vp = Vb + (32 * tg + 4 * hh + (i16 >> 2)) * 48 + (dhalf ? 32 + ((i16 & 3) ? 8 : 0) : (i16 & 3) * 8);
        f32x16 negm;
#pragma unroll
        for (int r = 0; r < 16; ++r) { oacc[r] = 0.f; negm[r] = 0.f; }
        bf16x8 k0, k1, k2, k3;
        auto kfetch = [&](int KN) { const int KM = KN > 0 ? KN - 1 : 0;
            k0 = ld128(kp + KN * 32 * 48 + 16 * hh); k1 = ld128(kp + KN * 32 * 48 + 32); k2 = ld128(kp + KM * 32 * 48 + 16 * hh); k3 = ld128(kp + KM * 32 * 48 + 32); };
        auto vfrag = [&](int KT, int hf) { return cat8(trrd(vp + (KT * 32 + 16 * hf) * 48), trrd(vp + (KT * 32 + 16 * hf + 8) * 48)); };
        auto rowmax = [&](const f32x16& s) { float m = fmaxf(fmaxf(s[0], s[1]), s[2]);
#pragma unroll
            for (int r = 3; r < 15; r += 2) m = fmaxf(fmaxf(m, s[r]), s[r + 1]);
            return fmaxf(m, s[15]); };
        auto pack = [&](const f32x16& s, int hf) { return mk8(cvtpk(__builtin_amdgcn_exp2f(s[8 * hf]), __builtin_amdgcn_exp2f(s[8 * hf + 1])), cvtpk(__builtin_amdgcn_exp2f(s[8 * hf + 2]), __builtin_amdgcn_exp2f(s[8 * hf + 3])),
                                                             cvtpk(__builtin_amdgcn_exp2f(s[8 * hf + 4]), __builtin_amdgcn_exp2f(s[8 * hf + 5])), cvtpk(__builtin_amdgcn_exp2f(s[8 * hf + 6]), __builtin_amdgcn_exp2f(s[8 * hf + 7]))); };
        auto tile = [&](int KT, int KN, bool first, int mask) {
            SB();
            f32x16 s = MFMA32(k0, qf0, negm); s = MFMA32(k1, qf1, s);
            SB();
            const bf16x8 va = vfrag(KT, 0), vb = vfrag(KT, 1);
            kfetch(KN);
            SB();
            if (mask) {
#pragma unroll
                for (int r = 0; r < 16; ++r) { const int jj = (r & 3) + 8 * (r >> 2) + 4 * hh; const bool ok = mask == 1 ? jj <= q32 : jj > q32; s[r] = ok ? s[r] : NEGBIG; }
            }
            const float m = x32max(rowmax(s));
            if (first || __any(m > THR)) {
                const float dl = first ? m : fmaxf(m, 0.f);
                const float f = first ? 1.f : __builtin_amdgcn_exp2f(-dl);
#pragma unroll
                for (int r = 0; r < 16; ++r) { s[r] -= dl; negm[r] -= dl; if (!first) oacc[r] *= f; }
            }
            oacc = MFMA32(va, pack(s, 0), oacc);
            oacc = MFMA32(vb, pack(s, 1), oacc);
        };
        auto tile2 = [&](int KT) {
            SB();
            f32x16 s = MFMA32(k0, qf0, negm), t = MFMA32(k2, qf0, negm); s = MFMA32(k1, qf1, s); t = MFMA32(k3, qf1, t);
            SB();
            const bf16x8 va = vfrag(KT, 0), vb = vfrag(KT, 1), vc = vfrag(KT - 1, 0), vd = vfrag(KT - 1, 1);
            kfetch(KT >= 2 ? KT - 2 : 0);
            SB();
            const float m = x32max(fmaxf(rowmax(s), rowmax(t)));
            if (__any(m > THR)) {
                const float dl = fmaxf(m, 0.f), f = __builtin_amdgcn_exp2f(-dl);
#pragma unroll
                for (int r = 0; r < 16; ++r) { s[r] -= dl; t[r] -= dl; negm[r] -= dl; oacc[r] *= f; }
            }
            oacc = MFMA32(va, pack(s, 0), oacc);
            oacc = MFMA32(vb, pack(s, 1), oacc);
            oacc = MFMA32(vc, pack(t, 0), oacc);
            oacc = MFMA32(vd, pack(t, 1), oacc);
        };
        auto wo_piece = [&](int n) { const int pc = n * 4 + rw; if (pc < 54) { const int p = pc / 18, o = pc % 18;
            glds16_dev((unsigned)(o * 1024 + lane * 16), wot + g3_step(p) * G3_SLOT, lds0 + g3_slot(0) + p * G3_SLOT + o * 1024); } };
        auto wo_pieces = [&](int n0, int n1) { if (!summer) for (int n = n0; n < n1; ++n) wo_piece(n); };
        const int lo = rt >= 2 ? 0 : (rt == 1 ? 4 - tg : 8 - tg);
        const int last = lo > 1 ? lo : 1;
        kfetch(8);
        tile(8, 7, true, 1);
        u32x4 hv[24];
        auto part_done = [&]() {
            if (lane == 0 && __hip_atomic_fetch_add((LAS unsigned*)(lds + L_CNT + 4), 1u, __ATOMIC_RELAXED, __HIP_MEMORY_SCOPE_WORKGROUP) == 5u) set_flag(F_P + (hp * 16 + rt) * 32, tag); };
        if (fetcher) {
            WAITV(0);
            part_done();
            flag_confirm(fk, fkp, tag);
#pragma unroll
            for (int n = 0; n < 24; ++n) { const int pc = 2 * n + (lw >> 1), tl = n >= 12, ar = (pc - 24 * tl) / 6, o = pc - 24 * tl - 6 * ar;
                hv[n] = ld_dev(wsr, (unsigned)(WS_KV + (size_t)(((rt - 2 + tl >= 0 ? rt - 2 + tl : 0) * 16 + hp) * 4 + ar) * 6144) + o * 1024 + lane * 16); }
        }
        if (tg >= 2) tile2(7);
        if (tg & 1) tile(tg == 3 ? 5 : 7, 0, false, 0);
        if (pkw && !fetcher) { WAITV(0); part_done(); }
        feature_map(p[0], phq);
        if (fetcher) {
#pragma unroll
            for (int n = 0; n < 24; ++n) { const int pc = 2 * n + (lw >> 1), tl = n >= 12, ar = (pc - 24 * tl) / 6, o = pc - 24 * tl - 6 * ar;
                *(LAS u32x4*)(lds + L_K + ar * 18432 + tl * 6144 + o * 1024 + lane * 16) = hv[n]; }
        }
        BAR();
        STAMP(7);
        gu32* fpp = summer ? F_P + (hp * 16 + r16) * 32 : F_WO + (cg + 8 * (lane & 31)) * 32;
        kfetch(7 - tg);
        if (rt >= 2) {
            if ((tg & 1) == 0) tile(7 - tg, 6 - tg, false, 0);
            if (tg < 2) tile2(6);
            const unsigned fp = flag_request(fpp);
            tile2(4);
            WAITV(0);
            flag_confirm(fp, fpp, tag);
            if (summer) pv_load(); else wo_pieces(0, 7);
            tile2(2); wo_pieces(7, 14);
            tile(0, 0, false, 2);
        } else {
            if (rt == 1) for (int KT = 7 - tg; KT >= last; --KT) tile(KT, KT - 1, false, 0);
            wait_flag(fpp, tag);
            if (summer) pv_load(); else wo_pieces(0, 14);
        }
    }
    STAMP(8);
    const unsigned obase = (unsigned)(WS_O + ((size_t)(rt * 8 + 2 * tg) * 144 + 9 * hp) * 256);
    const lptr stg = lds + L_OST;
    {
        const int tokb = 32 * tg + q32;
        const float gb = *(const LAS float*)(lds + L_GATE + tokb * 4);
        if (summer) {
            const unsigned one = (r16 < 8 && (r16 & 1) == 0) ? 0x00003f80u : (r16 < 8 ? 0x3f800000u : 0u);
            const bf16x8 idn = mk8((r16 >> 1) == 0 ? one : 0u, (r16 >> 1) == 1 ? one : 0u, (r16 >> 1) == 2 ? one : 0u, (r16 >> 1) == 3 ? one : 0u);
#pragma unroll
            for (int n = 0; n < 3; ++n) {
                f32x4 sm = {0, 0, 0, 0};
#pragma unroll
                for (int m = 0; m < 4; ++m) sm = MFMA16(idn, __builtin_bit_cast(bf16x8, pv[n][m]), sm);
                const int gr = (3 * rw + n) * 16 + r16, ph = gr >= 95, e0 = gr - 95 * ph, d = (e0 * 205) >> 10, u = 2 * (e0 - 5 * d) + g;
                if (gr < 190 && g < 2) *(LAS u32x2*)(lds + L_KVT + ph * 2016 + d * 96 + (u >= 5 ? 48 + (u - 5) * 8 : u * 8)) = u32x2{cvtpk(sm[0], sm[1]), cvtpk(sm[2], sm[3])};
            }
        } else {
            const int e0 = rw * 64 + lane;
            if (e0 < 76) *(LAS u32x2*)(lds + L_KVT + (e0 / 38) * 2016 + ((e0 % 38) >> 1) * 96 + (e0 & 1) * 48 + 40) = u32x2{0u, 0u};
            else if (e0 < 88) *(LAS u32x4*)(lds + L_KVT + ((e0 - 76) / 6) * 2016 + 20 * 96 + ((e0 - 76) % 6) * 16) = u32x4{0, 0, 0, 0};
        }
        auto sw = __builtin_amdgcn_permlane32_swap(__float_as_uint(oacc[10]), __float_as_uint(oacc[10]), false, false);
        const float sb = (1.f - gb) * __builtin_amdgcn_rcpf(__uint_as_float(sw[0]));
        const auto s0 = __builtin_amdgcn_permlane32_swap(cvtpk(oacc[0] * sb, oacc[1] * sb), cvtpk(oacc[4] * sb, oacc[5] * sb), false, false);
        const auto s1 = __builtin_amdgcn_permlane32_swap(cvtpk(oacc[2] * sb, oacc[3] * sb), cvtpk(oacc[6] * sb, oacc[7] * sb), false, false);
        st_dev(wsr, obase + (q32 >> 4) * 36864 + (2 * h + hh) * 256 + (q32 & 15) * 16, u32x4{s0[0], s1[0], s0[1], s1[1]});
        if (hh == 0) *(LAS unsigned*)(stg + tokb * 16 + h * 4) = cvtpk(oacc[8] * sb, oacc[9] * sb);
    }
    BAR();
    STAMP(9);
    {
        f32x4 dd[3];
        const int eo = (r16 >> 2) & 1;
        const bool mine = (r16 >> 3) == (g >> 1);
        const bf16x8 b0 = mk8(phq[0], phq[1], phq[2], phq[3]), b1 = mk8(phq[4], phq[5], phq[6], phq[7]), b2 = mk8(phq[8], 0u, 0u, 0u);
        bf16x8 af[3][3];
#pragma unroll
        for (int i = 0; i < 3; ++i) {
            const int sg = 4 * i + (r16 & 3);
            const int d = !mine ? 20 : i < 2 ? 8 * eo + sg : sg == 8 ? 16 + eo : sg == 10 ? 18 : 20;
            const lptr ap = lds + L_KVT + h * 2016 + d * 96 + (g & 1) * 48;
            af[i][0] = ld128(ap); af[i][1] = ld128(ap + 16); af[i][2] = ld128(ap + 32);
        }
        const int tok = 32 * tg + 16 * (g >> 1) + r16;
        const float gate = *(const LAS float*)(lds + L_GATE + tok * 4);
        SB();
#pragma unroll
        for (int i = 0; i < 3; ++i) { f32x4 a = MFMA16(af[i][0], b0, (f32x4{0, 0, 0, 0})); a = MFMA16(af[i][1], b1, a); dd[i] = MFMA16(af[i][2], b2, a); }
        const float sc = gate * __builtin_amdgcn_rcpf(dd[2][2] + 1e-6f);
        st_dev(wsr, obase + (g >> 1) * 36864 + (4 + 2 * h + (g & 1)) * 256 + r16 * 16,
               u32x4{cvtpk(dd[0][0] * sc, dd[0][1] * sc), cvtpk(dd[0][2] * sc, dd[0][3] * sc), cvtpk(dd[1][0] * sc, dd[1][1] * sc), cvtpk(dd[1][2] * sc, dd[1][3] * sc)});
        *(LAS u16*)(stg + tok * 16 + 8 + h * 4 + (g & 1) * 2) = (u16)cvtpk(dd[2][0] * sc, 0.f);
    }
    BAR();
    if (tid < 128) st_dev(wsr, (unsigned)(WS_O + ((size_t)(rt * 8 + (tid >> 4)) * 144 + 9 * hp + 8) * 256) + (tid & 15) * 16, *(const LAS u32x4*)(stg + tid * 16));
    STAMP(10);
    {
        const int rt3 = r64 >> 1, rh = r64 & 1, kh = wid >> 2, r = wid & 3;
        auto issue3 = [&](int p) {
            if (wid > 0) {
#pragma unroll
                for (int i = 0; i < 3; ++i) { const int pc = (wid - 1) + 7 * i;
                    if (pc < 18) glds16_dev((unsigned)(pc * 1024 + lane * 16), wot + g3_step(p) * G3_SLOT, lds0 + g3_slot(p) + pc * 1024); }
            }
        };
        issue3(3); issue3(4);
        if (wid == 0) WAITV(0); else if (wid < 5) WAITV(6); else WAITV(4);
        BAR();
        if (tid == 0) set_flag(F_O + (rt * 16 + hp) * 32, tag);
        issue3(5); issue3(6); issue3(7);
        const int wro = kh * 9216 + lane * 16, wr4 = kh * 9216 + 4096 + g * 128 + (r16 & 7) * 16;
        u32x4 w8[2][5];
        { const unsigned wb8 = (unsigned)(WS_WOT + (size_t)cg * 165888) + g3_step(8) * G3_SLOT;
#pragma unroll
          for (int e = 0; e < 2; ++e)
#pragma unroll
              for (int t = 0; t < 5; ++t) w8[e][t] = ld_dev(wsr, wb8 + e * 4608 + (t < 4 ? wro + t * 1024 : wr4)); }
        STAMP(11);
        if (wid == 0) wait_flag(F_O + (rt3 * 16 + r16) * 32, tag);
        DRAIN_BAR();
        STAMP(12);
        u32x4 of[9][2];
        const unsigned ob = (unsigned)(WS_O + ((size_t)(rt3 * 8 + 4 * rh + r) * 144 + 72 * kh) * 256 + lane * 16);
        auto o_load = [&](int p) { const unsigned o = ob + g3_step(p) * 2048; of[p][0] = ld_dev(wsr, o); of[p][1] = ld_dev(wsr, o + 1024); };
#pragma unroll
        for (int p = 0; p < ODEPTH; ++p) o_load(p);
        f32x4 a3[5];
#pragma unroll
        for (int t = 0; t < 5; ++t) a3[t] = f32x4{0, 0, 0, 0};
        bf16x8 wf[2][5];
#pragma unroll
        for (int e = 0; e < 2; ++e)
#pragma unroll
            for (int t = 0; t < 5; ++t) wf[e][t] = ld128(lds + g3_slot(0) + e * 4608 + (t < 4 ? wro + t * 1024 : wr4));
#pragma unroll
        for (int p = 0; p < 9; ++p) {
            if (p + ODEPTH < 9) o_load(p + ODEPTH);
            const lptr sp = lds + g3_slot(p + 1 < 8 ? p + 1 : 7);
#pragma unroll
            for (int e = 0; e < 2; ++e) {
                const bf16x8 bo = __builtin_bit_cast(bf16x8, of[p][e]);
                SB();
#pragma unroll
                for (int t = 0; t < 5; ++t) {
                    bf16x8 nw = wf[e][t];
                    if (p + 1 < 8) nw = ld128(sp + e * 4608 + (t < 4 ? wro + t * 1024 : wr4)); else if (p + 1 == 8) nw = __builtin_bit_cast(bf16x8, w8[e][t]);
                    SB();
                    a3[t] = MFMA16(wf[e][t], bo, a3[t]);
                    SB();
                    wf[e][t] = nw;
                }
            }
        }
        BAR();
        STAMP(13);
        const lptr rp0 = lds + r * 5120 + lane * 16;
        char* orow = (char*)out + (size_t)(rt3 * 128 + 64 * rh + 16 * r + r16) * 1152 + cg * 144 + g * 8;
        if (kh == 1) {
#pragma unroll
            for (int t = 0; t < 3; ++t) *(LAS f32x4*)(rp0 + t * 1024) = a3[t];
        } else {
#pragma unroll
            for (int t = 3; t < 5; ++t) *(LAS f32x4*)(rp0 + t * 1024) = a3[t];
        }
        BAR();
        if (kh == 0) {
#pragma unroll
            for (int t = 0; t < 3; ++t) { const f32x4 u = a3[t] + *(const LAS f32x4*)(rp0 + t * 1024); *(u32x2*)(orow + t * 32) = u32x2{cvtpk(u[0], u[1]), cvtpk(u[2], u[3])}; }
        } else {
#pragma unroll
            for (int t = 3; t < 5; ++t) { const f32x4 u = *(const LAS f32x4*)(rp0 + t * 1024) + a3[t]; if (t < 4 || g < 2) *(u32x2*)(orow + t * 32) = u32x2{cvtpk(u[0], u[1]), cvtpk(u[2], u[3])}; }
        }
        STAMP(14);
        asm volatile("fused_code_end:");
        if (b == 0 && tid == 0) set_flag((gu32*)(ws + WS_EPOCH), tag);
    }
}

extern "C" void kernel_launch(void* const* d_in, const int* in_sizes, int n_in, void* d_out, int out_size, void* d_ws, size_t ws_size, hipStream_t stream) {
    (void)hipFuncSetAttribute((const void*)fused_kernel, hipFuncAttributeMaxDynamicSharedMemorySize, LDS_BYTES);
    fused_kernel<<<256, 512, LDS_BYTES, stream>>>((const char*)d_in[0], (const char*)d_in[1], (const char*)d_in[5], (const char*)d_in[2], (const char*)d_in[3],
                                                  (const char*)d_in[6], (const char*)d_in[7], (char*)d_ws, (u16*)d_out);
}
```

```cpp
#include <hip/hip_runtime.h>
#include <hip/hip_bf16.h>
#include <stdint.h>

typedef unsigned short u16;
typedef short bf16x8 __attribute__((ext_vector_type(8)));
typedef short s16x4 __attribute__((ext_vector_type(4)));
typedef float f32x4 __attribute__((ext_vector_type(4)));
typedef float f32x16 __attribute__((ext_vector_type(16)));
typedef unsigned u32x4 __attribute__((ext_vector_type(4)));
typedef unsigned u32x2 __attribute__((ext_vector_type(2)));
typedef __bf16 bf16x2_t __attribute__((ext_vector_type(2)));
typedef float f32x2 __attribute__((ext_vector_type(2)));
#define LAS __attribute__((address_space(3)))
#define GAS __attribute__((address_space(1)))
typedef LAS char* lptr;
typedef GAS unsigned gu32;

constexpr size_t WS_EPOCH = 0;
constexpr size_t WS_FLAG = 4096;
constexpr size_t WS_WT   = 4096 + 5 * 256 * 128;
constexpr size_t WS_WOT  = WS_WT + 16 * 9 * 28672;
constexpr size_t WS_KV   = WS_WOT + 8 * 165888;
constexpr size_t WS_PART = WS_KV + 256 * 24576;
constexpr size_t WS_O    = WS_PART + 256 * 3072;

constexpr int XS = 16384, WS1 = 28672;
constexpr int L_X = 0, L_W = 3 * XS;
constexpr int L_WP = L_W + 2 * WS1;
constexpr int L_GW = L_WP + 23 * 1024;
constexpr int L_PHI = L_W + 3 * WS1;
constexpr int L_ZERO = L_PHI + 1792;
constexpr int L_WOP = L_PHI + 2048;
constexpr int L_PKX = 118784;
constexpr int L_CNT = 152064;
constexpr int L_K = 0;
constexpr int L_Q = 73728;
constexpr int L_VA = 86016;
constexpr int L_PK = 98304;
constexpr int L_GATE = 147456;
constexpr int L_KVT = 147968;
constexpr int L_OST = 73728;
constexpr int LDS_BYTES = 160 * 1024;
constexpr int ODEPTH = 5;
constexpr int G3_SLOT = 18432;
__device__ __forceinline__ constexpr int g3_slot(int p) { return (p < 3 ? 5 + p : p - 3) * G3_SLOT; }
constexpr unsigned SPIN_CAP = 1u << 18;
constexpr float LOG2E = 1.4426950408889634f;
constexpr float QSCALE = 0.23570226039551584f * LOG2E;
constexpr float THR = 8.f;
constexpr float NEGBIG = -1e30f;

__device__ __forceinline__ unsigned cvtpk(float lo, float hi) { const f32x2 v = {lo, hi}; return __builtin_bit_cast(unsigned, __builtin_convertvector(v, bf16x2_t)); }
__device__ __forceinline__ void glds16(unsigned voff, const void* sbase, unsigned lds_addr) {
    unsigned sv;
    asm volatile("s_mov_b32 %0, m0\n\ts_mov_b32 m0, %3\n\ts_nop 0\n\tglobal_load_lds_dwordx4 %1, %2\n\ts_mov_b32 m0, %0" : "=&s"(sv) : "v"(voff), "s"(sbase), "s"(lds_addr) : "memory");
}
__device__ __forceinline__ void glds16_dev(unsigned voff, const void* sbase, unsigned lds_addr) {
    unsigned sv;
    asm volatile("s_mov_b32 %0, m0\n\ts_mov_b32 m0, %3\n\ts_nop 0\n\tglobal_load_lds_dwordx4 %1, %2 sc1\n\ts_mov_b32 m0, %0" : "=&s"(sv) : "v"(voff), "s"(sbase), "s"(lds_addr) : "memory");
}
__device__ __forceinline__ void glds16_ptr(const void* g, unsigned lds_addr) {
    unsigned sv;
    asm volatile("s_mov_b32 %0, m0\n\ts_mov_b32 m0, %2\n\ts_nop 0\n\tglobal_load_lds_dwordx4 %1, off\n\ts_mov_b32 m0, %0" : "=&s"(sv) : "v"(g), "s"(lds_addr) : "memory");
}
#define WAITV(n) asm volatile("s_waitcnt vmcnt(" #n ")" ::: "memory")
#define BAR() do { asm volatile("" ::: "memory"); __builtin_amdgcn_s_waitcnt(0xC07F); __builtin_amdgcn_s_barrier(); asm volatile("" ::: "memory"); } while (0)
#define DRAIN_BAR() do { asm volatile("s_waitcnt vmcnt(0)" ::: "memory"); BAR(); } while (0)
#define SB() __builtin_amdgcn_sched_barrier(0)
#define MFMA16(a, b, c) __builtin_amdgcn_mfma_f32_16x16x32_bf16(a, b, c, 0, 0, 0)
#define MFMA32(a, b, c) __builtin_amdgcn_mfma_f32_32x32x16_bf16(a, b, c, 0, 0, 0)
__device__ __forceinline__ bf16x8 ld128(lptr p) { return *(const LAS bf16x8*)p; }
__device__ __forceinline__ s16x4 trrd(lptr p) { typedef short v4i16_t __attribute__((ext_vector_type(4))); return __builtin_bit_cast(s16x4, __builtin_amdgcn_ds_read_tr16_b64_v4i16((LAS v4i16_t*)p)); }
__device__ __forceinline__ bf16x8 cat8(s16x4 a, s16x4 b) { return __builtin_shufflevector(a, b, 0, 1, 2, 3, 4, 5, 6, 7); }
__device__ __forceinline__ bf16x8 mk8(unsigned a, unsigned b, unsigned c, unsigned d) { u32x4 w = {a, b, c, d}; return __builtin_bit_cast(bf16x8, w); }
__device__ __forceinline__ float vmax2(unsigned a, unsigned b) { float r; asm("v_max_f32 %0, %1, %2" : "=v"(r) : "v"(a), "v"(b)); return r; }
__device__ __forceinline__ float x16max(float x) { auto s = __builtin_amdgcn_permlane16_swap(__float_as_uint(x), __float_as_uint(x), false, false); return vmax2(s[0], s[1]); }
__device__ __forceinline__ float x16sum(float x) { auto s = __builtin_amdgcn_permlane16_swap(__float_as_uint(x), __float_as_uint(x), false, false); return __uint_as_float(s[0]) + __uint_as_float(s[1]); }
__device__ __forceinline__ float x32max(float x) { auto s = __builtin_amdgcn_permlane32_swap(__float_as_uint(x), __float_as_uint(x), false, false); return vmax2(s[0], s[1]); }
__device__ __forceinline__ void st_dev(const __amdgpu_buffer_rsrc_t rs, unsigned off, u32x4 v) { __builtin_amdgcn_raw_buffer_store_b128(v, rs, off, 0, 16); }
__device__ __forceinline__ u32x4 ld_dev(const __amdgpu_buffer_rsrc_t rs, unsigned off) { return __builtin_amdgcn_raw_buffer_load_b128(rs, off, 0, 16); }
__device__ __forceinline__ void wait_flag(gu32* p, unsigned tag) {
    for (unsigned spins = 0; spins < SPIN_CAP; ++spins) {
        const unsigned v = __hip_atomic_load(p, __ATOMIC_RELAXED, __HIP_MEMORY_SCOPE_AGENT);
        if (__all(v == tag)) break;
        __builtin_amdgcn_s_sleep(1);
    }
}
__device__ __forceinline__ void set_flag(gu32* p, unsigned tag) { __hip_atomic_store(p, tag, __ATOMIC_RELAXED, __HIP_MEMORY_SCOPE_AGENT); }
__device__ __forceinline__ u32x4 gather8(lptr p, int stride) {
    unsigned w[4];
#pragma unroll
    for (int j = 0; j < 4; ++j) w[j] = (unsigned)*(const LAS u16*)(p + 2 * j * stride) | ((unsigned)*(const LAS u16*)(p + (2 * j + 1) * stride) << 16);
    return u32x4{w[0], w[1], w[2], w[3]};
}
template <int V> struct IC { static constexpr int value = V; };
__device__ __forceinline__ int piece_start(int j) { return (9 * j + 1) >> 1; }

__device__ __forceinline__ unsigned flag_request(gu32* p) { unsigned v; asm volatile("global_load_dword %0, %1, off sc1" : "=v"(v) : "v"(p) : "memory"); return v; }
__device__ __forceinline__ void flag_confirm(unsigned v, gu32* p, unsigned tag) {
    asm volatile("" : "+v"(v));
    if (!__all(v == tag)) wait_flag(p, tag);
}

__device__ __forceinline__ void feature_map(const float (&p)[9], unsigned (&o)[9]) {
    float mx = fmaxf(fmaxf(p[0], p[1]), p[2]), mn = fminf(fminf(p[0], p[1]), p[2]);
#pragma unroll
    for (int i = 3; i < 9; i += 2) { mx = fmaxf(fmaxf(mx, p[i]), p[i + 1]); mn = fminf(fminf(mn, p[i]), p[i + 1]); }
    mx = x16max(mx); mn = -x16max(-mn);
    const float mxl = -mx * LOG2E, mnl = mn * LOG2E;
    float a[9], b[9], sa = 0.f, sb = 0.f;
#pragma unroll
    for (int i = 0; i < 9; ++i) { a[i] = __builtin_amdgcn_exp2f(__builtin_fmaf(p[i], LOG2E, mxl)); b[i] = __builtin_amdgcn_exp2f(__builtin_fmaf(p[i], -LOG2E, mnl)); sa += a[i]; sb += b[i]; }
    sa = x16sum(sa); sb = x16sum(sb);
    const float ra = __builtin_amdgcn_rcpf(sa), rb = __builtin_amdgcn_rcpf(sb);
#pragma unroll
    for (int i = 0; i < 9; ++i) { a[i] *= ra; b[i] *= rb; }
    o[0] = cvtpk(a[0], a[1]); o[1] = cvtpk(a[2], a[3]); o[2] = cvtpk(a[4], a[5]); o[3] = cvtpk(a[6], a[7]); o[4] = cvtpk(a[8], b[0]);
    o[5] = cvtpk(b[1], b[2]); o[6] = cvtpk(b[3], b[4]); o[7] = cvtpk(b[5], b[6]); o[8] = cvtpk(b[7], b[8]);
}

__global__ __launch_bounds__(512, 2) void fused_kernel(const char* __restrict__ x, const char* __restrict__ wA, const char* __restrict__ wB, const char* __restrict__ wphi,
                                                       const char* __restrict__ woA, const char* __restrict__ woB, const char* __restrict__ gw,
                                                       char* __restrict__ ws, u16* __restrict__ out) {
    asm volatile("" :: "s"(x), "s"(wA), "s"(wB), "s"(wphi), "s"(woA), "s"(woB), "s"(gw), "s"(ws), "s"(out));
    extern __shared__ __attribute__((aligned(16))) char lds_raw[];
    const lptr lds = (lptr)lds_raw;
    const unsigned lds0 = (unsigned)(uintptr_t)lds_raw;
    const int tid = threadIdx.x, lane = tid & 63, wid = __builtin_amdgcn_readfirstlane(tid >> 6);
    const int b = blockIdx.x, hp = (b & 3) * 4 + ((b >> 3) & 3), rt = ((b >> 2) & 1) * 8 + (b >> 5);
    const bool loader = wid >= 4;
    const int lw = wid & 3;
    const int h = lw >> 1, tg = loader ? 3 - 2 * (lw & 1) : 2 * (lw & 1);
    const int r16 = lane & 15, g = lane >> 4;
    gu32* flags = (gu32*)(ws + WS_FLAG);
    gu32* F_W = flags;
    gu32* F_WO = flags + 256 * 32;
    gu32* F_KV = flags + 512 * 32;
    gu32* F_P = flags + 768 * 32;
    gu32* F_O = flags + 1024 * 32;
    {
        unsigned long long pc0; unsigned len;
        asm volatile("s_getpc_b64 %0\nfused_code_begin:" : "=s"(pc0));
        asm volatile("s_mov_b32 %0, fused_code_end-fused_code_begin" : "=s"(len));
        if (wid == 3) {
            const unsigned j = blockIdx.x >> 3;
            if ((j + 1) * 1024 <= len) glds16(j * 1024 + lane * 16, (const void*)pc0, lds0 + 160256);
            if ((j + 33) * 1024 <= len) glds16((j + 32) * 1024 + lane * 16, (const void*)pc0, lds0 + 160256);
        }
    }
    unsigned tagv = flag_request((gu32*)(ws + WS_EPOCH));
    const __amdgpu_buffer_rsrc_t wsr = __builtin_amdgcn_make_buffer_rsrc(ws, 0, 0x2000000, 0x00020000);

    const int G0 = piece_start(rt), nG = piece_start(rt + 1) - G0;
    const int j2 = b >> 3, cgp = b & 7, C0 = piece_start(j2), nC = piece_start(j2 + 1) - C0;
#pragma unroll
    for (int i = 0; i < 3; ++i) {
        const int pc = wid + 8 * i;
        if (pc < 23) { int c = pc * 64 + lane; if (c >= nG * 288) c = 0;
            const int row = c / 36, ch = c % 36, w = ch / 3, cc = ch % 3, sg = w >> 1;
            const char* src = (sg < 3 ? wA + sg * 1152 : wB + (sg - 3) * 1152) + (size_t)(8 * G0 + row) * 3456 + (2 * hp + (w & 1)) * 36 + (cc == 2 ? 20 : cc * 16);
            glds16_ptr(src, lds0 + L_WP + pc * 1024); }
        else glds16(G0 * 16 + (lane < nG ? lane : 0) * 16, gw, lds0 + L_GW);
    }
    const int krot = (2 * (hp & 3) + (rt & 7)) % 9;
    auto kact = [&](int kb) { const int k = kb + krot; return k >= 9 ? k - 9 : k; };
    unsigned xo[4];
    auto issue_x = [&](int kb, int slot) {
        const char* src = x + kact(kb) * 128;
#pragma unroll
        for (int i = 0; i < 4; ++i) glds16(xo[i], src, lds0 + L_X + slot * XS + lw * 4096 + i * 1024);
    };
    if (loader) {
#pragma unroll
        for (int i = 0; i < 4; ++i) { const int row = 32 * lw + 8 * i + (lane >> 3); xo[i] = (unsigned)(rt * 128 + row) * 1152u + (unsigned)(((lane & 7) ^ ((row >> 1) & 7)) << 4); }
        issue_x(0, 0);
        WAITV(4);
    } else WAITV(0);
    BAR();
    asm volatile("" : "+v"(tagv));
    const unsigned tag = __builtin_amdgcn_readfirstlane(tagv) + 1u;
    {
        const int q4 = r16 >> 2, p4 = r16 & 3;
        const unsigned wtb = (unsigned)(WS_WT + (size_t)hp * 9 * 28672);
        u32x4 v[3]; unsigned off[3];
#pragma unroll
        for (int i = 0; i < 2; ++i) {
            const int U0 = i * 32 + wid * 4 + g, U = U0 < nG * 12 ? U0 : 0, Gi = (U * 43) >> 9, ht = U - 12 * Gi, hh2 = ht >= 6, t = ht - 6 * hh2;
            const lptr p = lds + L_WP + (8 * Gi + q4) * 576 + (((0x541032 >> (4 * t)) & 7) * 2 + hh2) * 48 + p4 * 8;
            v[i] = __builtin_bit_cast(u32x4, cat8(trrd(p), trrd(p + 4 * 576)));
            const int G = G0 + Gi;
            off[i] = U0 < nG * 12 ? wtb + (G >> 3) * 28672 + ((hh2 * 7 + t) * 2 + ((G >> 2) & 1)) * 1024 + ((G & 3) * 16 + r16) * 16 : 0xfffffff0u;
        }
        {
            const int U0 = wid * 4 + g, U = U0 < nG * 2 ? U0 : 0, Gi = U >> 1, hh2 = U & 1;
            const u32x4 gt = gather8(lds + L_WP + 8 * Gi * 576 + ((r16 < 12 ? r16 >> 1 : 0) * 2 + hh2) * 48 + 44 + 2 * (r16 & 1), 576);
            const u32x4 gv = *(const LAS u32x4*)(lds + L_GW + Gi * 16);
            v[2] = r16 < 12 ? gt : r16 == 12 ? gv : u32x4{0, 0, 0, 0};
            const int G = G0 + Gi;
            off[2] = U0 < nG * 2 ? wtb + (G >> 3) * 28672 + ((hh2 * 7 + 6) * 2 + ((G >> 2) & 1)) * 1024 + ((G & 3) * 16 + r16) * 16 : 0xfffffff0u;
        }
#pragma unroll
        for (int i = 0; i < 3; ++i) st_dev(wsr, off[i], v[i]);
    }
    if (loader) { issue_x(1, 1); issue_x(2, 2); WAITV(8); } else WAITV(0);
    BAR();
    if (tid == 0) { set_flag(F_W + (hp * 16 + rt) * 32, tag); *(LAS u32x2*)(lds + L_CNT) = u32x2{0u, 0u}; }
    const char* wt = ws + WS_WT + (size_t)hp * 9 * 28672;
    auto issue_w = [&](int kb, int slot) {
#pragma unroll
        for (int i = 0; i < 7; ++i) glds16_dev((unsigned)(lw * 1024 + lane * 16), wt + kact(kb) * 28672 + i * 4096, lds0 + L_W + slot * WS1 + i * 4096 + lw * 1024);
    };
    if (loader) {
#pragma unroll
        for (int i = 0; i < 2; ++i) { const int pc = lw + 4 * i;
            if (pc < 6) { int c = pc * 64 + lane; if (c >= nC * 72) c = 0;
                const int row = c / 9, cc = c % 9, C = C0 + (row >> 3), hq = C / 9, p = 8 * (C % 9) + (row & 7);
                int br, head, d;
                if (p < 64) { br = p < 32; head = (p >> 4) & 1; d = p & 15; } else { br = p < 68; head = (p >> 1) & 1; d = 16 + (p & 1); }
                glds16_ptr((br ? woB : woA) + ((2 * hq + head) * 18 + d) * 1152 + cgp * 144 + cc * 16, lds0 + L_WOP + pc * 1024); }
            else { int c = (pc - 6) * 64 + lane; if (c >= 108) c = 0; const int row = c / 3, cc = c % 3;
                glds16((unsigned)(hp * 1296 + row * 36 + (cc == 2 ? 20 : cc * 16)), wphi, lds0 + L_PHI + (pc - 6) * 1024); } }
    } else if (wid == 0) wait_flag(F_W + (hp * 16 + r16) * 32, tag);
    BAR();
    u32x4 pkt[2][2];
    if (!loader) {
        f32x4 acc[7][4];
#pragma unroll
        for (int t = 0; t < 7; ++t)
#pragma unroll
            for (int u = 0; u < 4; ++u) acc[t][u] = f32x4{0, 0, 0, 0};
        const int th = lw & 1;
        const int xr = L_X + (64 * th + r16) * 128 + ((g ^ (r16 >> 1)) << 4), wr = L_W + h * 14336 + lane * 16;
        bf16x8 wa[7], xa[4], wb[7], xb[4];
        BAR();
#pragma unroll
        for (int u = 0; u < 4; ++u) xa[u] = ld128(lds + xr + u * 2048);
#pragma unroll
        for (int t = 0; t < 7; ++t) wa[t] = ld128(lds + wr + t * 2048);
        int slot = 0;
        for (int i = 0; i < 8; ++i) {
            const lptr sx = lds + slot * XS, sw = lds + slot * WS1;
            SB();
#pragma unroll
            for (int u = 0; u < 4; ++u) xb[u] = ld128(sx + (xr ^ 64) + u * 2048);
#pragma unroll
            for (int t = 0; t < 7; ++t) { wb[t] = ld128(sw + wr + t * 2048 + 1024); SB();
#pragma unroll
                for (int u = 0; u < 4; ++u) acc[t][u] = MFMA16(wa[t], xa[u], acc[t][u]);
                SB(); }
            BAR();
            slot = slot == 2 ? 0 : slot + 1;
            const lptr nx = lds + slot * XS, nw = lds + slot * WS1;
            SB();
#pragma unroll
            for (int u = 0; u < 4; ++u) xa[u] = ld128(nx + xr + u * 2048);
#pragma unroll
            for (int t = 0; t < 7; ++t) { const bf16x8 n = ld128(nw + wr + t * 2048); SB();
#pragma unroll
                for (int u = 0; u < 4; ++u) acc[t][u] = MFMA16(wb[t], xb[u], acc[t][u]);
                SB(); wa[t] = n; }
        }
        {
            const lptr sx = lds + 2 * XS, sw = lds + 2 * WS1;
            SB();
#pragma unroll
            for (int u = 0; u < 4; ++u) xb[u] = ld128(sx + (xr ^ 64) + u * 2048);
#pragma unroll
            for (int t = 0; t < 7; ++t) { wb[t] = ld128(sw + wr + t * 2048 + 1024); SB();
#pragma unroll
                for (int u = 0; u < 4; ++u) acc[t][u] = MFMA16(wa[t], xa[u], acc[t][u]);
                SB(); }
            BAR();
#pragma unroll
            for (int t = 0; t < 7; ++t)
#pragma unroll
                for (int u = 0; u < 4; ++u) acc[t][u] = MFMA16(wb[t], xb[u], acc[t][u]);
        }
        const int row0 = (64 * th + r16) * 48;
        const lptr Kb = lds + L_K + h * 36864 + 256 * 48 + row0, Vb = Kb + 18432, Qb = lds + L_Q + h * 6144 + row0, VAb = lds + L_VA + h * 6144 + row0;
#pragma unroll
        for (int u = 0; u < 4; ++u) {
            *(LAS u32x2*)(Kb + u * 768 + 8 * g) = u32x2{cvtpk(acc[4][u][0], acc[4][u][1]), cvtpk(acc[4][u][2], acc[4][u][3])};
            *(LAS u32x2*)(Vb + u * 768 + 8 * g) = u32x2{cvtpk(acc[5][u][0], acc[5][u][1]), cvtpk(acc[5][u][2], acc[5][u][3])};
        }
        if (g == 2) {
#pragma unroll
            for (int u = 0; u < 4; ++u) {
                *(LAS u32x4*)(Kb + u * 768 + 32) = u32x4{cvtpk(acc[6][u][0], acc[6][u][1]), 0u, 0u, 0u};
                *(LAS u32x4*)(Vb + u * 768 + 32) = u32x4{cvtpk(acc[6][u][2], acc[6][u][3]), 0x00003f80u, 0u, 0u};
            }
        }
        unsigned st0[4], st1[4]; float gt[4];
#pragma unroll
        for (int u = 0; u < 4; ++u) {
            const unsigned s0 = cvtpk(acc[6][u][0], acc[6][u][1]), s1 = cvtpk(acc[6][u][2], acc[6][u][3]);
#pragma unroll
            for (int X = 0; X < 2; ++X) {
                const u32x4 tp = {cvtpk(acc[2 + X][u][0], acc[2 + X][u][1]), cvtpk(acc[2 + X][u][2], acc[2 + X][u][3]), s0, s1};
                if (u < 2) pkt[X][u] = tp; else *(LAS u32x4*)(lds + L_PKX + ((lw * 2 + X) * 2 + (u - 2)) * 1024 + lane * 16) = tp;
            }
            *(LAS u32x2*)(VAb + u * 768 + 8 * g) = u32x2{cvtpk(acc[0][u][0], acc[0][u][1]), cvtpk(acc[0][u][2], acc[0][u][3])};
            *(LAS u32x2*)(Qb + u * 768 + 8 * g) = u32x2{cvtpk(acc[1][u][0] * QSCALE, acc[1][u][1] * QSCALE), cvtpk(acc[1][u][2] * QSCALE, acc[1][u][3] * QSCALE)};
            st0[u] = s0; st1[u] = cvtpk(acc[6][u][2] * QSCALE, acc[6][u][3] * QSCALE);
            gt[u] = __builtin_amdgcn_rcpf(1.f + __builtin_amdgcn_exp2f(-acc[6][u][0] * LOG2E));
        }
        if (g == 1) {
#pragma unroll
            for (int u = 0; u < 4; ++u) { *(LAS u32x2*)(VAb + u * 768 + 32) = u32x2{st0[u], 0x00003f80u}; *(LAS unsigned*)(Qb + u * 768 + 32) = st1[u]; }
        } else if (g == 3 && h == 0) {
#pragma unroll
            for (int u = 0; u < 4; ++u) *(LAS float*)(lds + L_GATE + (64 * th + 16 * u + r16) * 4) = gt[u];
        }
    } else {
        issue_w(0, 0); issue_w(1, 1); issue_w(2, 2);
        WAITV(14);
        BAR();
        for (int i = 0; i < 8; ++i) {
            if (i == 0) WAITV(7); else if (i < 7) WAITV(11); else WAITV(1);
            if (i == 1 && lw == 0 && lane < 4) *(LAS u32x4*)(lds + L_ZERO + lane * 16) = u32x4{0, 0, 0, 0};
            BAR();
            if (i < 6) { issue_x(i + 3, i % 3); issue_w(i + 3, i % 3); }
            if (i == 6 || i == 7) {
                const int q4 = r16 >> 2, p4 = r16 & 3;
                const int U0 = (i - 6) * 16 + lw * 4 + g, U = U0 < nC * 5 ? U0 : 0, Ci = (U * 52) >> 8, t = U - 5 * Ci, C = C0 + Ci, kq = (C * 57) >> 11, s = (C - 36 * kq) >> 2, gg = C & 3;
                const lptr p = lds + L_WOP + (8 * Ci + q4) * 144 + (t < 4 ? t * 32 + p4 * 8 : 128 + (p4 & 1) * 8);
                const u32x4 v = __builtin_bit_cast(u32x4, cat8(trrd(p), trrd(p + 4 * 144)));
                const unsigned off = (unsigned)(WS_WOT + (size_t)cgp * 165888) + (s * 4 + kq) * 4608 + (t < 4 ? t * 1024 + (gg * 16 + r16) * 16 : 4096 + (gg * 8 + r16) * 16);
                st_dev(wsr, U0 < nC * 5 && (t < 4 || r16 < 8) ? off : 0xfffffff0u, v);
            }
        }
        BAR();
    }
    BAR();
    const int cg = (b >> 3) & 7, r64 = 4 * (b & 7) + (b >> 6);
    if (loader) {
#pragma unroll
        for (int X = 0; X < 2; ++X)
#pragma unroll
            for (int u = 0; u < 2; ++u) pkt[X][u] = *(const LAS u32x4*)(lds + L_PKX + (((lw ^ 1) * 2 + X) * 2 + u) * 1024 + lane * 16);
    } else {
        u32x4 kvv[6];
        const lptr kr = lds + L_K + h * 36864 + (256 + 64 * (lw & 1)) * 48 + lane * 16;
#pragma unroll
        for (int i = 0; i < 3; ++i) { kvv[i] = *(const LAS u32x4*)(kr + i * 1024); kvv[3 + i] = *(const LAS u32x4*)(kr + 18432 + i * 1024); }
        const unsigned ko = (unsigned)(WS_KV + (size_t)((rt * 16 + hp) * 4 + h * 2) * 6144) + (lw & 1) * 3072 + lane * 16;
#pragma unroll
        for (int i = 0; i < 3; ++i) { st_dev(wsr, ko + i * 1024, kvv[i]); st_dev(wsr, ko + 6144 + i * 1024, kvv[3 + i]); }
    }
    unsigned phq[9];
    float p[2][9];
    {
        unsigned phk[9];
        const int q4 = r16 >> 2, p4 = r16 & 3;
        const unsigned pw0 = lds0 + L_PHI + h * 864, zp = lds0 + L_ZERO;
        s16x4 R1[2][3], R2[2][2][3];
#pragma unroll
        for (int hf = 0; hf < 2; ++hf) {
            const bool ok = (p4 >> 1) == hf;
            const lptr a = (lptr)(uintptr_t)(ok ? pw0 + (4 * g + q4) * 48 + 16 * (p4 & 1) : zp), a2 = (lptr)(uintptr_t)(ok ? pw0 + (4 * g + q4) * 48 + 40 : zp);
            R1[hf][0] = trrd(a); R1[hf][1] = trrd(a + 8); R1[hf][2] = trrd(a2);
#pragma unroll
            for (int X = 0; X < 2; ++X) {
                const bool ok2 = ok && g == 0 && (q4 >> 1) == X;
                const lptr c = (lptr)(uintptr_t)(ok2 ? pw0 + (16 + (q4 & 1)) * 48 + 16 * (p4 & 1) : zp), c2 = (lptr)(uintptr_t)(ok2 ? pw0 + (16 + (q4 & 1)) * 48 + 40 : zp);
                R2[X][hf][0] = trrd(c); R2[X][hf][1] = trrd(c + 8); R2[X][hf][2] = trrd(c2);
            }
        }
        SB();
#pragma unroll
        for (int X = 0; X < 2; ++X) {
            const bf16x8 bA = __builtin_bit_cast(bf16x8, pkt[X][0]), bB = __builtin_bit_cast(bf16x8, pkt[X][1]);
            f32x4 d[3];
#pragma unroll
            for (int i = 0; i < 3; ++i) d[i] = MFMA16(cat8(R1[0][i], R2[X][0][i]), bA, (f32x4{0, 0, 0, 0}));
#pragma unroll
            for (int i = 0; i < 3; ++i) d[i] = MFMA16(cat8(R1[1][i], R2[X][1][i]), bB, d[i]);
#pragma unroll
            for (int j = 0; j < 4; ++j) { p[X][j] = d[0][j]; p[X][4 + j] = d[1][j]; }
            p[X][8] = (g & 1) ? d[2][3] : d[2][2];
        }
        feature_map(p[1], phk);
        const lptr pkp = lds + L_PK + h * 10240 + (32 * tg + 16 * (g >> 1) + r16) * 80 + (g & 1) * 40;
        *(LAS u32x2*)(pkp) = u32x2{phk[0], phk[1]}; *(LAS u32x2*)(pkp + 8) = u32x2{phk[2], phk[3]}; *(LAS u32x2*)(pkp + 16) = u32x2{phk[4], phk[5]};
        *(LAS u32x2*)(pkp + 24) = u32x2{phk[6], phk[7]}; *(LAS u32x2*)(pkp + 32) = u32x2{phk[8], 0u};
        WAITV(0);
        if (!loader && lane == 0 && __hip_atomic_fetch_add((LAS unsigned*)(lds + L_CNT), 1u, __ATOMIC_RELAXED, __HIP_MEMORY_SCOPE_WORKGROUP) == 3u) set_flag(F_KV + (hp * 16 + rt) * 32, tag);
    }
    BAR();
    if (tid == 0) set_flag(F_WO + b * 32, tag);
    const bool fetcher = !loader && (lw & 1) == 0 && rt > 0;
    gu32* fkp = F_KV + (hp * 16 + rt - 1 - (rt > 1 ? (lane & 1) : 0)) * 32;
    unsigned fk = 0;
    if (fetcher) fk = flag_request(fkp);
    const bool pkw = wid != 4 && wid != 6;
    if (pkw) {
        const int w6 = wid < 4 ? wid : wid == 5 ? 4 : 5, hh = w6 / 3, mt = w6 % 3, q4 = r16 >> 2, p4 = r16 & 3;
        int u = 4 * mt + p4; if (u > 9) u = 9;
        const lptr pa = lds + L_PK + hh * 10240 + (8 * g + q4) * 80 + u * 8;
        const lptr pb0 = lds + L_VA + hh * 6144 + (8 * g + q4) * 48 + p4 * 8;
        const lptr pb1 = lds + L_VA + hh * 6144 + (8 * g + q4) * 48 + 32;
        f32x4 d0 = {0, 0, 0, 0}, d1 = {0, 0, 0, 0};
        bf16x8 a[4], b0[4], b1[4];
#pragma unroll
        for (int ks = 0; ks < 4; ++ks) {
            a[ks] = cat8(trrd(pa + ks * 32 * 80), trrd(pa + ks * 32 * 80 + 4 * 80));
            b0[ks] = cat8(trrd(pb0 + ks * 32 * 48), trrd(pb0 + ks * 32 * 48 + 4 * 48));
            b1[ks] = cat8(trrd(pb1 + ks * 32 * 48), trrd(pb1 + ks * 32 * 48 + 4 * 48));
        }
        SB();
#pragma unroll
        for (int ks = 0; ks < 4; ++ks) { d0 = MFMA16(a[ks], b0[ks], d0); d1 = MFMA16(a[ks], b1[ks], d1); }
        const int uu = 4 * mt + g;
        const unsigned po = (unsigned)(WS_PART + (size_t)(hp * 16 + rt) * 3072 + hh * 1520);
        if (uu < 10) {
            __builtin_amdgcn_raw_buffer_store_b64(u32x2{cvtpk(d0[0], d0[1]), cvtpk(d0[2], d0[3])}, wsr, po + (r16 * 10 + uu) * 8, 0, 16);
            if (r16 < 3) __builtin_amdgcn_raw_buffer_store_b64(u32x2{cvtpk(d1[0], d1[1]), cvtpk(d1[2], d1[3])}, wsr, po + ((16 + r16) * 10 + uu) * 8, 0, 16);
        }
    }
    const int q32 = lane & 31, hh = lane >> 5;
    const char* wot = ws + WS_WOT + (size_t)cg * 165888;
    auto g3_step = [&](int p) { const int s = p + cg; return s >= 9 ? s - 9 : s; };
    f32x16 oacc;
    const bool summer = tg < 2;
    const int rw = 2 * h + (tg & 1);
    u32x4 pv[3][4];
    auto pv_load = [&]() {
#pragma unroll
        for (int n = 0; n < 3; ++n) { const int gr = (3 * rw + n) * 16 + r16;
#pragma unroll
            for (int m = 0; m < 4; ++m) pv[n][m] = ld_dev(wsr, (unsigned)(WS_PART + (size_t)(hp * 16 + 4 * m + g) * 3072) + (gr < 190 ? gr : 0) * 16); }
    };
    {
        const lptr Kb = lds + L_K + h * 36864, Vb = Kb + 18432, Qb = lds + L_Q + h * 6144;
        const bf16x8 qf0 = ld128(Qb + (32 * tg + q32) * 48 + 16 * hh);
        const unsigned q16 = *(const LAS unsigned*)(Qb + (32 * tg + q32) * 48 + 32);
        const bf16x8 qf1 = mk8(hh ? 0u : q16, 0u, 0u, 0u);
        const lptr kp = Kb + (32 * tg + q32) * 48;
        const int i16 = lane & 15, dhalf = (lane >> 4) & 1;
        const lptr vp = Vb + (32 * tg + 4 * hh + (i16 >> 2)) * 48 + (dhalf ? 32 + ((i16 & 3) ? 8 : 0) : (i16 & 3) * 8);
        f32x16 negm;
#pragma unroll
        for (int r = 0; r < 16; ++r) { oacc[r] = 0.f; negm[r] = 0.f; }
        bf16x8 k0, k1, k2, k3;
        auto kfetch = [&](int KN) { const int KM = KN > 0 ? KN - 1 : 0;
            k0 = ld128(kp + KN * 32 * 48 + 16 * hh); k1 = ld128(kp + KN * 32 * 48 + 32); k2 = ld128(kp + KM * 32 * 48 + 16 * hh); k3 = ld128(kp + KM * 32 * 48 + 32); };
        auto vfrag = [&](int KT, int hf) { return cat8(trrd(vp + (KT * 32 + 16 * hf) * 48), trrd(vp + (KT * 32 + 16 * hf + 8) * 48)); };
        auto rowmax = [&](const f32x16& s) { float m = fmaxf(fmaxf(s[0], s[1]), s[2]);
#pragma unroll
            for (int r = 3; r < 15; r += 2) m = fmaxf(fmaxf(m, s[r]), s[r + 1]);
            return fmaxf(m, s[15]); };
        auto pack = [&](const f32x16& s, int hf) { return mk8(cvtpk(__builtin_amdgcn_exp2f(s[8 * hf]), __builtin_amdgcn_exp2f(s[8 * hf + 1])), cvtpk(__builtin_amdgcn_exp2f(s[8 * hf + 2]), __builtin_amdgcn_exp2f(s[8 * hf + 3])),
                                                             cvtpk(__builtin_amdgcn_exp2f(s[8 * hf + 4]), __builtin_amdgcn_exp2f(s[8 * hf + 5])), cvtpk(__builtin_amdgcn_exp2f(s[8 * hf + 6]), __builtin_amdgcn_exp2f(s[8 * hf + 7]))); };
        auto tile = [&](int KT, int KN, bool first, int mask) {
            SB();
            f32x16 s = MFMA32(k0, qf0, negm); s = MFMA32(k1, qf1, s);
            SB();
            const bf16x8 va = vfrag(KT, 0), vb = vfrag(KT, 1);
            kfetch(KN);
            SB();
            if (mask) {
#pragma unroll
                for (int r = 0; r < 16; ++r) { const int jj = (r & 3) + 8 * (r >> 2) + 4 * hh; const bool ok = mask == 1 ? jj <= q32 : jj > q32; s[r] = ok ? s[r] : NEGBIG; }
            }
            const float m = x32max(rowmax(s));
            if (first || __any(m > THR)) {
                const float dl = first ? m : fmaxf(m, 0.f);
                const float f = first ? 1.f : __builtin_amdgcn_exp2f(-dl);
#pragma unroll
                for (int r = 0; r < 16; ++r) { s[r] -= dl; negm[r] -= dl; if (!first) oacc[r] *= f; }
            }
            oacc = MFMA32(va, pack(s, 0), oacc);
            oacc = MFMA32(vb, pack(s, 1), oacc);
        };
        auto tile2 = [&](int KT) {
            SB();
            f32x16 s = MFMA32(k0, qf0, negm), t = MFMA32(k2, qf0, negm); s = MFMA32(k1, qf1, s); t = MFMA32(k3, qf1, t);
            SB();
            const bf16x8 va = vfrag(KT, 0), vb = vfrag(KT, 1), vc = vfrag(KT - 1, 0), vd = vfrag(KT - 1, 1);
            kfetch(KT >= 2 ? KT - 2 : 0);
            SB();
            const float m = x32max(fmaxf(rowmax(s), rowmax(t)));
            if (__any(m > THR)) {
                const float dl = fmaxf(m, 0.f), f = __builtin_amdgcn_exp2f(-dl);
#pragma unroll
                for (int r = 0; r < 16; ++r) { s[r] -= dl; t[r] -= dl; negm[r] -= dl; oacc[r] *= f; }
            }
            oacc = MFMA32(va, pack(s, 0), oacc);
            oacc = MFMA32(vb, pack(s, 1), oacc);
            oacc = MFMA32(vc, pack(t, 0), oacc);
            oacc = MFMA32(vd, pack(t, 1), oacc);
        };
        auto wo_piece = [&](int n) { const int pc = n * 4 + rw; if (pc < 54) { const int p = pc / 18, o = pc % 18;
            glds16_dev((unsigned)(o * 1024 + lane * 16), wot + g3_step(p) * G3_SLOT, lds0 + g3_slot(0) + p * G3_SLOT + o * 1024); } };
        auto wo_pieces = [&](int n0, int n1) { if (!summer) for (int n = n0; n < n1; ++n) wo_piece(n); };
        const int lo = rt >= 2 ? 0 : (rt == 1 ? 4 - tg : 8 - tg);
        const int last = lo > 1 ? lo : 1;
        kfetch(8);
        tile(8, 7, true, 1);
        u32x4 hv[24];
        auto part_done = [&]() {
            if (lane == 0 && __hip_atomic_fetch_add((LAS unsigned*)(lds + L_CNT + 4), 1u, __ATOMIC_RELAXED, __HIP_MEMORY_SCOPE_WORKGROUP) == 5u) set_flag(F_P + (hp * 16 + rt) * 32, tag); };
        if (fetcher) {
            WAITV(0);
            part_done();
            flag_confirm(fk, fkp, tag);
#pragma unroll
            for (int n = 0; n < 24; ++n) { const int pc = 2 * n + (lw >> 1), tl = n >= 12, ar = (pc - 24 * tl) / 6, o = pc - 24 * tl - 6 * ar;
                hv[n] = ld_dev(wsr, (unsigned)(WS_KV + (size_t)(((rt - 2 + tl >= 0 ? rt - 2 + tl : 0) * 16 + hp) * 4 + ar) * 6144) + o * 1024 + lane * 16); }
        }
        if (tg >= 2) tile2(7);
        if (tg & 1) tile(tg == 3 ? 5 : 7, 0, false, 0);
        if (pkw && !fetcher) { WAITV(0); part_done(); }
        feature_map(p[0], phq);
        if (fetcher) {
#pragma unroll
            for (int n = 0; n < 24; ++n) { const int pc = 2 * n + (lw >> 1), tl = n >= 12, ar = (pc - 24 * tl) / 6, o = pc - 24 * tl - 6 * ar;
                *(LAS u32x4*)(lds + L_K + ar * 18432 + tl * 6144 + o * 1024 + lane * 16) = hv[n]; }
        }
        __builtin_amdgcn_s_waitcnt(0x0F70);
        BAR();
        gu32* fpp = summer ? F_P + (hp * 16 + r16) * 32 : F_WO + (cg + 8 * (lane & 31)) * 32;
        kfetch(7 - tg);
        if (rt >= 2) {
            __builtin_amdgcn_s_waitcnt(0x0F70);
            if ((tg & 1) == 0) tile(7 - tg, 6 - tg, false, 0);
            if (tg < 2) tile2(6);
            const unsigned fp = flag_request(fpp);
            tile2(4);
            WAITV(0);
            flag_confirm(fp, fpp, tag);
            if (summer) pv_load(); else wo_pieces(0, 7);
            tile2(2); wo_pieces(7, 14);
            tile(0, 0, false, 2);
        } else {
            if (rt == 1) for (int KT = 7 - tg; KT >= last; --KT) tile(KT, KT - 1, false, 0);
            wait_flag(fpp, tag);
            if (summer) pv_load(); else wo_pieces(0, 14);
        }
    }
    const unsigned obase = (unsigned)(WS_O + ((size_t)(rt * 8 + 2 * tg) * 144 + 9 * hp) * 256);
    const lptr stg = lds + L_OST;
    {
        const int tokb = 32 * tg + q32;
        const float gb = *(const LAS float*)(lds + L_GATE + tokb * 4);
        if (summer) {
            const unsigned one = (r16 < 8 && (r16 & 1) == 0) ? 0x00003f80u : (r16 < 8 ? 0x3f800000u : 0u);
            const bf16x8 idn = mk8((r16 >> 1) == 0 ? one : 0u, (r16 >> 1) == 1 ? one : 0u, (r16 >> 1) == 2 ? one : 0u, (r16 >> 1) == 3 ? one : 0u);
#pragma unroll
            for (int n = 0; n < 3; ++n) {
                f32x4 sm = {0, 0, 0, 0};
#pragma unroll
                for (int m = 0; m < 4; ++m) sm = MFMA16(idn, __builtin_bit_cast(bf16x8, pv[n][m]), sm);
                const int gr = (3 * rw + n) * 16 + r16, ph = gr >= 95, e0 = gr - 95 * ph, d = (e0 * 205) >> 10, u = 2 * (e0 - 5 * d) + g;
                if (gr < 190 && g < 2) *(LAS u32x2*)(lds + L_KVT + ph * 2016 + d * 96 + (u >= 5 ? 48 + (u - 5) * 8 : u * 8)) = u32x2{cvtpk(sm[0], sm[1]), cvtpk(sm[2], sm[3])};
            }
        } else {
            const int e0 = rw * 64 + lane;
            if (e0 < 76) *(LAS u32x2*)(lds + L_KVT + (e0 / 38) * 2016 + ((e0 % 38) >> 1) * 96 + (e0 & 1) * 48 + 40) = u32x2{0u, 0u};
            else if (e0 < 88) *(LAS u32x4*)(lds + L_KVT + ((e0 - 76) / 6) * 2016 + 20 * 96 + ((e0 - 76) % 6) * 16) = u32x4{0, 0, 0, 0};
        }
        auto sw = __builtin_amdgcn_permlane32_swap(__float_as_uint(oacc[10]), __float_as_uint(oacc[10]), false, false);
        const float sb = (1.f - gb) * __builtin_amdgcn_rcpf(__uint_as_float(sw[0]));
        const auto s0 = __builtin_amdgcn_permlane32_swap(cvtpk(oacc[0] * sb, oacc[1] * sb), cvtpk(oacc[4] * sb, oacc[5] * sb), false, false);
        const auto s1 = __builtin_amdgcn_permlane32_swap(cvtpk(oacc[2] * sb, oacc[3] * sb), cvtpk(oacc[6] * sb, oacc[7] * sb), false, false);
        st_dev(wsr, obase + (q32 >> 4) * 36864 + (2 * h + hh) * 256 + (q32 & 15) * 16, u32x4{s0[0], s1[0], s0[1], s1[1]});
        if (hh == 0) *(LAS unsigned*)(stg + tokb * 16 + h * 4) = cvtpk(oacc[8] * sb, oacc[9] * sb);
    }
    BAR();
    {
        f32x4 dd[3];
        const int eo = (r16 >> 2) & 1;
        const bool mine = (r16 >> 3) == (g >> 1);
        const bf16x8 b0 = mk8(phq[0], phq[1], phq[2], phq[3]), b1 = mk8(phq[4], phq[5], phq[6], phq[7]), b2 = mk8(phq[8], 0u, 0u, 0u);
        bf16x8 af[3][3];
#pragma unroll
        for (int i = 0; i < 3; ++i) {
            const int sg = 4 * i + (r16 & 3);
            const int d = !mine ? 20 : i < 2 ? 8 * eo + sg : sg == 8 ? 16 + eo : sg == 10 ? 18 : 20;
            const lptr ap = lds + L_KVT + h * 2016 + d * 96 + (g & 1) * 48;
            af[i][0] = ld128(ap); af[i][1] = ld128(ap + 16); af[i][2] = ld128(ap + 32);
        }
        const int tok = 32 * tg + 16 * (g >> 1) + r16;
        const float gate = *(const LAS float*)(lds + L_GATE + tok * 4);
        SB();
#pragma unroll
        for (int i = 0; i < 3; ++i) { f32x4 a = MFMA16(af[i][0], b0, (f32x4{0, 0, 0, 0})); a = MFMA16(af[i][1], b1, a); dd[i] = MFMA16(af[i][2], b2, a); }
        const float sc = gate * __builtin_amdgcn_rcpf(dd[2][2] + 1e-6f);
        st_dev(wsr, obase + (g >> 1) * 36864 + (4 + 2 * h + (g & 1)) * 256 + r16 * 16,
               u32x4{cvtpk(dd[0][0] * sc, dd[0][1] * sc), cvtpk(dd[0][2] * sc, dd[0][3] * sc), cvtpk(dd[1][0] * sc, dd[1][1] * sc), cvtpk(dd[1][2] * sc, dd[1][3] * sc)});
        *(LAS u16*)(stg + tok * 16 + 8 + h * 4 + (g & 1) * 2) = (u16)cvtpk(dd[2][0] * sc, 0.f);
    }
    BAR();
    if (tid < 128) st_dev(wsr, (unsigned)(WS_O + ((size_t)(rt * 8 + (tid >> 4)) * 144 + 9 * hp + 8) * 256) + (tid & 15) * 16, *(const LAS u32x4*)(stg + tid * 16));
    {
        const int rt3 = r64 >> 1, rh = r64 & 1, kq = wid >> 1, rp = wid & 1;
        auto issue3 = [&](int p) {
            if (wid > 0) {
#pragma unroll
                for (int i = 0; i < 3; ++i) { const int pc = (wid - 1) + 7 * i;
                    if (pc < 18) glds16_dev((unsigned)(pc * 1024 + lane * 16), wot + g3_step(p) * G3_SLOT, lds0 + g3_slot(p) + pc * 1024); }
            }
        };
        issue3(3); issue3(4);
        if (wid == 0) WAITV(0); else if (wid < 5) WAITV(6); else WAITV(4);
        BAR();
        if (tid == 0) set_flag(F_O + (rt * 16 + hp) * 32, tag);
        issue3(5); issue3(6); issue3(7);
        const int wro = kq * 4608 + lane * 16, wr4 = kq * 4608 + 4096 + g * 128 + (r16 & 7) * 16;
        u32x4 w8[5];
        { const unsigned wb8 = (unsigned)(WS_WOT + (size_t)cg * 165888) + g3_step(8) * G3_SLOT;
#pragma unroll
          for (int t = 0; t < 5; ++t) w8[t] = ld_dev(wsr, wb8 + (t < 4 ? wro + t * 1024 : wr4)); }
        if (wid == 0) wait_flag(F_O + (rt3 * 16 + r16) * 32, tag);
        DRAIN_BAR();
        u32x4 of[9][2];
        const unsigned ob = (unsigned)(WS_O + ((size_t)(rt3 * 8 + 4 * rh + 2 * rp) * 144 + 36 * kq) * 256 + lane * 16);
        auto o_load = [&](int p) { const unsigned o = ob + g3_step(p) * 1024; of[p][0] = ld_dev(wsr, o); of[p][1] = ld_dev(wsr, o + 36864); };
#pragma unroll
        for (int p = 0; p < ODEPTH; ++p) o_load(p);
        f32x4 a3[5][2];
#pragma unroll
        for (int t = 0; t < 5; ++t) { a3[t][0] = f32x4{0, 0, 0, 0}; a3[t][1] = f32x4{0, 0, 0, 0}; }
        bf16x8 wf[5];
#pragma unroll
        for (int t = 0; t < 4; ++t) wf[t] = ld128(lds + g3_slot(0) + wro + t * 1024);
        wf[4] = ld128(lds + g3_slot(0) + wr4);
#pragma unroll
        for (int p = 0; p < 9; ++p) {
            if (p + ODEPTH < 9) o_load(p + ODEPTH);
            const lptr sp = lds + g3_slot(p + 1 < 8 ? p + 1 : 7);
            const bf16x8 bo0 = __builtin_bit_cast(bf16x8, of[p][0]), bo1 = __builtin_bit_cast(bf16x8, of[p][1]);
            SB();
#pragma unroll
            for (int t = 0; t < 5; ++t) {
                bf16x8 nw = wf[t];
                if (p + 1 < 8) nw = ld128(sp + (t < 4 ? wro + t * 1024 : wr4)); else if (p + 1 == 8) nw = __builtin_bit_cast(bf16x8, w8[t]);
                SB();
                a3[t][0] = MFMA16(wf[t], bo0, a3[t][0]); a3[t][1] = MFMA16(wf[t], bo1, a3[t][1]);
                SB();
                wf[t] = nw;
            }
        }
        BAR();
        const lptr rp0 = lds + rp * 40960 + lane * 16;
        char* orow = (char*)out + (size_t)(rt3 * 128 + 64 * rh + 32 * rp + r16) * 1152 + cg * 144 + g * 8;
        auto finish = [&](auto KQ) {
            constexpr int q = decltype(KQ)::value;
#pragma unroll
            for (int n = 0; n < 10; ++n) if ((n & 3) != q) *(LAS f32x4*)(rp0 + (n * 4 + q) * 1024) = a3[n % 5][n / 5];
            BAR();
            f32x4 pr[3][3];
#pragma unroll
            for (int j = 0; j < 3; ++j) if (q + 4 * j < 10) {
#pragma unroll
                for (int o = 0; o < 3; ++o) pr[j][o] = *(const LAS f32x4*)(rp0 + ((q + 4 * j) * 4 + ((q + 1 + o) & 3)) * 1024); }
#pragma unroll
            for (int j = 0; j < 3; ++j) if (q + 4 * j < 10) { const int n = q + 4 * j, t = n % 5, tt = n / 5;
                const f32x4 u = (a3[t][tt] + pr[j][0]) + (pr[j][1] + pr[j][2]);
                if (t < 4 || g < 2) *(u32x2*)(orow + tt * 16 * 1152 + t * 32) = u32x2{cvtpk(u[0], u[1]), cvtpk(u[2], u[3])};
            }
        };
        if (kq == 0) finish(IC<0>{}); else if (kq == 1) finish(IC<1>{}); else if (kq == 2) finish(IC<2>{}); else finish(IC<3>{});
        asm volatile("fused_code_end:");
        if (b == 0 && tid == 0) set_flag((gu32*)(ws + WS_EPOCH), tag);
    }
}

extern "C" void kernel_launch(void* const* d_in, const int* in_sizes, int n_in, void* d_out, int out_size, void* d_ws, size_t ws_size, hipStream_t stream) {
    (void)hipFuncSetAttribute((const void*)fused_kernel, hipFuncAttributeMaxDynamicSharedMemorySize, LDS_BYTES);
    fused_kernel<<<256, 512, LDS_BYTES, stream>>>((const char*)d_in[0], (const char*)d_in[1], (const char*)d_in[5], (const char*)d_in[2], (const char*)d_in[3],
                                                  (const char*)d_in[6], (const char*)d_in[7], (char*)d_ws, (u16*)d_out);
}
```
